```python
import torch

def _softmax(x, axis=-1):
    return torch.softmax(x, dim=axis)
def _gelu(x):
    return torch.nn.functional.gelu(x, approximate="tanh")
def _silu(x):
    return torch.nn.functional.silu(x)
def _sigmoid(x):
    return torch.sigmoid(x)
def _layernorm(x, axis=-1, eps=1e-5):
    m = x.mean(dim=axis, keepdim=True)
    v = x.var(dim=axis, keepdim=True, unbiased=False)
    return (x - m) / torch.sqrt(v + eps)
def _scatter_add(tgt, idx, src, axis=0):
    return tgt.clone().index_add_(axis, idx, src)
def _segment_sum(src, seg, n):
    out = torch.zeros((n,) + src.shape[1:], dtype=src.dtype)
    return out.index_add_(0, seg, src)
def _pad(x, pw):
    flat = tuple(v for pair in reversed(pw) for v in pair)
    return torch.nn.functional.pad(x, flat)

kernel_name = 'recurrent_scan__ssm_conv_s4__mixer_channel_mlp__prompt'

def setup_inputs():

    _w = (lambda a: (lambda t: t.to(torch.bfloat16) if t.is_floating_point() else t)(torch.as_tensor(a)))
    def _A_setup():
        B, L, D, N = 1, 2048, 512, 128
        A_re = -(lambda _x: torch.exp(torch.as_tensor(_x)))((lambda *s: torch.randn(*s, dtype=torch.bfloat16) * 3.2)(D, N))
        A_im = torch.pi * (lambda *s: torch.randn(*s, dtype=torch.bfloat16) * 0.05)(D, N)
        B_re = (lambda *s: torch.randn(*s, dtype=torch.bfloat16) * 0.1)(D, N)
        B_im = (lambda *s: torch.randn(*s, dtype=torch.bfloat16) * 0.1)(D, N)
        C_re = (lambda *s: torch.randn(*s, dtype=torch.bfloat16) * 0.1)(D, N)
        C_im = (lambda *s: torch.randn(*s, dtype=torch.bfloat16) * 0.2)(D, N)
        log_dt = (lambda *s: torch.randn(*s, dtype=torch.bfloat16) * 0.05)(D) * 0.5 - 4.0
        Dskip = (lambda *s: torch.randn(*s, dtype=torch.bfloat16) * 0.05)(D)
        return {
            "u": _w((lambda *s: torch.randn(*s, dtype=torch.bfloat16) * 0.05)(B, L, D)),
            "A_re": _w(A_re),
            "A_im": _w(A_im),
            "B_re": _w(B_re),
            "B_im": _w(B_im),
            "C_re": _w(C_re),
            "C_im": _w(C_im),
            "log_dt": _w(log_dt),
            "Dskip": _w(Dskip),
        }
    _A_in = _A_setup()

    def _B_setup():
        B, S, D, F = 1, 2048, 512, 1792
        return {
            "x": _w((lambda *s: torch.randn(*s, dtype=torch.bfloat16) * 0.05)(B, S, D)),
            "w1": _w((lambda *s: torch.randn(*s, dtype=torch.bfloat16) * 1.6)(D, F) * (D ** -0.5)),
            "b1": _w((lambda *s: torch.randn(*s, dtype=torch.bfloat16) * 0.8)(F)),
            "w2": _w((lambda *s: torch.randn(*s, dtype=torch.bfloat16) * 0.05)(F, D) * (F ** -0.5)),
            "b2": _w((lambda *s: torch.randn(*s, dtype=torch.bfloat16) * 0.05)(D)),
        }
    _B_in = _B_setup()

    return {
        "A_u": _A_in["u"],
        "A_A_re": _A_in["A_re"],
        "A_A_im": _A_in["A_im"],
        "A_B_re": _A_in["B_re"],
        "A_B_im": _A_in["B_im"],
        "A_C_re": _A_in["C_re"],
        "A_C_im": _A_in["C_im"],
        "A_log_dt": _A_in["log_dt"],
        "A_Dskip": _A_in["Dskip"],
        "B_x": _B_in["x"],
        "B_w1": _B_in["w1"],
        "B_b1": _B_in["b1"],
        "B_w2": _B_in["w2"],
        "B_b2": _B_in["b2"],
    }

def reference(A_u, A_A_re, A_A_im, A_B_re, A_B_im, A_C_re, A_C_im, A_log_dt, A_Dskip, B_x, B_w1, B_b1, B_w2, B_b2):
    def _A_ref(u, A_re, A_im, B_re, B_im, C_re, C_im, log_dt, Dskip):
        Bsz, L, D = u.shape
        dt = torch.exp(log_dt)[:, None]
        A = A_re + 1j * A_im
        Bc = B_re + 1j * B_im
        Cc = C_re + 1j * C_im
        logdA = dt * A
        dB = dt * Bc
        l = (lambda x, dt: x.to(dt))(torch.arange(L), A_re.dtype)
        P = torch.exp(logdA[:, :, None] * l[None, None, :])
        K = torch.einsum("dn,dnl,dn->dl", Cc, P, dB).real
        L2 = 2 * L
        Kp = _pad(K, ((0, 0), (0, L)))
        up = (lambda x, *d: x.permute(*d))(u, 0, 2, 1)
        up = _pad(up, ((0, 0), (0, 0), (0, L)))
        Kf = (lambda x, axes=(-2,-1): torch.fft.rfft2(x.float(), dim=axes))(Kp[:, None, :], axes=(-2, -1))
        Uf = (lambda x, axes=(-2,-1): torch.fft.rfft2(x.float(), dim=axes))(up[:, :, None, :], axes=(-2, -1))
        Yf = Uf * Kf[None, :, :, :]
        y = (lambda x, s, axes=(-2,-1): torch.fft.irfft2(x, s=s, dim=axes).to(torch.bfloat16))(Yf, (1, L2), axes=(-2, -1))[:, :, 0, :L]
        y = (lambda x, *d: x.permute(*d))(y, 0, 2, 1) + u * Dskip[None, None, :]
        return y

    def _B_ref(x, w1, b1, w2, b2):
        y = _layernorm(x, axis=-1)
        h = _gelu(torch.einsum("bsd,df->bsf", y, w1) + b1)
        return torch.einsum("bsf,fd->bsd", h, w2) + b2

    x = A_u
    xn = _layernorm(x, axis=-1)
    s = _A_ref(xn, A_A_re, A_A_im, A_B_re, A_B_im, A_C_re, A_C_im, A_log_dt, A_Dskip)
    g = _silu(_B_ref(xn, B_w1, B_b1, B_w2, B_b2))
    return x + s * g
```

```cpp
#include <hip/hip_runtime.h>
#include <hip/hip_bf16.h>

typedef unsigned int u32;
typedef unsigned short u16;
typedef u32 v4u __attribute__((ext_vector_type(4)));
typedef u32 v2u __attribute__((ext_vector_type(2)));
typedef float f32x2 __attribute__((ext_vector_type(2)));
typedef float f32x4 __attribute__((ext_vector_type(4)));
typedef float f32x16 __attribute__((ext_vector_type(16)));
typedef short s16x4 __attribute__((ext_vector_type(4)));
typedef short s16x8 __attribute__((ext_vector_type(8)));
typedef __bf16 bf16x2 __attribute__((ext_vector_type(2)));
#define GAS __attribute__((address_space(1)))
#define LAS __attribute__((address_space(3)))
#define DEV __device__ __forceinline__
#define RLX_AGENT __ATOMIC_RELAXED, __HIP_MEMORY_SCOPE_AGENT
#define SC1 16

constexpr int WS_E = 0;
constexpr int WS_FW2 = 256;
constexpr int WS_FXN = 512;
constexpr int WS_FH = 1024;
constexpr int WS_FS = 2048;
constexpr int WS_XNT = 65536;
constexpr int WS_W2T = WS_XNT + 2097152;
constexpr int WS_HT = WS_W2T + 1835008;
constexpr int WS_ST = WS_HT + 7340032;
constexpr int WS_END = WS_ST + 2097152;
constexpr int LD_KREG = 131072;
constexpr int LD_STAT = LD_KREG + 4352;
constexpr int LD_CS = LD_KREG + 8192 + 4352;
constexpr int LD_B1 = LD_CS + 512;
constexpr int LD_HST = 4352;
constexpr int LD_U = 0;
constexpr int LD_CX = 16384;
constexpr int LD_ST = 32768;
constexpr int LD_RED = 65536;
constexpr int LDS_BYTES = 163840;
constexpr u32 SPIN_CAP = 1u << 16;
constexpr u32 CODE_BYTES = 32768;

DEV float bf2f(u32 h) { return __builtin_bit_cast(float, h << 16); }
DEV float bflo(u32 d) { return __builtin_bit_cast(float, d << 16); }
DEV float bfhi(u32 d) { return __builtin_bit_cast(float, d & 0xffff0000u); }
DEV u32 pk2(float lo, float hi) {
  f32x2 v = {lo, hi};
  return __builtin_bit_cast(u32, __builtin_convertvector(v, bf16x2));
}
DEV float bfr(float v) { return bflo(pk2(v, v)); }
DEV f32x4 mfma16(v4u a, v4u b, f32x4 c) {
  return __builtin_amdgcn_mfma_f32_16x16x32_bf16(__builtin_bit_cast(s16x8, a), __builtin_bit_cast(s16x8, b), c, 0, 0, 0);
}
DEV f32x16 mfma32(v4u a, v4u b, f32x16 c) {
  return __builtin_amdgcn_mfma_f32_32x32x16_bf16(__builtin_bit_cast(s16x8, a), __builtin_bit_cast(s16x8, b), c, 0, 0, 0);
}
DEV v2u trread(LAS char* p) { return __builtin_bit_cast(v2u, __builtin_amdgcn_ds_read_tr16_b64_v4i16((LAS s16x4*)p)); }
DEV v4u mk_rsrc(const void* p, u32 bytes) {
  const unsigned long long a = (unsigned long long)p;
  v4u r = {(u32)a, (u32)(a >> 32), bytes, 0x00020000u};
  return r;
}
DEV void dma16(u32 voff, v4u rs, u32 soff, u32 lds) {
  asm volatile("s_mov_b32 m0, %0\n\ts_nop 0\n\tbuffer_load_dwordx4 %1, %2, %3 offen lds" ::"s"(lds), "v"(voff), "s"(rs), "s"(soff) : "memory");
}
#define WAITV(n) asm volatile("s_waitcnt vmcnt(" #n ")" ::: "memory")
#define WAITL() asm volatile("s_waitcnt lgkmcnt(0)" ::: "memory")
#define BAR() do { asm volatile("s_waitcnt lgkmcnt(0)" ::: "memory"); __builtin_amdgcn_s_barrier(); asm volatile("" ::: "memory"); } while (0)
#define SCHED() __builtin_amdgcn_sched_barrier(0)
#define ORDER_BEHIND_POLL() __builtin_amdgcn_fence(__ATOMIC_ACQUIRE, "wavefront")

DEV f32x2 cmul(f32x2 a, f32x2 z) {
  const f32x2 zr = {-z.y, z.x};
  const f32x2 ax = {a.x, a.x}, ay = {a.y, a.y};
  return ax * z + ay * zr;
}
DEV f32x2 zpow(float la, float th, float k) {
  const float mag = __builtin_amdgcn_exp2f(la * k);
  const float fr = __builtin_amdgcn_fractf(th * k);
  f32x2 r = {mag * __builtin_amdgcn_cosf(fr), mag * __builtin_amdgcn_sinf(fr)};
  return r;
}
DEV void poll_flags(GAS u32* p, bool active, u32 epoch, u32 first) {
  u32 v = first;
#pragma unroll 1
  for (u32 spins = 0; spins < SPIN_CAP; ++spins) {
    if (__all(!active || v == epoch)) break;
    __builtin_amdgcn_s_sleep(1);
    v = __hip_atomic_load(p, RLX_AGENT);
  }
  ORDER_BEHIND_POLL();
}
DEV void ln_stats(float s1, float s2, float& mean, float& var, float& den1) {
  mean = s1 * (1.0f / 512.0f);
  var = fmaxf(s2 * (1.0f / 512.0f) - mean * mean, 0.0f);
  den1 = bfr(__builtin_sqrtf(bfr(bfr(var) + 1e-5f)));
}

__global__ void __launch_bounds__(256, 1)
s4_mixer_kernel(const u16* __restrict__ x, const u16* __restrict__ A_re, const u16* __restrict__ A_im,
                const u16* __restrict__ B_re, const u16* __restrict__ B_im, const u16* __restrict__ C_re,
                const u16* __restrict__ C_im, const u16* __restrict__ log_dt, const u16* __restrict__ Dskip,
                const u16* __restrict__ w1, const u16* __restrict__ b1, const u16* __restrict__ w2,
                const u16* __restrict__ b2, u16* __restrict__ out, char* __restrict__ ws) {
  extern __shared__ __attribute__((aligned(16))) char smem[];
  LAS char* const L = (LAS char*)smem;
  const int tid = threadIdx.x, lane = tid & 63;
  const int w = __builtin_amdgcn_readfirstlane(tid >> 6);
  const int b = blockIdx.x, xcd = b & 7, idx = b >> 3;
  const int r16 = lane & 15, q = lane >> 4;
  const bool helper = idx >= 28;

  u32 pf0, pf1;
  {
    unsigned long long pc;
    asm volatile("s_getpc_b64 %0" : "=s"(pc));
    u32 off = (u32)(lane * 32 + idx) * 64u + (u32)w * 131072u;
    off = off < CODE_BYTES ? off : 0u;
    asm volatile("global_load_dword %0, %1, %2" : "=v"(pf0) : "v"(off), "s"(pc));
  }
  const int ch = 64 * xcd + 2 * idx + (w >> 1);
  u32 pAr, pAi, pBr, pBi, pCr, pCi, pLd, pDs, pE, pB1;
  {
    const u32 po = (u32)(ch * 128 + (w & 1) * 64 + lane) * 2u;
    const u32 co = (u32)ch * 2u + (u32)(lane & 0);
    u32 cov = co; asm volatile("" : "+v"(cov));
    u32 zv = 0; asm volatile("" : "+v"(zv));
    asm volatile("global_load_ushort %0, %1, %2" : "=v"(pLd) : "v"(cov), "s"(log_dt));
    asm volatile("global_load_ushort %0, %1, %2" : "=v"(pAr) : "v"(po), "s"(A_re));
    asm volatile("global_load_ushort %0, %1, %2" : "=v"(pAi) : "v"(po), "s"(A_im));
    asm volatile("global_load_ushort %0, %1, %2" : "=v"(pBr) : "v"(po), "s"(B_re));
    asm volatile("global_load_ushort %0, %1, %2" : "=v"(pBi) : "v"(po), "s"(B_im));
    asm volatile("global_load_ushort %0, %1, %2" : "=v"(pCr) : "v"(po), "s"(C_re));
    asm volatile("global_load_ushort %0, %1, %2" : "=v"(pCi) : "v"(po), "s"(C_im));
    asm volatile("global_load_ushort %0, %1, %2" : "=v"(pDs) : "v"(cov), "s"(Dskip));
    asm volatile("global_load_dword %0, %1, %2 sc1" : "=v"(pE) : "v"(zv), "s"(ws));
  }
  const int mt = (xcd >> 1) * 4 + idx / 7, nt = (xcd & 1) * 7 + idx % 7;
  const int hid = xcd * 4 + (idx - 28);
  {
    const u32 bo = (u32)((helper ? 0 : nt * 128) + (tid & 127)) * 2u;
    asm volatile("global_load_ushort %0, %1, %2" : "=v"(pB1) : "v"(bo), "s"(b1));
    const u32 so = (w < 2) ? (u32)(b * 8192 + (tid & 127) * 64) : (u32)(b * 7168 + min(tid & 127, 111) * 64);
    const u16* sp = (w < 2) ? x : w1;
    asm volatile("global_load_dword %0, %1, %2" : "=v"(pf1) : "v"(so), "s"(sp));
  }
  const v4u rsX = mk_rsrc(x, 2097152u), rsW1 = mk_rsrc(w1, 1835008u), rsW2 = mk_rsrc(w2, 1835008u);
  const u32 voX = (u32)((32 * w + (lane >> 3)) * 1024 + (((lane & 7) ^ (lane >> 3)) << 4));
  const u32 voW = (u32)((4 * w + q) * 3584 + ((r16 ^ ((q << 2) | w)) << 4));
  const int rot = idx & 7;
#define G1_STAGE(st)                                                                                         \
  {                                                                                                          \
    const int sa_ = ((st) + rot) & 7;                                                                        \
    _Pragma("unroll") for (int p = 0; p < 4; ++p) {                                                          \
      dma16(voX, rsX, (u32)(mt * 131072 + p * 8192 + sa_ * 128), (u32)(((st) & 3) * 32768 + w * 4096 + p * 1024)); \
      dma16(voW, rsW1, (u32)((sa_ * 64 + 16 * p) * 3584 + nt * 256), (u32)(((st) & 3) * 32768 + 16384 + p * 4096 + w * 1024)); \
    }                                                                                                        \
  }
  if (!helper) {
    G1_STAGE(0) G1_STAGE(1) G1_STAGE(2) G1_STAGE(3)
  } else {
    const u32 vo2 = (u32)((lane >> 3) * 1024 + (lane & 7) * 16 + w * 8192);
#pragma unroll
    for (int p = 0; p < 14; ++p)
      dma16(vo2, rsW2, (u32)(((hid & 3) * 448 + p * 32) * 1024 + (hid >> 2) * 128), (u32)(65536 + (p * 4 + w) * 1024));
#pragma unroll
    for (int p = 0; p < 16; ++p) {
      const u32 vo = (u32)(w * 16384 + ((lane ^ (((p & 3) << 2) | ((p >> 2) & 3))) << 4));
      dma16(vo, rsX, (u32)((hid * 64 + p) * 1024), (u32)((w * 16 + p) * 1024));
    }
#pragma unroll
    for (int p = 0; p < 2; ++p) {
      const u32 vo = (u32)(w * 16384 + ((lane ^ 15) << 4));
      dma16(vo, rsX, (u32)((hid * 64 + 15) * 1024), (u32)((w * 16 + 15) * 1024));
    }
  }

  asm volatile("s_waitcnt vmcnt(32)"
               : "+v"(pf0), "+v"(pf1), "+v"(pAr), "+v"(pAi), "+v"(pBr), "+v"(pBi), "+v"(pCr), "+v"(pCi), "+v"(pLd), "+v"(pDs), "+v"(pE), "+v"(pB1)
               :: "memory");
  const u32 epoch = __builtin_amdgcn_readfirstlane(pE) + 1u;
  const float dskip = bf2f(pDs);
  LAS char* const KR = L + LD_KREG + w * 8192;
  {
    const float dt = bfr(__builtin_amdgcn_exp2f(bf2f(pLd) * 1.4426950408889634f));
    const float la = dt * bf2f(pAr) * 1.4426950408889634f;
    const float th = dt * bf2f(pAi) * 0.15915494309189535f;
    const f32x2 dB = {dt * bf2f(pBr), dt * bf2f(pBi)};
    const f32x2 Cc = {bf2f(pCr), bf2f(pCi)};
    const f32x2 coef = cmul(Cc, dB);
    const f32x2 z1 = zpow(la, th, 1.f), z8 = zpow(la, th, 8.f), z16 = zpow(la, th, 16.f), z256 = zpow(la, th, 256.f);
    const f32x2 z2 = cmul(z1, z1), z4 = cmul(z2, z2);
    const u32 wo = (u32)lane * 4u;
#define TW(row, val) *(LAS u32*)(KR + (row) * 256 + (wo ^ (u32)(((row) & 15) << 4))) = (val)
    {
      f32x2 qa = {1.f, 0.f}, qb = z8;
#pragma unroll
      for (int i = 0; i < 16; ++i) {
        TW(2 * i, pk2(qa.x, qa.y));
        TW(2 * i + 1, pk2(qb.x, qb.y));
        if (i < 15) { qa = cmul(qa, z16); qb = cmul(qb, z16); }
      }
    }
    const int r32 = lane & 31, hh = lane >> 5;
    v4u qf[8];
#pragma unroll
    for (int s = 0; s < 8; ++s) qf[s] = *(LAS v4u*)(KR + r32 * 256 + (((2 * s + hh) ^ (r32 & 15)) << 4));
    WAITL();
#define PROW(h, g) (((g) & 3) + 8 * ((g) >> 2) + 4 * (h))
#define PW(h, g, v) TW(PROW(h, g), pk2((v).x, -(v).y))
    {
      f32x2 Wc = coef, t = coef;
      PW(0, 0, t);
#pragma unroll
      for (int a = 1; a <= 8; ++a) { t = cmul(t, z1); PW(0, a, t); }
      Wc = cmul(Wc, z256); t = Wc; PW(1, 0, t);
#pragma unroll
      for (int a = 1; a <= 4; ++a) { t = cmul(t, z2); PW(1, a, t); }
      Wc = cmul(Wc, z256); PW(1, 5, Wc); t = cmul(Wc, z4); PW(1, 6, t); t = cmul(Wc, z8); PW(1, 7, t);
      Wc = cmul(Wc, z256); PW(1, 8, Wc); t = cmul(Wc, z4); PW(1, 9, t); t = cmul(Wc, z8); PW(1, 10, t);
      Wc = cmul(Wc, z256); PW(0, 9, Wc); t = cmul(Wc, z8); PW(0, 10, t);
      Wc = cmul(Wc, z256); PW(0, 11, Wc); t = cmul(Wc, z8); PW(0, 12, t);
      Wc = cmul(Wc, z256); PW(0, 13, Wc); t = cmul(Wc, z8); PW(0, 14, t);
      Wc = cmul(Wc, z256); PW(1, 11, Wc); t = cmul(Wc, z8); PW(1, 12, t);
    }
    f32x16 kacc;
#pragma unroll
    for (int i = 0; i < 16; ++i) kacc[i] = 0.f;
#pragma unroll
    for (int s = 0; s < 8; ++s) {
      const v4u pf = *(LAS v4u*)(KR + r32 * 256 + (((2 * s + hh) ^ (r32 & 15)) << 4));
      kacc = mfma32(pf, qf[s], kacc);
    }
    WAITL();
#pragma unroll
    for (int i = 0; i < 4; ++i) {
      f32x4 v = {kacc[4 * i], kacc[4 * i + 1], kacc[4 * i + 2], kacc[4 * i + 3]};
      *(LAS f32x4*)(KR + i * 1024 + lane * 16) = v;
    }
    BAR();
    LAS char* const KP = L + LD_KREG + (w ^ 1) * 8192;
#pragma unroll
    for (int i = 0; i < 4; ++i) {
      const f32x4 v = *(LAS f32x4*)(KP + i * 1024 + lane * 16);
      kacc[4 * i] += v.x; kacc[4 * i + 1] += v.y; kacc[4 * i + 2] += v.z; kacc[4 * i + 3] += v.w;
    }
    BAR();
    const bool odd = r32 & 1;
    float e[4][9];
    int cseg[4];
    if (hh == 0) {
#pragma unroll
      for (int i = 0; i < 9; ++i) e[0][i] = kacc[i];
#pragma unroll
      for (int u = 1; u < 4; ++u) {
        const float a = kacc[7 + 2 * u], c2 = kacc[8 + 2 * u];
        e[u][0] = a; e[u][1] = a; e[u][2] = a; e[u][3] = a; e[u][4] = odd ? c2 : a;
        e[u][5] = c2; e[u][6] = c2; e[u][7] = c2; e[u][8] = c2;
      }
      cseg[0] = 0; cseg[1] = 4; cseg[2] = 5; cseg[3] = 6;
    } else {
      e[0][0] = kacc[0]; e[0][1] = kacc[0]; e[0][2] = kacc[1]; e[0][3] = kacc[2]; e[0][4] = kacc[2];
      e[0][5] = kacc[2]; e[0][6] = kacc[3]; e[0][7] = kacc[4]; e[0][8] = kacc[4];
#pragma unroll
      for (int u = 1; u < 3; ++u) {
        const float a = kacc[2 + 3 * u], m = kacc[3 + 3 * u], c2 = kacc[4 + 3 * u];
        e[u][0] = a; e[u][1] = a; e[u][2] = a; e[u][3] = m; e[u][4] = m; e[u][5] = m;
        e[u][6] = c2; e[u][7] = c2; e[u][8] = c2;
      }
      const float a = kacc[11], c2 = kacc[12];
      e[3][0] = a; e[3][1] = a; e[3][2] = a; e[3][3] = a; e[3][4] = odd ? c2 : a;
      e[3][5] = c2; e[3][6] = c2; e[3][7] = c2; e[3][8] = c2;
      cseg[0] = 1; cseg[1] = 2; cseg[2] = 3; cseg[3] = 7;
    }
    LAS char* const KC = KR + (w & 1) * 64;
#pragma unroll
    for (int u = 0; u < 4; ++u) {
      v4u d;
      if (w & 1) { d.x = pk2(e[u][8], e[u][7]); d.y = pk2(e[u][6], e[u][5]); d.z = pk2(e[u][4], e[u][3]); d.w = pk2(e[u][2], e[u][1]); }
      else       { d.x = pk2(e[u][7], e[u][6]); d.y = pk2(e[u][5], e[u][4]); d.z = pk2(e[u][3], e[u][2]); d.w = pk2(e[u][1], e[u][0]); }
      *(LAS v4u*)(KC + (255 - 32 * cseg[u] - r32) * 16) = d;
    }
    if (lane == 0) {
      v4u t0 = {(w & 1) ? pk2(kacc[0], 0.f) : 0u, 0u, 0u, 0u}, t1 = {0u, 0u, 0u, 0u};
      *(LAS v4u*)(KC + 4096) = t0;
      *(LAS v4u*)(KC + 4112) = t1;
    }
  }

  const __amdgpu_buffer_rsrc_t rsWS = __builtin_amdgcn_make_buffer_rsrc((void*)ws, 0, WS_END, 0x00020000);
  GAS u32* const ctl = (GAS u32*)ws;
  const v4u ones = {0x3f803f80u, 0x3f803f80u, 0x3f803f80u, 0x3f803f80u};
  const int tg = xcd * 4 + (idx >> 3), cg = idx & 7;
  v4u aw[14][4];
  u32 fxn;

  if (!helper) {
    const int mi = w >> 1, ni = w & 1;
    if (tid < 128) *(LAS float*)(L + LD_B1 + tid * 4) = bf2f(pB1);
    f32x4 acc[4][4], gram[2], sumx[2], csum[2];
#pragma unroll
    for (int i = 0; i < 4; ++i)
#pragma unroll
      for (int j = 0; j < 4; ++j) acc[i][j] = f32x4{0.f, 0.f, 0.f, 0.f};
#pragma unroll
    for (int i = 0; i < 2; ++i) { gram[i] = f32x4{0.f, 0.f, 0.f, 0.f}; sumx[i] = gram[i]; csum[i] = gram[i]; }
    u32 xa[4], wa[4][2];
#pragma unroll
    for (int i = 0; i < 4; ++i) xa[i] = (u32)((64 * mi + 16 * ((i + 2 * ni) & 3) + r16) * 128 + ((q ^ (r16 & 7)) << 4));
    {
      const int qq = r16 >> 2, p = lane & 3;
#pragma unroll
      for (int j = 0; j < 4; ++j)
#pragma unroll
        for (int t = 0; t < 2; ++t) {
          const int c = 4 * ni + ((j + 2 * mi) & 3);
          const int row = 8 * q + 4 * t + qq;
          wa[j][t] = (u32)(16384 + row * 256 + (((2 * c + (p >> 1)) ^ ((qq << 2) | ((2 * q + t) & 3))) << 4) + 8 * (p & 1));
        }
    }
#define G1_COMPUTE(st)                                                                       \
  _Pragma("unroll") for (int ks = 0; ks < 2; ++ks) {                                         \
    v4u xf[4], wf[4];                                                                        \
    _Pragma("unroll") for (int i = 0; i < 4; ++i) {                                          \
      xf[i] = *(LAS v4u*)(L + ((st) & 3) * 32768 + (xa[i] ^ (u32)(ks * 64)));                \
      const v2u lo = trread(L + ((st) & 3) * 32768 + ks * 8192 + wa[i][0]);                  \
      const v2u hi = trread(L + ((st) & 3) * 32768 + ks * 8192 + wa[i][1]);                  \
      wf[i] = v4u{lo.x, lo.y, hi.x, hi.y};                                                   \
    }                                                                                        \
    _Pragma("unroll") for (int j = 0; j < 4; ++j)                                            \
      _Pragma("unroll") for (int i = 0; i < 4; ++i) acc[j][i] = mfma16(wf[j], xf[i], acc[j][i]); \
    _Pragma("unroll") for (int u = 0; u < 2; ++u) {                                          \
      gram[u] = mfma16(xf[u], xf[u], gram[u]);                                               \
      sumx[u] = mfma16(ones, xf[u], sumx[u]);                                                \
      csum[u] = mfma16(wf[u], ones, csum[u]);                                                \
    }                                                                                        \
  }
    WAITV(24); BAR(); G1_COMPUTE(0)
    WAITV(16); BAR(); G1_STAGE(4) G1_COMPUTE(1)
    WAITV(16); BAR(); G1_STAGE(5) G1_COMPUTE(2)
    WAITV(16); BAR(); G1_STAGE(6) G1_COMPUTE(3)
    WAITV(16); BAR(); G1_STAGE(7) G1_COMPUTE(4)
    WAITV(16); BAR(); G1_COMPUTE(5)
    WAITV(8); BAR(); G1_COMPUTE(6)
    WAITV(0); BAR();
    const u32 fw2 = __hip_atomic_load(ctl + WS_FW2 / 4 + 4 * cg + w, RLX_AGENT);
    fxn = __hip_atomic_load(ctl + WS_FXN / 4 + (lane & 31), RLX_AGENT);
    SCHED();
    G1_COMPUTE(7)
    SCHED();
    poll_flags(ctl + WS_FW2 / 4 + 4 * cg + w, true, epoch, fw2);
#pragma unroll
    for (int s = 0; s < 14; ++s)
#pragma unroll
      for (int c = 0; c < 4; ++c)
        aw[s][c] = __builtin_amdgcn_raw_buffer_load_b128(rsWS, WS_W2T + ((cg * 56 + 14 * w + s) * 4 + c) * 1024 + lane * 16, 0, SC1);
    SCHED();
#pragma unroll
    for (int u = 0; u < 2; ++u) {
      const int k = r16 & 3;
      const float s2 = k == 0 ? gram[u].x : k == 1 ? gram[u].y : k == 2 ? gram[u].z : gram[u].w;
      float mean, var, den1;
      ln_stats(sumx[u].x, s2, mean, var, den1);
      const float v2 = bfr(var / (den1 * den1));
      const float den2 = bfr(__builtin_sqrtf(bfr(v2 + 1e-5f)));
      const float R = 1.0f / (den1 * den2);
      if (q == (r16 >> 2)) *(LAS f32x2*)(L + LD_STAT + (64 * mi + 16 * (u + 2 * ni) + r16) * 8) = f32x2{R, R * mean};
      if (r16 == 0) *(LAS f32x4*)(L + LD_CS + (64 * ni + 16 * (u + 2 * mi) + 4 * q) * 4) = csum[u];
    }
    BAR();
    f32x2 st[4];
#pragma unroll
    for (int i = 0; i < 4; ++i) st[i] = *(LAS f32x2*)(L + LD_STAT + (64 * mi + 16 * ((i + 2 * ni) & 3) + r16) * 8);
#pragma unroll
    for (int jp = 0; jp < 2; ++jp) {
      f32x4 cs[2], bb[2];
#pragma unroll
      for (int e = 0; e < 2; ++e) {
        const int fo = (64 * ni + 16 * ((2 * jp + e + 2 * mi) & 3) + 4 * q) * 4;
        cs[e] = *(LAS f32x4*)(L + LD_CS + fo);
        bb[e] = *(LAS f32x4*)(L + LD_B1 + fo);
      }
#pragma unroll
      for (int i = 0; i < 4; ++i) {
        u32 o[4];
#pragma unroll
        for (int e = 0; e < 2; ++e) {
          const f32x4 pre = acc[2 * jp + e][i] * st[i].x + (bb[e] - cs[e] * st[i].y);
          const f32x4 arg = pre * (pre * pre * -0.1029432397f + -2.302208198f);
          f32x4 g;
#pragma unroll
          for (int k = 0; k < 4; ++k) g[k] = __builtin_amdgcn_rcpf(1.0f + __builtin_amdgcn_exp2f(arg[k]));
          g = g * pre;
          o[2 * e] = pk2(g.x, g.y);
          o[2 * e + 1] = pk2(g.z, g.w);
        }
        const int ksi = nt * 4 + 2 * ni + ((jp + mi) & 1);
        const int off = WS_HT + (((2 * mt + mi) * 56 + ksi) * 4 + ((i + 2 * ni) & 3)) * 1024 + lane * 16;
        __builtin_amdgcn_raw_buffer_store_b128(v4u{o[0], o[1], o[2], o[3]}, rsWS, off, 0, SC1);
      }
    }
    WAITV(0);
    BAR();
    if (tid == 0) __hip_atomic_store(ctl + WS_FH / 4 + mt * 16 + nt, epoch, RLX_AGENT);
  } else {
    WAITV(18); BAR();
    {
      const int qq = r16 >> 2, p = lane & 3;
      LAS char* const sb = L + 65536 + (4 * q + qq) * 128 + 64 * (w >> 1) + 16 * p + 8 * (w & 1);
#pragma unroll
      for (int s = 0; s < 14; ++s) {
        const v2u lo = trread(sb + s * 4096), hi = trread(sb + s * 4096 + 2048);
        __builtin_amdgcn_raw_buffer_store_b128(v4u{lo.x, lo.y, hi.x, hi.y}, rsWS,
                                               WS_W2T + (((hid >> 2) * 56 + (hid & 3) * 14 + s) * 4 + w) * 1024 + lane * 16, 0, SC1);
      }
    }
    WAITV(0);
    BAR();
    if (tid == 0) __hip_atomic_store(ctl + WS_FW2 / 4 + hid, epoch, RLX_AGENT);
    {
      f32x4 gram = {0.f, 0.f, 0.f, 0.f}, sumx = gram;
      const int fr = ((r16 & 3) << 2) | ((r16 >> 2) & 3);
#pragma unroll
      for (int s = 0; s < 16; ++s) {
        const v4u xf = *(LAS v4u*)(L + (16 * w + r16) * 1024 + (((4 * s + q) ^ fr) << 4));
        gram = mfma16(xf, xf, gram);
        sumx = mfma16(ones, xf, sumx);
      }
      const int k = r16 & 3;
      const float s2 = k == 0 ? gram.x : k == 1 ? gram.y : k == 2 ? gram.z : gram.w;
      float mean, var, den1;
      ln_stats(sumx.x, s2, mean, var, den1);
      const float rd = 1.0f / den1;
      if (q == (r16 >> 2)) *(LAS f32x2*)(KR + LD_HST + r16 * 8) = f32x2{rd, -bfr(mean) * rd};
      WAITL();
      const int qq = r16 >> 2, p = lane & 3;
#pragma unroll
      for (int G = 0; G < 2; ++G) {
        f32x4 sv[4];
#pragma unroll
        for (int i = 0; i < 4; ++i) sv[i] = *(LAS f32x4*)(KR + LD_HST + G * 64 + i * 16);
#pragma unroll
        for (int cb = 0; cb < 8; ++cb) {
          v2u d[2];
#pragma unroll
          for (int t = 0; t < 2; ++t) {
            const int row = 8 * G + 4 * t + qq;
            const int f = (qq << 2) | ((2 * G + t) & 3);
            d[t] = trread(L + (16 * w + row) * 1024 + (((8 * cb + 2 * q + (p >> 1)) ^ f) << 4) + 8 * (p & 1));
          }
          v4u o;
          o.x = pk2(bflo(d[0].x) * sv[0].x + sv[0].y, bfhi(d[0].x) * sv[0].z + sv[0].w);
          o.y = pk2(bflo(d[0].y) * sv[1].x + sv[1].y, bfhi(d[0].y) * sv[1].z + sv[1].w);
          o.z = pk2(bflo(d[1].x) * sv[2].x + sv[2].y, bfhi(d[1].x) * sv[2].z + sv[2].w);
          o.w = pk2(bflo(d[1].y) * sv[3].x + sv[3].y, bfhi(d[1].y) * sv[3].z + sv[3].w);
          __builtin_amdgcn_raw_buffer_store_b128(o, rsWS, WS_XNT + (hid * 8 + 2 * w + G) * 8192 + (cb * 64 + lane) * 16, 0, SC1);
        }
      }
    }
    WAITV(0);
    BAR();
    if (tid == 0) __hip_atomic_store(ctl + WS_FXN / 4 + hid, epoch, RLX_AGENT);
    poll_flags(ctl + WS_FW2 / 4 + 4 * cg + w, true, epoch, epoch + 1u);
#pragma unroll
    for (int s = 0; s < 14; ++s)
#pragma unroll
      for (int c = 0; c < 4; ++c)
        aw[s][c] = __builtin_amdgcn_raw_buffer_load_b128(rsWS, WS_W2T + ((cg * 56 + 14 * w + s) * 4 + c) * 1024 + lane * 16, 0, SC1);
    SCHED();
    fxn = epoch + 1u;
  }

  poll_flags(ctl + WS_FXN / 4 + (lane & 31), true, epoch, fxn);
  {
    const int c0 = 64 * xcd + 2 * idx;
    const v4u u0 = __builtin_amdgcn_raw_buffer_load_b128(rsWS, WS_XNT + tid * 8192 + c0 * 16, 0, SC1);
    const v4u u1 = __builtin_amdgcn_raw_buffer_load_b128(rsWS, WS_XNT + tid * 8192 + c0 * 16 + 16, 0, SC1);
    if (tid < 64) *(LAS v4u*)(L + LD_U + (tid >> 5) * 4608 + (tid & 31) * 16) = v4u{0u, 0u, 0u, 0u};
    *(LAS v4u*)(L + LD_U + 512 + tid * 16) = u0;
    *(LAS v4u*)(L + LD_U + 4608 + 512 + tid * 16) = u1;
    BAR();
    const int cw = w >> 1, hw = w & 1;
    const int sg = 1 - (r16 & 1);
    LAS char* const ka = L + LD_KREG + (2 * cw + sg) * 8192 + sg * 64 + 2 * (2031 - r16 + 8 * q + sg) - hw * 256 - 7 * 512 - 3 * 64;
    LAS char* const ub = L + LD_U + cw * 4608 + 2 * (256 - 16 + 16 * r16 + 8 * q) - hw * 256 - 3 * 64;
    f32x4 y[8];
#pragma unroll
    for (int i = 0; i < 8; ++i) y[i] = f32x4{0.f, 0.f, 0.f, 0.f};
#pragma unroll
    for (int rr = 0; rr < 4; ++rr) {
      v4u af[8], bfv[8];
#pragma unroll
      for (int g = 0; g < 8; ++g) {
        LAS u32* pa = (LAS u32*)(ka + (7 - g) * 512 + (3 - rr) * 64);
        af[g] = v4u{pa[0], pa[1], pa[2], pa[3]};
        bfv[g] = *(LAS v4u*)(ub + g * 512 + (3 - rr) * 64);
      }
#pragma unroll
      for (int T = 0; T < 8; ++T)
#pragma unroll
        for (int g = 0; g <= T; ++g) y[T] = mfma16(af[g], bfv[T - g], y[T]);
    }
    f32x4 mine[4];
    if (hw) {
#pragma unroll
      for (int i = 0; i < 4; ++i) { *(LAS f32x4*)(L + LD_CX + w * 4096 + i * 1024 + lane * 16) = y[i]; mine[i] = y[4 + i]; }
    } else {
#pragma unroll
      for (int i = 0; i < 4; ++i) { *(LAS f32x4*)(L + LD_CX + w * 4096 + i * 1024 + lane * 16) = y[4 + i]; mine[i] = y[i]; }
    }
    BAR();
#pragma unroll
    for (int i = 0; i < 4; ++i) {
      const f32x4 yy = mine[i] + *(LAS f32x4*)(L + LD_CX + (w ^ 1) * 4096 + i * 1024 + lane * 16);
      const int t = 256 * (4 * hw + i) + 16 * r16 + 4 * q;
      const v2u xv = *(LAS v2u*)(L + LD_U + cw * 4608 + 512 + t * 2);
      const u32 ya = pk2(yy.x, yy.y), yb = pk2(yy.z, yy.w);
      const u32 da = pk2(bflo(xv.x) * dskip, bfhi(xv.x) * dskip), db = pk2(bflo(xv.y) * dskip, bfhi(xv.y) * dskip);
      v2u o;
      o.x = pk2(bflo(ya) + bflo(da), bfhi(ya) + bfhi(da));
      o.y = pk2(bflo(yb) + bflo(db), bfhi(yb) + bfhi(db));
      __builtin_amdgcn_raw_buffer_store_b64(o, rsWS, WS_ST + ch * 4096 + t * 2, 0, SC1);
    }
  }
  {
    const int n0 = (7 * w) >> 1;
    poll_flags(ctl + WS_FH / 4 + (tg >> 1) * 16 + n0 + (lane & 3), true, epoch, epoch + 1u);
    WAITV(0);
    BAR();
    if (tid == 0) __hip_atomic_store(ctl + WS_FS / 4 + xcd * 32 + idx, epoch, RLX_AGENT);
    constexpr int WIN = 9;
    v4u hf[14][4];
    const int hbase = WS_HT + (tg * 56 + 14 * w) * 4096 + lane * 16;
#define H_LOAD(s) _Pragma("unroll") for (int j = 0; j < 4; ++j) \
      hf[s][j] = __builtin_amdgcn_raw_buffer_load_b128(rsWS, hbase + (s) * 4096 + ((j + w) & 3) * 1024, 0, SC1);
#pragma unroll
    for (int s = 0; s < WIN; ++s) { H_LOAD(s) }
    const u32 fs = __hip_atomic_load(ctl + WS_FS / 4 + cg * 32 + (lane & 31), RLX_AGENT);
    const int tok = tg * 64 + 16 * w + r16;
    v4u xv[2], bv[2], sv[2];
#pragma unroll
    for (int pp = 0; pp < 2; ++pp) {
      xv[pp] = *(const GAS v4u*)(x + tok * 512 + cg * 64 + 32 * pp + 8 * q);
      bv[pp] = *(const GAS v4u*)(b2 + cg * 64 + 32 * pp + 8 * q);
    }
    SCHED();
    f32x4 acc[4][4];
#pragma unroll
    for (int i = 0; i < 4; ++i)
#pragma unroll
      for (int j = 0; j < 4; ++j) acc[i][j] = f32x4{0.f, 0.f, 0.f, 0.f};
#pragma unroll
    for (int s = 0; s < 14; ++s) {
#pragma unroll
      for (int j = 0; j < 4; ++j)
#pragma unroll
        for (int c = 0; c < 4; ++c) acc[c][j] = mfma16(aw[s][c], hf[s][j], acc[c][j]);
      SCHED();
      if (s + WIN < 14) { H_LOAD(s + WIN) }
      if (s == WIN - 1) {
        poll_flags(ctl + WS_FS / 4 + cg * 32 + (lane & 31), true, epoch, fs);
#pragma unroll
        for (int pp = 0; pp < 2; ++pp)
          sv[pp] = __builtin_amdgcn_raw_buffer_load_b128(rsWS, WS_ST + (cg * 64 + 32 * pp + (tid >> 3)) * 4096 + tg * 128 + (tid & 7) * 16, 0, SC1);
      }
      SCHED();
    }
#pragma unroll
    for (int pp = 0; pp < 2; ++pp) *(LAS v4u*)(L + LD_ST + (32 * pp + (tid >> 3)) * 128 + (tid & 7) * 16) = sv[pp];
#pragma unroll
    for (int j = 1; j < 4; ++j)
#pragma unroll
      for (int c = 0; c < 4; ++c)
        *(LAS f32x4*)(L + LD_RED + ((((j + w) & 3) * 3 + (j - 1)) * 4 + c) * 1024 + lane * 16) = acc[c][j];
    BAR();
    const int qq = r16 >> 2, p = lane & 3;
#pragma unroll
    for (int pp = 0; pp < 2; ++pp) {
      u32 o[4];
#pragma unroll
      for (int e = 0; e < 2; ++e) {
        const int c = 2 * pp + e;
        f32x4 m = acc[c][0];
#pragma unroll
        for (int j = 0; j < 3; ++j) m += *(LAS f32x4*)(L + LD_RED + ((w * 3 + j) * 4 + c) * 1024 + lane * 16);
        const v2u sq = trread(L + LD_ST + (32 * pp + 8 * q + 4 * e + qq) * 128 + (16 * w + 4 * p) * 2);
        const u32 ba = bv[pp][2 * e], bb = bv[pp][2 * e + 1], xa = xv[pp][2 * e], xb = xv[pp][2 * e + 1];
        m += f32x4{bflo(ba), bfhi(ba), bflo(bb), bfhi(bb)};
        f32x4 g;
#pragma unroll
        for (int k = 0; k < 4; ++k) g[k] = m[k] * __builtin_amdgcn_rcpf(1.0f + __builtin_amdgcn_exp2f(m[k] * -1.4426950408889634f));
        o[2 * e] = pk2(bflo(xa) + bflo(sq.x) * g.x, bfhi(xa) + bfhi(sq.x) * g.y);
        o[2 * e + 1] = pk2(bflo(xb) + bflo(sq.y) * g.z, bfhi(xb) + bfhi(sq.y) * g.w);
      }
      *(GAS v4u*)(out + tok * 512 + cg * 64 + 32 * pp + 8 * q) = v4u{o[0], o[1], o[2], o[3]};
    }
  }
  if (b == 0) {
    poll_flags(ctl + WS_FS / 4 + tid, true, epoch, epoch + 1u);
    BAR();
    if (tid == 0) __hip_atomic_store(ctl + WS_E / 4, epoch, RLX_AGENT);
  }
}

extern "C" void kernel_launch(void* const* d_in, const int* in_sizes, int n_in, void* d_out, int out_size, void* d_ws,
                              size_t ws_size, hipStream_t stream) {
  (void)in_sizes; (void)n_in; (void)out_size; (void)ws_size;
  (void)hipFuncSetAttribute((const void*)s4_mixer_kernel, hipFuncAttributeMaxDynamicSharedMemorySize, LDS_BYTES);
  s4_mixer_kernel<<<256, 256, LDS_BYTES, stream>>>(
      (const u16*)d_in[0], (const u16*)d_in[1], (const u16*)d_in[2], (const u16*)d_in[3], (const u16*)d_in[4],
      (const u16*)d_in[5], (const u16*)d_in[6], (const u16*)d_in[7], (const u16*)d_in[8], (const u16*)d_in[10],
      (const u16*)d_in[11], (const u16*)d_in[12], (const u16*)d_in[13], (u16*)d_out, (char*)d_ws);
}
```

```cpp
#include <hip/hip_runtime.h>
#include <hip/hip_bf16.h>

typedef unsigned int u32;
typedef unsigned short u16;
typedef u32 v4u __attribute__((ext_vector_type(4)));
typedef u32 v2u __attribute__((ext_vector_type(2)));
typedef float f32x2 __attribute__((ext_vector_type(2)));
typedef float f32x4 __attribute__((ext_vector_type(4)));
typedef float f32x16 __attribute__((ext_vector_type(16)));
typedef short s16x4 __attribute__((ext_vector_type(4)));
typedef short s16x8 __attribute__((ext_vector_type(8)));
typedef __bf16 bf16x2 __attribute__((ext_vector_type(2)));
#define GAS __attribute__((address_space(1)))
#define LAS __attribute__((address_space(3)))
#define DEV __device__ __forceinline__
#define RLX_AGENT __ATOMIC_RELAXED, __HIP_MEMORY_SCOPE_AGENT
#define SC1 16
#define TS(i) do { } while (0)
#define TC(i) do { } while (0)

constexpr int WS_E = 0;
constexpr int FL = 32;
constexpr int WS_STAT = 4096;
constexpr int WS_FXN = 65536;
constexpr int WS_FW2 = WS_FXN + 256 * 128;
constexpr int WS_FH = WS_FW2 + 1024 * 128;
constexpr int WS_FS = WS_FH + 1024 * 128;
constexpr int WS_XNT = 524288;
constexpr int WS_W2T = WS_XNT + 2097152;
constexpr int WS_HT = WS_W2T + 1835008;
constexpr int WS_ST = WS_HT + 7340032;
constexpr int WS_END = WS_ST + 2097152;
constexpr int LD_Q = 98304;
constexpr int LD_KREG = 131072;
constexpr int LD_PS = LD_KREG + 7424;
constexpr int LD_DUMMY = LD_KREG + 7680;
constexpr int LD_B1 = LD_KREG + 8192 + 7424;
constexpr int LD_HST = LD_KREG + 3 * 8192 + 7424;
constexpr int LD_STAT = LD_KREG + 4352;
constexpr int LD_U = 0;
constexpr int LD_CX = 16384;
constexpr int LD_HX = 32768;
constexpr int LD_RED = 0;
constexpr int LD_ST = 114688;
constexpr int ABL_HT = WS_HT;
constexpr int LDS_BYTES = 163840;
constexpr u32 SPIN_CAP = 1u << 16;
constexpr u32 CODE_BYTES = $$$$$$$$$$$$$$$$$$$$$$$$$$$$$$$$$$;

DEV float bf2f(u32 h) { return __builtin_bit_cast(float, h << 16); }
DEV float bflo(u32 d) { return __builtin_bit_cast(float, d << 16); }
DEV float bfhi(u32 d) { return __builtin_bit_cast(float, d & 0xffff0000u); }
DEV u32 pk2(float lo, float hi) {
  f32x2 v = {lo, hi};
  return __builtin_bit_cast(u32, __builtin_convertvector(v, bf16x2));
}
DEV u32 pk2v(f32x2 v) { return __builtin_bit_cast(u32, __builtin_convertvector(v, bf16x2)); }
DEV float bfr(float v) { return bflo(pk2(v, v)); }
DEV f32x4 mfma16(v4u a, v4u b, f32x4 c) {
  return __builtin_amdgcn_mfma_f32_16x16x32_bf16(__builtin_bit_cast(s16x8, a), __builtin_bit_cast(s16x8, b), c, 0, 0, 0);
}
DEV f32x16 mfma32(v4u a, v4u b, f32x16 c) {
  return __builtin_amdgcn_mfma_f32_32x32x16_bf16(__builtin_bit_cast(s16x8, a), __builtin_bit_cast(s16x8, b), c, 0, 0, 0);
}
DEV v2u trread(LAS char* p) { return __builtin_bit_cast(v2u, __builtin_amdgcn_ds_read_tr16_b64_v4i16((LAS s16x4*)p)); }
DEV v4u mk_rsrc(const void* p, u32 bytes, u32 fmt) {
  const unsigned long long a = (unsigned long long)p;
  v4u r = {(u32)a, (u32)(a >> 32), bytes, fmt};
  return r;
}
DEV void dma16(u32 voff, v4u rs, u32 soff, u32 lds) {
  asm volatile("s_mov_b32 m0, %0\n\ts_nop 0\n\tbuffer_load_dwordx4 %1, %2, %3 offen lds" ::"s"(lds), "v"(voff), "s"(rs), "s"(soff) : "memory");
}
#define WAITV(n) asm volatile("s_waitcnt vmcnt(" #n ")" ::: "memory")
#define WAITL() asm volatile("s_waitcnt lgkmcnt(0)" ::: "memory")
#define SCHED() __builtin_amdgcn_sched_barrier(0)
#define BAR() do { asm volatile("s_waitcnt lgkmcnt(0)" ::: "memory"); __builtin_amdgcn_s_barrier(); asm volatile("" ::: "memory"); SCHED(); } while (0)
#define ORDER_BEHIND_POLL() __builtin_amdgcn_fence(__ATOMIC_ACQUIRE, "wavefront")

DEV f32x2 cmul(f32x2 a, f32x2 z) {
  const f32x2 zr = {-z.y, z.x};
  f32x2 t, r;
  asm("v_pk_mul_f32 %0, %1, %2 op_sel:[1,0] op_sel_hi:[1,1]" : "=v"(t) : "v"(a), "v"(zr));
  asm("v_pk_fma_f32 %0, %1, %2, %3 op_sel_hi:[0,1,1]" : "=v"(r) : "v"(a), "v"(z), "v"(t));
  return r;
}
DEV f32x2 zpow(float la, float th, float k) {
  const float mag = __builtin_amdgcn_exp2f(la * k);
  const float fr = __builtin_amdgcn_fractf(th * k);
  f32x2 r = {mag * __builtin_amdgcn_cosf(fr), mag * __builtin_amdgcn_sinf(fr)};
  return r;
}
template <int SLEEP>
DEV void poll_flags(GAS u32* p, u32 epoch, u32 first) {
  if (!__all(first == epoch)) {
#pragma unroll 1
    for (u32 spins = 0; spins < SPIN_CAP; ++spins) {
      const u32 v = __hip_atomic_load(p, RLX_AGENT);
      if (__all(v == epoch)) break;
      __builtin_amdgcn_s_sleep(SLEEP);
    }
  }
  ORDER_BEHIND_POLL();
}
DEV void publish(GAS u32* p, u32 epoch, int lane) {
  WAITV(0);
  if (lane == 0) __hip_atomic_store(p, epoch, RLX_AGENT);
}
DEV void set_progress(LAS char* L, int wv, u32 k) {
  WAITL();
  *(volatile LAS u32*)(L + LD_PS + wv * 4) = k;
}
DEV void wait_progress(LAS char* L, int word, u32 k) {
#pragma unroll 1
  for (u32 spins = 0; spins < (1u << 22); ++spins) {
    const u32 pv = *(volatile LAS u32*)(L + LD_PS + word * 4);
    if (__all(pv >= k)) break;
    __builtin_amdgcn_s_sleep(1);
  }
  asm volatile("" ::: "memory");
}
DEV void ln_stats(float s1, float s2, float& mean, float& var, float& den1) {
  mean = s1 * (1.0f / 512.0f);
  var = fmaxf(s2 * (1.0f / 512.0f) - mean * mean, 0.0f);
  den1 = bfr(__builtin_amdgcn_sqrtf(bfr(bfr(var) + 1e-5f)));
}
DEV float pick4(f32x4 g, int k) { return k == 0 ? g.x : k == 1 ? g.y : k == 2 ? g.z : g.w; }
#define LD16(p, i) ((u32)__hip_atomic_load((const GAS u16*)(p) + (i), __ATOMIC_RELAXED, __HIP_MEMORY_SCOPE_WAVEFRONT))

__global__ void __launch_bounds__(512, 2)
s4_mixer_kernel(const u16* __restrict__ x, u32 x_bytes, u32 x_fmt, const u16* __restrict__ w1, u32 w1_bytes, u32 w1_fmt,
                const u16* __restrict__ w2, char* __restrict__ ws,
                const u16* __restrict__ A_re, const u16* __restrict__ A_im, const u16* __restrict__ B_re,
                const u16* __restrict__ B_im, const u16* __restrict__ C_re, const u16* __restrict__ C_im,
                const u16* __restrict__ log_dt, const u16* __restrict__ Dskip, const u16* __restrict__ b1,
                const u16* __restrict__ b2, u16* __restrict__ out) {
  extern __shared__ __attribute__((aligned(16))) char smem[];
  LAS char* const L = (LAS char*)smem;
  unsigned long long pc;
  asm volatile("s_getpc_b64 %0" : "=s"(pc));
  {
    u32 coff = ((((threadIdx.x & 63) << 3) | (threadIdx.x >> 6)) * 32u + (blockIdx.x >> 3)) * 64u;
    coff = coff < CODE_BYTES ? coff : 0u;
    asm volatile("s_mov_b32 m0, %0\n\ts_nop 0\n\tglobal_load_lds_dword %1, %2" ::"s"((u32)LD_DUMMY), "v"(coff), "s"(pc) : "memory");
    SCHED();
  }
  const int tid = threadIdx.x, lane = tid & 63;
  const int wv = __builtin_amdgcn_readfirstlane(tid >> 6);
  const int w = wv & 3;
  const bool swave = wv >= 4;
  const int b = blockIdx.x, xcd = b & 7, idx = b >> 3;
  const int r16 = lane & 15, q = lane >> 4, qq = r16 >> 2, p4 = lane & 3;
  const bool helper = idx >= 28;
  TS(0);
  const v4u rsX = mk_rsrc(x, x_bytes, x_fmt);
  const v4u rsW1 = mk_rsrc(w1, w1_bytes, w1_fmt);
  asm volatile("" ::"s"(rsX), "s"(rsW1), "s"(w2), "s"(ws), "s"(A_re), "s"(A_im), "s"(B_re), "s"(B_im), "s"(C_re), "s"(C_im), "s"(log_dt), "s"(Dskip), "s"(b1));
  u32 eraw;
  asm volatile("s_load_dword %0, %1, 0x0" : "=s"(eraw) : "s"(ws));
  const int mq = (idx * 37) >> 8, mr = idx - 7 * mq;
  const int mt = (xcd >> 1) * 4 + mq, nt = (xcd & 1) * 7 + mr;
  const int hid = xcd * 4 + (idx - 28);
  const int tg = xcd * 4 + (idx >> 3), cg = idx & 7;
  const int cw = w >> 1, hw = w & 1;
  const int ch = 64 * xcd + 2 * idx + cw;
  const __amdgpu_buffer_rsrc_t rsWS = __builtin_amdgcn_make_buffer_rsrc((void*)ws, 0, WS_END, 0x00020000);
  GAS u32* const ctl = (GAS u32*)ws;
  const v4u ones = {0x3f803f80u, 0x3f803f80u, 0x3f803f80u, 0x3f803f80u};
  int rot2 = cg + 2 * (tg & 3);
  rot2 -= rot2 >= 7 ? 7 : 0;
#define KS2(s) (7 * wv + ((s) + rot2 >= 7 ? (s) + rot2 - 7 : (s) + rot2))
#define AW_LOAD(s, c) \
  aw[s][c] = __builtin_amdgcn_raw_buffer_load_b128(rsWS, lane * 16 + ((((c) + 2 * (wv & 1)) & 3) << 10), WS_W2T + (cg * 56 + KS2(s)) * 4096, SC1)
  const u32 wo = (u32)lane * 4u;
#define TW(rowp, row, val) *(LAS u32*)((rowp) + (wo ^ (u32)(((row) & 15) << 4))) = (val)
  LAS char* const PT = L + LD_KREG + w * 8192;

  auto gemm2_tail = [&](v4u (&aw)[7][4], const u32 epoch) __attribute__((always_inline)) {
  {
    TS(9);
    int tz = threadIdx.x;
    asm volatile("" : "+v"(tz));
    const int lane = tz & 63, r16 = lane & 15, q = lane >> 4, qq = r16 >> 2, p4 = lane & 3;
    poll_flags<2>(ctl + WS_FH / 4 + (tg * 32 + ((7 * wv) >> 1) + (lane & 3)) * FL, epoch, epoch + 1u);
    TS(10);
    constexpr int WIN = 4;
    v4u hf[7][4];
#define H_LOAD(s) _Pragma("unroll") for (int j = 0; j < 4; ++j) \
      hf[s][j] = __builtin_amdgcn_raw_buffer_load_b128(rsWS, lane * 16 + (((j + (wv >> 1)) & 3) << 10), WS_HT + (tg * 56 + KS2(s)) * 4096, SC1);
#pragma unroll
    for (int s = 0; s < WIN; ++s) { H_LOAD(s) SCHED(); }
    GAS u32* const fsp = ctl + WS_FS / 4 + ((tg >> 4) * 512 + cg * 64 + wv * 8 + (lane >> 3)) * FL;
    const int tok = tg * 64 + 16 * (wv >> 1) + r16;
    const int c8 = cg * 64 + 32 * (wv & 1) + 8 * q;
    u32 fs;
    v4u xv, bv, sv;
    f32x4 acc[4][4];
#pragma unroll
    for (int i = 0; i < 4; ++i)
#pragma unroll
      for (int j = 0; j < 4; ++j) acc[i][j] = f32x4{0.f, 0.f, 0.f, 0.f};
#pragma unroll
    for (int s = 0; s < 7; ++s) {
#pragma unroll
      for (int j = 0; j < 4; ++j)
#pragma unroll
        for (int c = 0; c < 4; ++c) acc[c][j] = mfma16(aw[s][c], hf[s][j], acc[c][j]);
      SCHED();
      if (s == 0) fs = __hip_atomic_load(fsp, RLX_AGENT);
      if (s + WIN < 7) { H_LOAD(s + WIN) }
      if (s == 2) {
        xv = *(const GAS v4u*)(x + tok * 512 + c8);
        bv = *(const GAS v4u*)(b2 + c8);
      }
      if (s == 4) {
        poll_flags<2>(fsp, epoch, fs);
        sv = __builtin_amdgcn_raw_buffer_load_b128(rsWS, (lane >> 3) * 4096 + (lane & 7) * 16, WS_ST + (cg * 64 + wv * 8) * 4096 + tg * 128, SC1);
      }
      SCHED();
    }
    TS(11);
    wait_progress(L, lane & 7, 4u);
#pragma unroll
    for (int j = 0; j < 4; ++j)
#pragma unroll
      for (int cp = 0; cp < 2; ++cp)
        if (j + cp > 0) {
          const int d = (((j + (wv >> 1)) & 3) << 1) | ((cp + wv) & 1);
#pragma unroll
          for (int e = 0; e < 2; ++e)
            *(LAS f32x4*)(L + LD_RED + (d * 7 + 2 * j + cp - 1) * 2048 + e * 1024 + lane * 16) = acc[2 * cp + e][j];
        }
    *(LAS v4u*)(L + LD_ST + wv * 1024 + lane * 16) = sv;
    TS(12);
    BAR();
    TS(13);
    f32x4 part[2][7];
    v2u sq[2];
#pragma unroll
    for (int e = 0; e < 2; ++e) {
#pragma unroll
      for (int j = 0; j < 7; ++j) part[e][j] = *(LAS f32x4*)(L + LD_RED + (wv * 7 + j) * 2048 + e * 1024 + lane * 16);
      sq[e] = trread(L + LD_ST + (32 * (wv & 1) + 8 * q + 4 * e + qq) * 128 + (16 * (wv >> 1) + 4 * p4) * 2);
    }
    SCHED();
    u32 o[4];
#pragma unroll
    for (int e = 0; e < 2; ++e) {
      const u32 ba = bv[2 * e], bb = bv[2 * e + 1], xa = xv[2 * e], xb = xv[2 * e + 1];
      f32x4 m = acc[e][0] + f32x4{bflo(ba), bfhi(ba), bflo(bb), bfhi(bb)};
#pragma unroll
      for (int j = 0; j < 7; ++j) m += part[e][j];
      f32x4 g;
#pragma unroll
      for (int k = 0; k < 4; ++k) g[k] = m[k] * __builtin_amdgcn_rcpf(1.0f + __builtin_amdgcn_exp2f(m[k] * -1.4426950408889634f));
      o[2 * e] = pk2(bflo(xa) + bflo(sq[e].x) * g.x, bfhi(xa) + bfhi(sq[e].x) * g.y);
      o[2 * e + 1] = pk2(bflo(xb) + bflo(sq[e].y) * g.z, bfhi(xb) + bfhi(sq[e].y) * g.w);
    }
    *(GAS v4u*)(out + tok * 512 + c8) = v4u{o[0], o[1], o[2], o[3]};
  }
  TS(14);
  if (b == 0) {
    poll_flags<2>(ctl + WS_FS / 4 + tid * FL, epoch, epoch + 1u);
    poll_flags<2>(ctl + WS_FS / 4 + (512 + tid) * FL, epoch, epoch + 1u);
    BAR();
    if (tid == 0) __hip_atomic_store(ctl + WS_E / 4, epoch, RLX_AGENT);
  }
  __builtin_amdgcn_endpgm();
  };

  if (helper) {
    LAS char* const HX = L + LD_HX + wv * 8192;
#pragma unroll
    for (int p = 0; p < 8; ++p)
      dma16((u32)(wv * 8192 + ((lane ^ (((p & 3) << 2) | (p >> 2))) << 4)), rsX, (u32)((hid * 64 + p) * 1024), (u32)(LD_HX + (wv * 8 + p) * 1024));
    u32 epoch;
    asm volatile("s_waitcnt vmcnt(0) lgkmcnt(0)\n\ts_add_u32 %0, %1, 1" : "=s"(epoch) : "s"(eraw) : "memory", "scc");
    f32x4 gram[2], sumx[2];
#pragma unroll
    for (int i = 0; i < 2; ++i) { gram[i] = f32x4{0.f, 0.f, 0.f, 0.f}; sumx[i] = gram[i]; }
    const int r8 = lane & 7, fr = ((r8 & 3) << 2) | (r8 >> 2);
    v4u xr[8];
#pragma unroll
    for (int s = 0; s < 8; ++s) xr[s] = *(LAS v4u*)(HX + r8 * 1024 + (lane & 8) * 64 + (((4 * s + q) ^ fr) << 4));
    v2u d[8][2];
#pragma unroll
    for (int s = 0; s < 8; ++s)
#pragma unroll
      for (int t = 0; t < 2; ++t)
        d[s][t] = trread(HX + (4 * t + qq) * 1024 + (((8 * s + 2 * q + (p4 >> 1)) ^ ((qq << 2) | t)) << 4) + 8 * (p4 & 1));
    SCHED();
#pragma unroll
    for (int s = 0; s < 8; ++s) {
      gram[s & 1] = mfma16(xr[s], xr[s], gram[s & 1]);
      sumx[s & 1] = mfma16(ones, xr[s], sumx[s & 1]);
    }
    float s1 = sumx[0].x + sumx[1].x, s2 = pick4(gram[0] + gram[1], r16 & 3);
    s1 += __shfl(s1, (lane + 40) & 63);
    s2 += __shfl(s2, (lane + 40) & 63);
    float mean, var, den1;
    ln_stats(s1, s2, mean, var, den1);
    const float rd = __builtin_amdgcn_rcpf(den1);
    const float den2 = bfr(__builtin_amdgcn_sqrtf(bfr(bfr(var * rd * rd) + 1e-5f)));
    const float R = __builtin_amdgcn_rcpf(den1 * den2);
    if (q == qq && r16 < 8) {
      *(LAS f32x2*)(L + LD_HST + (8 * wv + r16) * 8) = f32x2{rd, -bfr(mean) * rd};
      __builtin_amdgcn_raw_buffer_store_b128(v4u{__builtin_bit_cast(u32, R), epoch, __builtin_bit_cast(u32, R * mean), epoch}, rsWS,
                                             WS_STAT + (hid * 64 + 8 * wv + r16) * 16, 0, SC1);
    }
    WAITL();
    f32x4 sv[4];
#pragma unroll
    for (int i = 0; i < 4; ++i) sv[i] = *(LAS f32x4*)(L + LD_HST + wv * 64 + i * 16);
#pragma unroll
    for (int cb = 0; cb < 8; ++cb) {
      v4u o;
      o.x = pk2(bflo(d[cb][0].x) * sv[0].x + sv[0].y, bfhi(d[cb][0].x) * sv[0].z + sv[0].w);
      o.y = pk2(bflo(d[cb][0].y) * sv[1].x + sv[1].y, bfhi(d[cb][0].y) * sv[1].z + sv[1].w);
      o.z = pk2(bflo(d[cb][1].x) * sv[2].x + sv[2].y, bfhi(d[cb][1].x) * sv[2].z + sv[2].w);
      o.w = pk2(bflo(d[cb][1].y) * sv[3].x + sv[3].y, bfhi(d[cb][1].y) * sv[3].z + sv[3].w);
      __builtin_amdgcn_raw_buffer_store_b128(o, rsWS, lane * 16 + WS_XNT + (hid * 8 + wv) * 8192 + cb * 1024, 0, SC1);
    }
    publish(ctl + WS_FXN / 4 + (hid * 8 + wv) * FL, epoch, lane);
    __builtin_amdgcn_s_waitcnt(0x0F70);
    SCHED();
  }

  if (swave) {
    __builtin_amdgcn_s_setprio(2);
    const u32 voX = (u32)((32 * w + (lane >> 3)) * 1024 + (((lane & 7) ^ (lane >> 3)) << 4));
    const u32 voW = (u32)((4 * w + q) * 3584 + ((r16 ^ ((q << 2) | w)) << 4));
    const int rot = (mr + 2 * mq) & 7;
#define G1_STAGE(st) G1_PART(st, 0, 4)
#define G1_PART(st, p0, p1)                                                                                  \
  {                                                                                                          \
    const int sa_ = ((st) + rot) & 7;                                                                        \
    _Pragma("unroll") for (int p = p0; p < p1; ++p) {                                                        \
      dma16(voX, rsX, (u32)(mt * 131072 + p * 8192 + sa_ * 128), (u32)(((st) & 3) * 32768 + w * 4096 + p * 1024)); \
      dma16(voW, rsW1, (u32)((sa_ * 64 + 16 * p) * 3584 + nt * 256), (u32)(((st) & 3) * 32768 + 16384 + p * 4096 + w * 1024)); \
    }                                                                                                        \
  }
    if (!helper) { G1_STAGE(0) G1_STAGE(1) G1_STAGE(2) }
    if (lane == 0) *(LAS u32*)(L + LD_PS + wv * 4) = 0u;
    TS(1);
    WAITV(16);
    u32 epoch;
    asm volatile("s_waitcnt lgkmcnt(0)\n\ts_add_u32 %0, %1, 1" : "=s"(epoch) : "s"(eraw) : "memory", "scc");
    TS(2);
    BAR();
    TS(3);
    TC(0);
    const int r32 = lane & 31, hh = lane >> 5;
    v4u pf[8], qf[8];
    {
#pragma unroll
      for (int s = 0; s < 8; ++s) {
        qf[s] = *(LAS v4u*)(L + LD_Q + w * 8192 + r32 * 256 + (((2 * s + hh) ^ (r32 & 15)) << 4));
        pf[s] = *(LAS v4u*)(PT + (2 * s + hh) * 464 + r32 * 16);
      }
    }
    SCHED();
    TC(1);
    if (!helper) {
      WAITV(8); BAR(); TC(2); G1_STAGE(3) G1_PART(4, 0, 2) TC(3);
      WAITV(12); BAR(); TC(4); G1_PART(4, 2, 4) G1_STAGE(5) TC(5);
      WAITV(16); BAR(); TC(6);
    }
    SCHED();
    f32x16 kacc;
#pragma unroll
    for (int i = 0; i < 16; ++i) kacc[i] = 0.f;
#pragma unroll
    for (int s = 0; s < 4; ++s) {
      kacc = mfma32(pf[2 * s], qf[2 * s], kacc);
      kacc = mfma32(pf[2 * s + 1], qf[2 * s + 1], kacc);
      SCHED();
      if (!helper) G1_PART(6, s, s + 1)
      SCHED();
    }
#pragma unroll
    for (int i = 0; i < 4; ++i)
      *(LAS f32x4*)(PT + i * 1024 + lane * 16) = f32x4{kacc[4 * i], kacc[4 * i + 1], kacc[4 * i + 2], kacc[4 * i + 3]};
    TC(7);
    WAITV(16); BAR();
    TC(8);
    {
      LAS char* const PP = L + LD_KREG + (w ^ 1) * 8192;
      f32x4 v[4];
#pragma unroll
      for (int i = 0; i < 4; ++i) v[i] = *(LAS f32x4*)(PP + i * 1024 + lane * 16);
      SCHED();
      if (!helper) G1_STAGE(7)
      SCHED();
#pragma unroll
      for (int i = 0; i < 15; ++i) {
        float t = kacc[i];
        asm("v_add_f32 %0, %0, %1" : "+v"(t) : "v"(v[i >> 2][i & 3]));
        kacc[i] = t;
      }
    }
    TC(9);
    WAITV(16); BAR();
    TC(10);
    GAS u32* const fxnp = ctl + WS_FXN / 4 + (hw * 128 + lane) * FL;
    GAS u32* const fw2p = ctl + WS_FW2 / 4 + ((cg * 56 + 7 * (w + 4 * ((lane >> 4) & 1))) * 2 + min(r16, 13)) * FL;
    const int sto = WS_STAT + ((helper ? 0 : mt) * 128 + 32 * w + r32) * 16;
    v4u sg = __builtin_amdgcn_raw_buffer_load_b128(rsWS, sto, 0, SC1);
    const u32 fw2 = __hip_atomic_load(fw2p, RLX_AGENT);
    const u32 pDs = LD16(Dskip, ch);
    const u32 fxa = __hip_atomic_load(fxnp, RLX_AGENT);
    const u32 fxb = __hip_atomic_load(fxnp + 64 * FL, RLX_AGENT);
    SCHED();
    {
      const bool odd = r32 & 1, up = hh != 0;
      float e[4][9];
      const int ia[4][9] = {{0, 1, 2, 3, 4, 5, 6, 7, 8}, {9, 9, 9, 9, -1, 10, 10, 10, 10}, {11, 11, 11, 11, -1, 12, 12, 12, 12}, {13, 13, 13, 13, -1, 14, 14, 14, 14}};
      const int ib[4][9] = {{0, 0, 1, 2, 2, 2, 3, 4, 4}, {5, 5, 5, 6, 6, 6, 7, 7, 7}, {8, 8, 8, 9, 9, 9, 10, 10, 10}, {11, 11, 11, 11, -1, 12, 12, 12, 12}};
#pragma unroll
      for (int u = 0; u < 4; ++u)
#pragma unroll
        for (int i = 0; i < 9; ++i) {
          const float va = ia[u][i] >= 0 ? kacc[ia[u][i]] : (odd ? kacc[ia[u][5]] : kacc[ia[u][3]]);
          const float vb = ib[u][i] >= 0 ? kacc[ib[u][i]] : (odd ? kacc[ib[u][5]] : kacc[ib[u][3]]);
          e[u][i] = up ? vb : va;
        }
      LAS char* const KC = PT + hw * 64 + (255 - r32) * 16;
#pragma unroll
      for (int u = 0; u < 4; ++u) {
        v4u d;
        if (hw) { d.x = pk2(e[u][8], e[u][7]); d.y = pk2(e[u][6], e[u][5]); d.z = pk2(e[u][4], e[u][3]); d.w = pk2(e[u][2], e[u][1]); }
        else    { d.x = pk2(e[u][7], e[u][6]); d.y = pk2(e[u][5], e[u][4]); d.z = pk2(e[u][3], e[u][2]); d.w = pk2(e[u][1], e[u][0]); }
        const int sa = u == 0 ? 0 : u + 3, sb = u == 3 ? 7 : u + 1;
        *(LAS v4u*)(KC - (up ? sb : sa) * 512) = d;
      }
      if (lane < 2) *(LAS v4u*)(PT + hw * 64 + 4096 + lane * 16) = v4u{(hw && lane == 0) ? pk2(kacc[0], 0.f) : 0u, 0u, 0u, 0u};
    }
    TC(11);
    if (!helper) { WAITV(5); BAR(); }
    TC(12);
    TS(4);
    if (!helper) {
      if (!__all(sg.y == epoch && sg.w == epoch)) {
#pragma unroll 1
        for (u32 spins = 0; spins < SPIN_CAP; ++spins) {
          __builtin_amdgcn_s_sleep(4);
          asm volatile("" ::: "memory");
          sg = __builtin_amdgcn_raw_buffer_load_b128(rsWS, sto, 0, SC1);
          if (__all(sg.y == epoch && sg.w == epoch)) break;
        }
      }
      *(LAS v2u*)(L + LD_STAT + (32 * w + r32) * 8) = v2u{sg.x, sg.z};
    }
    poll_flags<4>(fw2p, epoch, fw2);
    set_progress(L, wv, 1u);
    TS(5);
    poll_flags<4>(fxnp, epoch, fxa);
    poll_flags<4>(fxnp + 64 * FL, epoch, fxb);
    const v4u u0 = __builtin_amdgcn_raw_buffer_load_b128(rsWS, lane * 8192, WS_XNT + (128 * hw) * 8192 + ch * 16, SC1);
    const v4u u1 = __builtin_amdgcn_raw_buffer_load_b128(rsWS, lane * 8192, WS_XNT + (128 * hw + 64) * 8192 + ch * 16, SC1);
    SCHED();
    TC(13);
    v4u aw[7][4];
#pragma unroll
    for (int c = 0; c < 28; ++c) AW_LOAD(c >> 2, c & 3);
    {
      const u32 po = (u32)(WS_W2T + (cg * 56 + 7 * w) * 4096 + ((tg & 3) * 56 + min(lane, 55)) * 128);
      asm volatile("s_mov_b32 m0, %0\n\ts_nop 0\n\tglobal_load_lds_dword %1, %2 sc1" ::"s"((u32)LD_DUMMY), "v"(po), "s"(ws) : "memory");
    }
    SCHED();
    TS(6);
    LAS char* const UB = L + LD_U + cw * 4608;
    if (hw == 0 && lane < 32) *(LAS v4u*)(UB + lane * 16) = v4u{0u, 0u, 0u, 0u};
    *(LAS v4u*)(UB + 512 + (128 * hw + lane) * 16) = u0;
    *(LAS v4u*)(UB + 512 + (128 * hw + 64 + lane) * 16) = u1;
    set_progress(L, wv, 2u);
    wait_progress(L, wv ^ 1, 2u);
    wait_progress(L, w, 4u);
    TS(7);
    const float dskip = bf2f(pDs);
    const int sgm = 1 - (r16 & 1);
    LAS char* const ka = L + LD_KREG + (2 * cw + sgm) * 8192 + sgm * 64 + 2 * (2031 - r16 + 8 * q + sgm) - hw * 256 - 7 * 512 - 3 * 64;
    LAS char* const ub = UB + 2 * (256 - 16 + 16 * r16 + 8 * q) - hw * 256 - 3 * 64;
    f32x4 y[8];
#pragma unroll
    for (int i = 0; i < 8; ++i) y[i] = f32x4{0.f, 0.f, 0.f, 0.f};
    v4u af[4][8], bfv[4][8];
    u32 kb[4];
#pragma unroll
    for (int i = 0; i < 4; ++i) { kb[i] = (u32)(size_t)ka + i * 1024; asm volatile("" : "+v"(kb[i])); }
#define CV_READ(rr, g)                                                                                   \
  {                                                                                                      \
    LAS u32* pa = (LAS u32*)(size_t)(kb[(7 - (g)) >> 1] + ((7 - (g)) & 1) * 512 + (3 - (rr)) * 64);      \
    af[rr][g] = v4u{pa[0], pa[1], pa[2], pa[3]};                                                         \
    bfv[rr][g] = *(LAS v4u*)(ub + (g) * 512 + (3 - (rr)) * 64);                                          \
  }
    CV_READ(0, 0) CV_READ(0, 1) CV_READ(0, 2) CV_READ(0, 3) CV_READ(0, 4)
    SCHED();
#pragma unroll
    for (int rr = 0; rr < 4; ++rr) {
#pragma unroll
      for (int k = 0; k < 8; ++k) {
        if (k + 5 < 8) CV_READ(rr, k + 5)
        else if (rr < 3) CV_READ(rr + 1, k + 5 - 8)
#pragma unroll
        for (int o = 0; o <= k; ++o)
          if (k + o <= 7) {
            y[k + o] = mfma16(af[rr][k], bfv[rr][o], y[k + o]);
            if (o < k) y[k + o] = mfma16(af[rr][o], bfv[rr][k], y[k + o]);
          }
        SCHED();
      }
    }
    TC(14);
    f32x4 mine[4];
    if (hw) {
#pragma unroll
      for (int i = 0; i < 4; ++i) { *(LAS f32x4*)(L + LD_CX + w * 4096 + i * 1024 + lane * 16) = y[i]; mine[i] = y[4 + i]; }
    } else {
#pragma unroll
      for (int i = 0; i < 4; ++i) { *(LAS f32x4*)(L + LD_CX + w * 4096 + i * 1024 + lane * 16) = y[4 + i]; mine[i] = y[i]; }
    }
    set_progress(L, wv, 3u);
    wait_progress(L, wv ^ 1, 3u);
    f32x4 oy[4];
    v2u xv[4];
#pragma unroll
    for (int i = 0; i < 4; ++i) {
      oy[i] = *(LAS f32x4*)(L + LD_CX + (w ^ 1) * 4096 + i * 1024 + lane * 16);
      xv[i] = *(LAS v2u*)(UB + 512 + (256 * (4 * hw + i) + 16 * r16 + 4 * q) * 2);
    }
    set_progress(L, wv, 4u);
#pragma unroll
    for (int i = 0; i < 4; ++i) {
      const f32x4 yy = mine[i] + oy[i];
      const u32 ya = pk2(yy.x, yy.y), yb = pk2(yy.z, yy.w);
      const u32 da = pk2(bflo(xv[i].x) * dskip, bfhi(xv[i].x) * dskip), db = pk2(bflo(xv[i].y) * dskip, bfhi(xv[i].y) * dskip);
      v2u o;
      o.x = pk2(bflo(ya) + bflo(da), bfhi(ya) + bfhi(da));
      o.y = pk2(bflo(yb) + bflo(db), bfhi(yb) + bfhi(db));
      __builtin_amdgcn_raw_buffer_store_b64(o, rsWS, (16 * r16 + 4 * q) * 2 + WS_ST + ch * 4096 + (4 * hw + i) * 512, 0, SC1);
    }
    TS(8);
    __builtin_amdgcn_s_setprio(0);
    publish(ctl + WS_FS / 4 + (hw * 512 + ch) * FL, epoch, lane);
    gemm2_tail(aw, epoch);
  } else {
    if (!helper) __builtin_amdgcn_s_setprio(3);
    const int po = ch * 128 + hw * 64 + lane;
    const u32 pLd = LD16(log_dt, ch), pAr = LD16(A_re, po), pAi = LD16(A_im, po);
    const u32 pBr = LD16(B_re, po), pBi = LD16(B_im, po), pCr = LD16(C_re, po), pCi = LD16(C_im, po);
    const u32 pB1 = LD16(b1, (helper ? 0 : nt * 128) + (tid & 127));
    const int uks = 2 * (helper ? 0 : idx) + (w >> 1), upp = w & 1;
    v4u w2a, w2b;
    {
      const GAS v4u* src = (const GAS v4u*)(w2 + (uks * 32 + (lane >> 2)) * 512 + xcd * 64 + upp * 32 + (lane & 3) * 8);
      asm volatile("" : "+v"(src));
      w2a = src[0];
      w2b = src[16 * 64];
    }
    if (lane == 0) *(LAS u32*)(L + LD_PS + wv * 4) = 0u;
    SCHED();
    TS(1);
    const int mi = w >> 1, ni = w & 1;
    f32x4 acc[4][4], csum[4];
#pragma unroll
    for (int i = 0; i < 4; ++i) {
#pragma unroll
      for (int j = 0; j < 4; ++j) { acc[i][j] = f32x4{0.f, 0.f, 0.f, 0.f}; asm volatile("" : "+v"(acc[i][j])); }
      csum[i] = f32x4{0.f, 0.f, 0.f, 0.f};
      asm volatile("" : "+v"(csum[i]));
    }
    u32 xb[2][2], wb[2][4][2];
#pragma unroll
    for (int h = 0; h < 2; ++h) {
#pragma unroll
      for (int k = 0; k < 2; ++k) {
        xb[h][k] = (u32)(((64 * mi + r16) * 128 + ((q ^ (r16 & 7)) << 4)) ^ (k * 64)) + h * 65536;
        asm volatile("" : "+v"(xb[h][k]));
      }
#pragma unroll
      for (int j = 0; j < 4; ++j)
#pragma unroll
        for (int t = 0; t < 2; ++t) {
          wb[h][j][t] = (u32)(16384 + (8 * q + 4 * t + qq) * 256 + (((2 * (4 * ni + j) + (p4 >> 1)) ^ ((qq << 2) | ((2 * q + t) & 3))) << 4) + 8 * (p4 & 1)) + h * 65536;
          asm volatile("" : "+v"(wb[h][j][t]));
        }
    }
    v4u onev = ones;
    asm volatile("" : "+v"(onev));
    SCHED();
    TC(0);
    u32 epoch;
    asm volatile("s_waitcnt lgkmcnt(0)\n\ts_add_u32 %0, %1, 1" : "=s"(epoch) : "s"(eraw) : "memory", "scc");
    SCHED();
    {
      const float dt = bfr(__builtin_amdgcn_exp2f(bf2f(pLd) * 1.4426950408889634f));
      const float la = dt * bf2f(pAr) * 1.4426950408889634f;
      const float th = dt * bf2f(pAi) * 0.15915494309189535f;
      const f32x2 dBc = {dt * bf2f(pBr), -dt * bf2f(pBi)};
      const f32x2 Ccc = {bf2f(pCr), -bf2f(pCi)};
      const f32x2 z8 = zpow(la, th, 8.f), z16 = zpow(la, th, 16.f);
      const f32x2 y1 = zpow(la, -th, 1.f), y256 = zpow(la, -th, 256.f), y8 = {z8.x, -z8.y};
      const f32x2 y2 = cmul(y1, y1), y4 = cmul(y2, y2);
#define PROW(h, g) (((g) & 3) + 8 * ((g) >> 2) + 4 * (h))
#define PW(h, g, v) *(LAS u32*)(PT + (lane >> 2) * 464 + (lane & 3) * 4 + PROW(h, g) * 16) = pk2v(v)
      f32x2 W[8];
      W[0] = cmul(Ccc, dBc);
#pragma unroll
      for (int c = 1; c < 8; ++c) W[c] = cmul(W[c - 1], y256);
      {
        f32x2 te = W[0], to = cmul(W[0], y1), t1 = W[1];
        PW(0, 0, te); PW(0, 1, to); PW(1, 0, t1);
#pragma unroll
        for (int a = 1; a <= 4; ++a) {
          te = cmul(te, y2); PW(0, 2 * a, te);
          if (a < 4) { to = cmul(to, y2); PW(0, 2 * a + 1, to); }
          t1 = cmul(t1, y2); PW(1, a, t1);
        }
      }
      PW(1, 5, W[2]); PW(1, 6, cmul(W[2], y4)); PW(1, 7, cmul(W[2], y8));
      PW(1, 8, W[3]); PW(1, 9, cmul(W[3], y4)); PW(1, 10, cmul(W[3], y8));
      PW(0, 9, W[4]); PW(0, 10, cmul(W[4], y8));
      PW(0, 11, W[5]); PW(0, 12, cmul(W[5], y8));
      PW(0, 13, W[6]); PW(0, 14, cmul(W[6], y8));
      PW(1, 11, W[7]); PW(1, 12, cmul(W[7], y8));
      SCHED();
      TC(2);
      LAS char* const QA = L + LD_Q + w * 8192;
      *(LAS v4u*)(QA + lane * 16) = w2a;
      *(LAS v4u*)(QA + 1024 + lane * 16) = w2b;
      v2u wt[2][2];
#pragma unroll
      for (int e = 0; e < 2; ++e)
#pragma unroll
        for (int t = 0; t < 2; ++t) wt[e][t] = trread(QA + (16 * t + 4 * q + qq) * 64 + 16 * p4 + 8 * e);
      asm volatile("" ::: "memory");
      TC(3);
      f32x2 qa = {1.f, 0.f}, qb = z8;
#pragma unroll
      for (int i = 0; i < 16; ++i) {
        TW(QA + 2 * i * 256, 2 * i, pk2v(qa));
        TW(QA + (2 * i + 1) * 256, 2 * i + 1, pk2v(qb));
        if (i < 15) { qa = (i == 0) ? z16 : cmul(qa, z16); qb = cmul(qb, z16); }
      }
#pragma unroll
      for (int e = 0; e < 2; ++e)
        __builtin_amdgcn_raw_buffer_store_b128(v4u{wt[e][0].x, wt[e][0].y, wt[e][1].x, wt[e][1].y}, rsWS,
                                               lane * 16 + (helper ? WS_END : WS_W2T) + ((xcd * 56 + uks) * 4 + 2 * upp + e) * 1024, 0, SC1);
    }
    *(LAS float*)(L + LD_B1 + (tid & 127) * 4) = bf2f(pB1);
    TC(4);
    TS(2);
    v4u aw[7][4];
    if (!helper) {
      v4u xf[2][4], wf[2][4];
#define G1_READ(n)                                                                                            \
  _Pragma("unroll") for (int i = 0; i < 4; ++i) {                                                             \
    const v2u lo = trread(L + (((n) >> 1) & 1) * 32768 + ((n) & 1) * 8192 + wb[((n) >> 2) & 1][i][0]);        \
    const v2u hi = trread(L + (((n) >> 1) & 1) * 32768 + ((n) & 1) * 8192 + wb[((n) >> 2) & 1][i][1]);        \
    wf[(n) & 1][i] = v4u{lo.x, lo.y, hi.x, hi.y};                                                             \
    xf[(n) & 1][i] = *(LAS v4u*)(L + (((n) >> 1) & 1) * 32768 + i * 2048 + xb[((n) >> 2) & 1][(n) & 1]);      \
  }
      __builtin_amdgcn_s_setprio(0);
      TC(5);
      BAR();
      TC(6);
      TS(3);
      G1_READ(0)
      SCHED();
#pragma unroll 1
      for (int it = 0; it < 2; ++it) {
#pragma unroll
        for (int n = 0; n < 8; ++n) {
          if (n & 1) {
            if (n < 4 || it == 0) BAR();
            if (n == 5 && it == 0) {
              __builtin_amdgcn_s_setprio(3);
              publish(ctl + WS_FW2 / 4 + ((xcd * 56 + uks) * 2 + upp) * FL, epoch, lane);
              __builtin_amdgcn_s_setprio(0);
            }
            SCHED();
          }
          G1_READ(n + 1)
#pragma unroll
          for (int j = 0; j < 4; ++j) {
#pragma unroll
            for (int i = 0; i < 4; ++i) acc[j][i] = mfma16(wf[n & 1][j], xf[n & 1][i], acc[j][i]);
            csum[j] = mfma16(wf[n & 1][j], onev, csum[j]);
          }
#pragma unroll
          for (int k = 0; k < 12; ++k) {
            __builtin_amdgcn_sched_group_barrier(0x008, 1, 0);
            __builtin_amdgcn_sched_group_barrier(0x100, 1, 0);
          }
          __builtin_amdgcn_sched_group_barrier(0x008, 8, 0);
          SCHED();
        }
      }
      TS(4);
      __builtin_amdgcn_s_setprio(3);
      wait_progress(L, 4 + (lane & 3), 1u);
      TS(5);
      float rs[4], rm[4];
      f32x4 bb[4];
#pragma unroll
      for (int i = 0; i < 4; ++i) {
        const f32x2 st = *(LAS f32x2*)(L + LD_STAT + (64 * mi + 16 * i + r16) * 8);
        rs[i] = st.x; rm[i] = -st.y;
        asm volatile("" : "+v"(rm[i]));
        bb[i] = *(LAS f32x4*)(L + LD_B1 + (64 * ni + 16 * i + 4 * q) * 4);
      }
      SCHED();
#pragma unroll
      for (int jp = 0; jp < 2; ++jp) {
#pragma unroll
        for (int i = 0; i < 4; ++i) {
          TC(8 + jp * 4 + i);
          f32x2 pre[4], ee[4];
#pragma unroll
          for (int k = 0; k < 4; ++k) {
            const f32x4 a4 = acc[2 * jp + (k >> 1)][i], c4 = csum[2 * jp + (k >> 1)], b4 = bb[2 * jp + (k >> 1)];
            const f32x2 a2 = (k & 1) ? f32x2{a4.z, a4.w} : f32x2{a4.x, a4.y};
            const f32x2 c2 = (k & 1) ? f32x2{c4.z, c4.w} : f32x2{c4.x, c4.y};
            const f32x2 b2v = (k & 1) ? f32x2{b4.z, b4.w} : f32x2{b4.x, b4.y};
            pre[k] = a2 * rs[i] + (c2 * rm[i] + b2v);
            f32x2 arg = pre[k] * (pre[k] * pre[k] * -0.1029432397f + -2.302208198f);
            asm("" : "+v"(arg));
            ee[k] = f32x2{__builtin_amdgcn_exp2f(arg.x), __builtin_amdgcn_exp2f(arg.y)};
          }
          u32 o[4];
#pragma unroll
          for (int k = 0; k < 4; ++k) {
            f32x2 dn = ee[k] + 1.0f;
            asm("" : "+v"(dn));
            o[k] = pk2v(pre[k] * f32x2{__builtin_amdgcn_rcpf(dn.x), __builtin_amdgcn_rcpf(dn.y)});
          }
          __builtin_amdgcn_raw_buffer_store_b128(v4u{o[0], o[1], o[2], o[3]}, rsWS,
                                                 lane * 16 + ABL_HT + (((2 * mt + mi) * 56 + nt * 4 + 2 * ni + jp) * 4 + i) * 1024, 0, SC1);
          SCHED();
        }
      }
      set_progress(L, wv, 4u);
      TS(6);
#pragma unroll
      for (int c = 0; c < 12; ++c) AW_LOAD(c >> 2, c & 3);
      SCHED();
      WAITV(12);
      if (lane == 0) __hip_atomic_store(ctl + WS_FH / 4 + ((2 * mt + mi) * 32 + 2 * nt + ni) * FL, epoch, RLX_AGENT);
      SCHED();
      TS(7);
#pragma unroll
      for (int c = 12; c < 28; ++c) AW_LOAD(c >> 2, c & 3);
      SCHED();
      __builtin_amdgcn_s_setprio(0);
    } else {
      __builtin_amdgcn_s_setprio(0);
      BAR(); BAR(); BAR();
      set_progress(L, wv, 4u);
      wait_progress(L, 4 + (lane & 3), 1u);
#pragma unroll
      for (int s = 0; s < 7; ++s)
#pragma unroll
        for (int c = 0; c < 4; ++c) AW_LOAD(s, c);
      SCHED();
    }
    TS(8);
    gemm2_tail(aw, epoch);
  }
}

extern "C" void kernel_launch(void* const* d_in, const int* in_sizes, int n_in, void* d_out, int out_size, void* d_ws,
                              size_t ws_size, hipStream_t stream) {
  (void)in_sizes; (void)n_in; (void)out_size; (void)ws_size;
  (void)hipFuncSetAttribute((const void*)s4_mixer_kernel, hipFuncAttributeMaxDynamicSharedMemorySize, LDS_BYTES);
  s4_mixer_kernel<<<256, 512, LDS_BYTES, stream>>>(
      (const u16*)d_in[0], 2097152u, 0x00020000u, (const u16*)d_in[10], 1835008u, 0x00020000u, (const u16*)d_in[12], (char*)d_ws, (const u16*)d_in[1], (const u16*)d_in[2],
      (const u16*)d_in[3], (const u16*)d_in[4], (const u16*)d_in[5], (const u16*)d_in[6], (const u16*)d_in[7],
      (const u16*)d_in[8], (const u16*)d_in[11], (const u16*)d_in[13], (u16*)d_out);
}
```

```cpp
#include <hip/hip_runtime.h>
#include <hip/hip_bf16.h>

typedef unsigned int u32;
typedef unsigned short u16;
typedef u32 v4u __attribute__((ext_vector_type(4)));
typedef u32 v2u __attribute__((ext_vector_type(2)));
typedef float f32x2 __attribute__((ext_vector_type(2)));
typedef float f32x4 __attribute__((ext_vector_type(4)));
typedef float f32x16 __attribute__((ext_vector_type(16)));
typedef short s16x4 __attribute__((ext_vector_type(4)));
typedef short s16x8 __attribute__((ext_vector_type(8)));
typedef __bf16 bf16x2 __attribute__((ext_vector_type(2)));
#define GAS __attribute__((address_space(1)))
#define LAS __attribute__((address_space(3)))
#define DEV __device__ __forceinline__
#define RLX_AGENT __ATOMIC_RELAXED, __HIP_MEMORY_SCOPE_AGENT
#define SC1 16
#define TS(i) do { } while (0)
#define TC(i) do { } while (0)

constexpr int WS_E = 0;
constexpr int FL = 32;
constexpr int WS_STAT = 4096;
constexpr int WS_FXN = 65536;
constexpr int WS_FW2 = WS_FXN + 256 * 128;
constexpr int WS_FH = WS_FW2 + 1024 * 128;
constexpr int WS_FS = WS_FH + 1024 * 128;
constexpr int WS_XNT = 524288;
constexpr int WS_W2T = WS_XNT + 2097152;
constexpr int WS_HT = WS_W2T + 1835008;
constexpr int WS_ST = WS_HT + 7340032;
constexpr int WS_END = WS_ST + 2097152;
constexpr int LD_Q = 98304;
constexpr int LD_KREG = 131072;
constexpr int LD_PS = LD_KREG + 7424;
constexpr int LD_DUMMY = LD_KREG + 7680;
constexpr int LD_B1 = LD_KREG + 8192 + 7424;
constexpr int LD_HST = LD_KREG + 3 * 8192 + 7424;
constexpr int LD_STAT = 12288;
constexpr int LD_U = 0;
constexpr int LD_CX = 16384;
constexpr int LD_HX = 32768;
constexpr int LD_RED = 0;
constexpr int LD_ST = 114688;
constexpr int ABL_HT = WS_HT;
constexpr int LDS_BYTES = 163840;
constexpr u32 SPIN_CAP = 1u << 16;
constexpr u32 CODE_BYTES = $$$$$$$$$$$$$$$$$$$$$$$$$$$$$$$$$$$$$$$$$$$$$$;

DEV float bf2f(u32 h) { return __builtin_bit_cast(float, h << 16); }
DEV float bflo(u32 d) { return __builtin_bit_cast(float, d << 16); }
DEV float bfhi(u32 d) { return __builtin_bit_cast(float, d & 0xffff0000u); }
DEV u32 pk2(float lo, float hi) {
  f32x2 v = {lo, hi};
  return __builtin_bit_cast(u32, __builtin_convertvector(v, bf16x2));
}
DEV u32 pk2v(f32x2 v) { return __builtin_bit_cast(u32, __builtin_convertvector(v, bf16x2)); }
DEV float bfr(float v) { return bflo(pk2(v, v)); }
DEV f32x4 mfma16(v4u a, v4u b, f32x4 c) {
  return __builtin_amdgcn_mfma_f32_16x16x32_bf16(__builtin_bit_cast(s16x8, a), __builtin_bit_cast(s16x8, b), c, 0, 0, 0);
}
DEV f32x16 mfma32(v4u a, v4u b, f32x16 c) {
  return __builtin_amdgcn_mfma_f32_32x32x16_bf16(__builtin_bit_cast(s16x8, a), __builtin_bit_cast(s16x8, b), c, 0, 0, 0);
}
DEV v2u trread(LAS char* p) { return __builtin_bit_cast(v2u, __builtin_amdgcn_ds_read_tr16_b64_v4i16((LAS s16x4*)p)); }
DEV v4u mk_rsrc(const void* p, u32 bytes, u32 fmt) {
  const unsigned long long a = (unsigned long long)p;
  v4u r = {(u32)a, (u32)(a >> 32), bytes, fmt};
  return r;
}
DEV void dma16(u32 voff, v4u rs, u32 soff, u32 lds) {
  asm volatile("s_mov_b32 m0, %0\n\ts_nop 0\n\tbuffer_load_dwordx4 %1, %2, %3 offen lds" ::"s"(lds), "v"(voff), "s"(rs), "s"(soff) : "memory");
}
#define WAITV(n) asm volatile("s_waitcnt vmcnt(" #n ")" ::: "memory")
#define WAITL() asm volatile("s_waitcnt lgkmcnt(0)" ::: "memory")
#define SCHED() __builtin_amdgcn_sched_barrier(0)
#define BAR() do { asm volatile("s_waitcnt lgkmcnt(0)" ::: "memory"); __builtin_amdgcn_s_barrier(); asm volatile("" ::: "memory"); SCHED(); } while (0)
#define ORDER_BEHIND_POLL() __builtin_amdgcn_fence(__ATOMIC_ACQUIRE, "wavefront")

DEV f32x2 cmul(f32x2 a, f32x2 z) {
  const f32x2 zr = {-z.y, z.x};
  f32x2 t, r;
  asm("v_pk_mul_f32 %0, %1, %2 op_sel:[1,0] op_sel_hi:[1,1]" : "=v"(t) : "v"(a), "v"(zr));
  asm("v_pk_fma_f32 %0, %1, %2, %3 op_sel_hi:[0,1,1]" : "=v"(r) : "v"(a), "v"(z), "v"(t));
  return r;
}
DEV float sadd(float a, float b) { float r; asm("v_add_f32 %0, %1, %2" : "=v"(r) : "v"(a), "v"(b)); return r; }
DEV float smul(float a, float b) { float r; asm("v_mul_f32 %0, %1, %2" : "=v"(r) : "v"(a), "v"(b)); return r; }
DEV f32x2 zpow(float la, float th, float k) {
  const float mag = __builtin_amdgcn_exp2f(la * k);
  const float fr = __builtin_amdgcn_fractf(th * k);
  f32x2 r = {mag * __builtin_amdgcn_cosf(fr), mag * __builtin_amdgcn_sinf(fr)};
  return r;
}
template <int SLEEP>
DEV void poll_flags(GAS u32* p, u32 epoch, u32 first) {
  if (!__all(first == epoch)) {
#pragma unroll 1
    for (u32 spins = 0; spins < SPIN_CAP; ++spins) {
      const u32 v = __hip_atomic_load(p, RLX_AGENT);
      if (__all(v == epoch)) break;
      __builtin_amdgcn_s_sleep(SLEEP);
    }
  }
  ORDER_BEHIND_POLL();
}
DEV void publish(GAS u32* p, u32 epoch, int lane) {
  WAITV(0);
  if (lane == 0) __hip_atomic_store(p, epoch, RLX_AGENT);
}
DEV void set_progress(LAS char* L, int wv, u32 k) {
  WAITL();
  *(volatile LAS u32*)(L + LD_PS + wv * 4) = k;
}
DEV void wait_progress(LAS char* L, int word, u32 k) {
#pragma unroll 1
  for (u32 spins = 0; spins < (1u << 22); ++spins) {
    const u32 pv = *(volatile LAS u32*)(L + LD_PS + word * 4);
    if (__all(pv >= k)) break;
    __builtin_amdgcn_s_sleep(1);
  }
  asm volatile("" ::: "memory");
}
DEV void ln_stats(float s1, float s2, float& mean, float& var, float& den1) {
  mean = s1 * (1.0f / 512.0f);
  var = fmaxf(s2 * (1.0f / 512.0f) - mean * mean, 0.0f);
  den1 = bfr(__builtin_amdgcn_sqrtf(bfr(bfr(var) + 1e-5f)));
}
DEV float pick4(f32x4 g, int k) { return k == 0 ? g.x : k == 1 ? g.y : k == 2 ? g.z : g.w; }
#define LD16(p, i) ((u32)__hip_atomic_load((const GAS u16*)(p) + (i), __ATOMIC_RELAXED, __HIP_MEMORY_SCOPE_WAVEFRONT))

__global__ void __launch_bounds__(512, 2)
s4_mixer_kernel(const u16* __restrict__ x, u32 x_bytes, u32 x_fmt, const u16* __restrict__ w1, u32 w1_bytes, u32 w1_fmt,
                const u16* __restrict__ w2, char* __restrict__ ws,
                const u16* __restrict__ A_re, const u16* __restrict__ A_im, const u16* __restrict__ B_re,
                const u16* __restrict__ B_im, const u16* __restrict__ C_re, const u16* __restrict__ C_im,
                const u16* __restrict__ log_dt, const u16* __restrict__ Dskip, const u16* __restrict__ b1,
                const u16* __restrict__ b2, u16* __restrict__ out) {
  extern __shared__ __attribute__((aligned(16))) char smem[];
  LAS char* const L = (LAS char*)smem;
  unsigned long long pc;
  asm volatile("s_getpc_b64 %0" : "=s"(pc));
  {
    u32 coff = ((((threadIdx.x & 63) << 3) | (threadIdx.x >> 6)) * 32u + (blockIdx.x >> 3)) * 64u;
    coff = coff < CODE_BYTES ? coff : 0u;
    asm volatile("s_mov_b32 m0, %0\n\ts_nop 0\n\tglobal_load_lds_dword %1, %2" ::"s"((u32)LD_DUMMY), "v"(coff), "s"(pc) : "memory");
    SCHED();
  }
  const int tid = threadIdx.x, lane = tid & 63;
  const int wv = __builtin_amdgcn_readfirstlane(tid >> 6);
  const int w = wv & 3;
  const bool swave = wv >= 4;
  const int b = blockIdx.x, xcd = b & 7, idx = b >> 3;
  const int r16 = lane & 15, q = lane >> 4, qq = r16 >> 2, p4 = lane & 3;
  const bool helper = idx >= 28;
  TS(0);
  const v4u rsX = mk_rsrc(x, x_bytes, x_fmt);
  const v4u rsW1 = mk_rsrc(w1, w1_bytes, w1_fmt);
  asm volatile("" ::"s"(rsX), "s"(rsW1), "s"(w2), "s"(ws), "s"(A_re), "s"(A_im), "s"(B_re), "s"(B_im), "s"(C_re), "s"(C_im), "s"(log_dt), "s"(Dskip), "s"(b1));
  u32 eraw;
  asm volatile("s_load_dword %0, %1, 0x0" : "=s"(eraw) : "s"(ws));
  const int mq = (idx * 37) >> 8, mr = idx - 7 * mq;
  const int mt = (xcd >> 1) * 4 + mq, nt = (xcd & 1) * 7 + mr;
  const int hid = xcd * 4 + (idx - 28);
  const int tg = (xcd >> 1) * 8 + (idx >> 2), cg = (xcd & 1) * 4 + (idx & 3);
  const int cw = w >> 1, hw = w & 1;
  const int ch = 64 * xcd + 2 * idx + cw;
  const __amdgpu_buffer_rsrc_t rsWS = __builtin_amdgcn_make_buffer_rsrc((void*)ws, 0, WS_END, 0x00020000);
  GAS u32* const ctl = (GAS u32*)ws;
  const v4u ones = {0x3f803f80u, 0x3f803f80u, 0x3f803f80u, 0x3f803f80u};
  int rot2 = (tg & 7) + 2 * (cg & 3);
  rot2 -= rot2 >= 7 ? 7 : 0;
#define KS2(s) (7 * wv + ((s) + rot2 >= 7 ? (s) + rot2 - 7 : (s) + rot2))
#define AW_LOAD(s, c) \
  aw[s][c] = __builtin_amdgcn_raw_buffer_load_b128(rsWS, lane * 16 + ((((c) + 2 * (wv & 1)) & 3) << 10), WS_W2T + (cg * 56 + KS2(s)) * 4096, SC1)
  const u32 wo = (u32)lane * 4u;
#define TW(rowp, row, val) *(LAS u32*)((rowp) + (wo ^ (u32)(((row) & 15) << 4))) = (val)
  LAS char* const PT = L + LD_KREG + w * 8192;

  auto gemm2_tail = [&](v4u (&aw)[7][4], const u32 epoch, GAS u32* const pend, u32 fh0) __attribute__((always_inline)) {
  {
    TS(9);
    int tz = threadIdx.x;
    asm volatile("" : "+v"(tz));
    const int lane = tz & 63, r16 = lane & 15, q = lane >> 4, qq = r16 >> 2, p4 = lane & 3;
    asm volatile("" : "+v"(fh0));
    poll_flags<2>(ctl + WS_FH / 4 + (tg * 32 + ((7 * wv) >> 1) + (lane & 3)) * FL, epoch, fh0);
    TS(10);
    constexpr int WIN = 4;
    v4u hf[7][4];
#define H_LOAD(s) _Pragma("unroll") for (int j = 0; j < 4; ++j) \
      hf[s][j] = __builtin_amdgcn_raw_buffer_load_b128(rsWS, lane * 16 + (((j + (wv >> 1)) & 3) << 10), WS_HT + (tg * 56 + KS2(s)) * 4096, SC1);
#pragma unroll
    for (int s = 0; s < WIN; ++s) { H_LOAD(s) SCHED(); }
    if (pend) {
      WAITV(16);
      if (lane == 0) __hip_atomic_store(pend, epoch, RLX_AGENT);
      SCHED();
    }
    GAS u32* const fsp = ctl + WS_FS / 4 + ((tg >> 4) * 512 + cg * 64 + wv * 8 + (lane >> 3)) * FL;
    const int tok = tg * 64 + 16 * (wv >> 1) + r16;
    const int c8 = cg * 64 + 32 * (wv & 1) + 8 * q;
    u32 fs, fe0 = epoch, fe1 = epoch;
    bool early;
    v4u xv, bv, sv;
    f32x4 acc[4][4];
#pragma unroll
    for (int i = 0; i < 4; ++i)
#pragma unroll
      for (int j = 0; j < 4; ++j) acc[i][j] = f32x4{0.f, 0.f, 0.f, 0.f};
#pragma unroll
    for (int s = 0; s < 7; ++s) {
#pragma unroll
      for (int j = 0; j < 4; ++j)
#pragma unroll
        for (int c = 0; c < 4; ++c) acc[c][j] = mfma16(aw[s][c], hf[s][j], acc[c][j]);
      SCHED();
      if (s == 2) fs = __hip_atomic_load(fsp, RLX_AGENT);
      if (s + WIN < 7) { H_LOAD(s + WIN) }
      if (s == 2) {
        xv = *(const GAS v4u*)(x + tok * 512 + c8);
        bv = *(const GAS v4u*)(b2 + c8);
      }
      if (s == 5) {
        early = __all(fs == epoch);
        ORDER_BEHIND_POLL();
        sv = __builtin_amdgcn_raw_buffer_load_b128(rsWS, (lane >> 3) * 4096 + (lane & 7) * 16, early ? WS_ST + (cg * 64 + wv * 8) * 4096 + tg * 128 : WS_END, SC1);
        if (b == 0) {
          fe0 = __hip_atomic_load(ctl + WS_FS / 4 + tz * FL, RLX_AGENT);
          fe1 = __hip_atomic_load(ctl + WS_FS / 4 + (512 + tz) * FL, RLX_AGENT);
        }
      }
      SCHED();
    }
    TS(11);
    wait_progress(L, lane & 7, 4u);
#pragma unroll
    for (int j = 0; j < 4; ++j)
#pragma unroll
      for (int cp = 0; cp < 2; ++cp)
        if (j + cp > 0) {
          const int d = (((j + (wv >> 1)) & 3) << 1) | ((cp + wv) & 1);
#pragma unroll
          for (int e = 0; e < 2; ++e)
            *(LAS f32x4*)(L + LD_RED + (d * 7 + 2 * j + cp - 1) * 2048 + e * 1024 + lane * 16) = acc[2 * cp + e][j];
        }
    if (!early) {
      poll_flags<2>(fsp, epoch, epoch + 1u);
      sv = __builtin_amdgcn_raw_buffer_load_b128(rsWS, (lane >> 3) * 4096 + (lane & 7) * 16, WS_ST + (cg * 64 + wv * 8) * 4096 + tg * 128, SC1);
    }
    *(LAS v4u*)(L + LD_ST + wv * 1024 + lane * 16) = sv;
    TS(12);
    asm volatile("" : "+v"(fe0), "+v"(fe1));
    poll_flags<2>(ctl + WS_FS / 4 + tz * FL, epoch, fe0);
    poll_flags<2>(ctl + WS_FS / 4 + (512 + tz) * FL, epoch, fe1);
    BAR();
    if (b == 0 && tz == 0) __hip_atomic_store(ctl + WS_E / 4, epoch, RLX_AGENT);
    TS(13);
    f32x4 part[2][7];
    v2u sq[2];
#pragma unroll
    for (int e = 0; e < 2; ++e) {
#pragma unroll
      for (int j = 0; j < 7; ++j) part[e][j] = *(LAS f32x4*)(L + LD_RED + (wv * 7 + j) * 2048 + e * 1024 + lane * 16);
      sq[e] = trread(L + LD_ST + (32 * (wv & 1) + 8 * q + 4 * e + qq) * 128 + (16 * (wv >> 1) + 4 * p4) * 2);
    }
    SCHED();
    u32 o[4];
#pragma unroll
    for (int e = 0; e < 2; ++e) {
      const u32 ba = bv[2 * e], bb = bv[2 * e + 1], xa = xv[2 * e], xb = xv[2 * e + 1];
      f32x4 m = acc[e][0] + f32x4{bflo(ba), bfhi(ba), bflo(bb), bfhi(bb)};
#pragma unroll
      for (int j = 0; j < 7; ++j) m += part[e][j];
      f32x4 g;
#pragma unroll
      for (int k = 0; k < 4; ++k) g[k] = m[k] * __builtin_amdgcn_rcpf(1.0f + __builtin_amdgcn_exp2f(m[k] * -1.4426950408889634f));
      o[2 * e] = pk2(bflo(xa) + bflo(sq[e].x) * g.x, bfhi(xa) + bfhi(sq[e].x) * g.y);
      o[2 * e + 1] = pk2(bflo(xb) + bflo(sq[e].y) * g.z, bfhi(xb) + bfhi(sq[e].y) * g.w);
    }
    *(GAS v4u*)(out + tok * 512 + c8) = v4u{o[0], o[1], o[2], o[3]};
  }
  TS(14);
  __builtin_amdgcn_endpgm();
  };

  if (helper) {
    LAS char* const HX = L + LD_HX + wv * 8192;
#pragma unroll
    for (int p = 0; p < 8; ++p)
      dma16((u32)(wv * 8192 + ((lane ^ (((p & 3) << 2) | (p >> 2))) << 4)), rsX, (u32)((hid * 64 + p) * 1024), (u32)(LD_HX + (wv * 8 + p) * 1024));
    u32 epoch;
    asm volatile("s_waitcnt vmcnt(0) lgkmcnt(0)\n\ts_add_u32 %0, %1, 1" : "=s"(epoch) : "s"(eraw) : "memory", "scc");
    if (!swave) TS(3);
    f32x4 gram[2], sumx[2];
#pragma unroll
    for (int i = 0; i < 2; ++i) { gram[i] = f32x4{0.f, 0.f, 0.f, 0.f}; sumx[i] = gram[i]; }
    const int r8 = lane & 7, fr = ((r8 & 3) << 2) | (r8 >> 2);
    v4u xr[8];
#pragma unroll
    for (int s = 0; s < 8; ++s) xr[s] = *(LAS v4u*)(HX + r8 * 1024 + (lane & 8) * 64 + (((4 * s + q) ^ fr) << 4));
    v2u d[8][2];
#pragma unroll
    for (int s = 0; s < 8; ++s)
#pragma unroll
      for (int t = 0; t < 2; ++t)
        d[s][t] = trread(HX + (4 * t + qq) * 1024 + (((8 * s + 2 * q + (p4 >> 1)) ^ ((qq << 2) | t)) << 4) + 8 * (p4 & 1));
    SCHED();
#pragma unroll
    for (int s = 0; s < 8; ++s) {
      gram[s & 1] = mfma16(xr[s], xr[s], gram[s & 1]);
      sumx[s & 1] = mfma16(ones, xr[s], sumx[s & 1]);
    }
    float s1 = sumx[0].x + sumx[1].x, s2 = pick4(gram[0] + gram[1], r16 & 3);
    s1 += __shfl(s1, (lane + 40) & 63);
    s2 += __shfl(s2, (lane + 40) & 63);
    float mean, var, den1;
    ln_stats(s1, s2, mean, var, den1);
    const float rd = __builtin_amdgcn_rcpf(den1);
    const float den2 = bfr(__builtin_amdgcn_sqrtf(bfr(bfr(var * rd * rd) + 1e-5f)));
    const float R = __builtin_amdgcn_rcpf(den1 * den2);
    if (q == qq && r16 < 8) {
      *(LAS f32x2*)(L + LD_HST + (8 * wv + r16) * 8) = f32x2{rd, -bfr(mean) * rd};
      __builtin_amdgcn_raw_buffer_store_b128(v4u{__builtin_bit_cast(u32, R), epoch, __builtin_bit_cast(u32, R * mean), epoch}, rsWS,
                                             WS_STAT + (hid * 64 + 8 * wv + r16) * 16, 0, SC1);
    }
    WAITL();
    if (!swave) TS(4);
    f32x4 sv[4];
#pragma unroll
    for (int i = 0; i < 4; ++i) sv[i] = *(LAS f32x4*)(L + LD_HST + wv * 64 + i * 16);
#pragma unroll
    for (int cb = 0; cb < 8; ++cb) {
      v4u o;
      o.x = pk2(bflo(d[cb][0].x) * sv[0].x + sv[0].y, bfhi(d[cb][0].x) * sv[0].z + sv[0].w);
      o.y = pk2(bflo(d[cb][0].y) * sv[1].x + sv[1].y, bfhi(d[cb][0].y) * sv[1].z + sv[1].w);
      o.z = pk2(bflo(d[cb][1].x) * sv[2].x + sv[2].y, bfhi(d[cb][1].x) * sv[2].z + sv[2].w);
      o.w = pk2(bflo(d[cb][1].y) * sv[3].x + sv[3].y, bfhi(d[cb][1].y) * sv[3].z + sv[3].w);
      __builtin_amdgcn_raw_buffer_store_b128(o, rsWS, lane * 16 + WS_XNT + (hid * 8 + wv) * 8192 + cb * 1024, 0, SC1);
    }
    if (!swave) TS(5);
    WAITV(0);
    BAR();
    if (tid == 0) __hip_atomic_store(ctl + WS_FXN / 4 + hid * FL, epoch, RLX_AGENT);
    if (!swave) TS(6);
    SCHED();
  }

  if (swave) {
    __builtin_amdgcn_s_setprio(2);
    const u32 voX = (u32)((32 * w + (lane >> 3)) * 1024 + (((lane & 7) ^ (lane >> 3)) << 4));
    const u32 voW = (u32)((4 * w + q) * 3584 + ((r16 ^ ((q << 2) | w)) << 4));
    const int rot = (mr + 2 * mq) & 7;
#define G1_STAGE(st) G1_PART(st, 0, 4)
#define G1_PART(st, p0, p1)                                                                                  \
  {                                                                                                          \
    const int sa_ = ((st) + rot) & 7;                                                                        \
    _Pragma("unroll") for (int p = p0; p < p1; ++p) {                                                        \
      dma16(voX, rsX, (u32)(mt * 131072 + p * 8192 + sa_ * 128), (u32)(((st) & 3) * 32768 + w * 4096 + p * 1024)); \
      dma16(voW, rsW1, (u32)((sa_ * 64 + 16 * p) * 3584 + nt * 256), (u32)(((st) & 3) * 32768 + 16384 + p * 4096 + w * 1024)); \
    }                                                                                                        \
  }
    if (!helper) { G1_STAGE(0) G1_STAGE(1) G1_PART(2, 0, 2) }
    if (lane == 0) *(LAS u32*)(L + LD_PS + wv * 4) = 0u;
    TS(1);
    WAITV(12);
    u32 epoch;
    asm volatile("s_waitcnt lgkmcnt(0)\n\ts_add_u32 %0, %1, 1" : "=s"(epoch) : "s"(eraw) : "memory", "scc");
    TS(2);
    BAR();
    TS(3);
    TC(0);
    if (!helper) G1_PART(2, 2, 4)
    const int r32 = lane & 31, hh = lane >> 5;
    v4u pf[8], qf[8];
    {
#pragma unroll
      for (int s = 0; s < 8; ++s) {
        qf[s] = *(LAS v4u*)(L + LD_Q + w * 8192 + r32 * 256 + (((2 * s + hh) ^ (r32 & 15)) << 4));
        pf[s] = *(LAS v4u*)(PT + (2 * s + hh) * 464 + r32 * 16);
      }
    }
    SCHED();
    TC(1);
    if (!helper) {
      WAITV(8); BAR(); TC(2); G1_STAGE(3) G1_PART(4, 0, 2) TC(3);
      WAITV(12); BAR(); TC(4); G1_PART(4, 2, 4) G1_STAGE(5) TC(5);
      WAITV(16); BAR(); TC(6);
      G1_STAGE(6) TC(7);
      WAITV(16); BAR(); TC(8); G1_STAGE(7) TC(9);
      WAITV(16); BAR();
    }
    TC(10);
    GAS u32* const fxnp = ctl + WS_FXN / 4 + (hw * 16 + r16) * FL;
    GAS u32* const fw2p = ctl + WS_FW2 / 4 + ((cg * 56 + 7 * (w + 4 * ((lane >> 4) & 1))) * 2 + min(r16, 13)) * FL;
    const int sto = WS_STAT + ((helper ? 0 : mt) * 128 + 32 * w + r32) * 16;
    v4u sg = __builtin_amdgcn_raw_buffer_load_b128(rsWS, sto, 0, SC1);
    const u32 fw2 = __hip_atomic_load(fw2p, RLX_AGENT);
    const u32 pDs = LD16(Dskip, ch);
    SCHED();
    f32x16 kacc;
#pragma unroll
    for (int i = 0; i < 16; ++i) kacc[i] = 0.f;
#pragma unroll
    for (int s = 0; s < 8; ++s) kacc = mfma32(pf[s], qf[s], kacc);
    SCHED();
    TC(11);
    if (!helper) { WAITV(3); BAR(); }
    TC(12);
    TS(4);
#pragma unroll
    for (int i = 0; i < 4; ++i)
      *(LAS f32x4*)(L + LD_CX + w * 4096 + i * 1024 + lane * 16) = f32x4{kacc[4 * i], kacc[4 * i + 1], kacc[4 * i + 2], kacc[4 * i + 3]};
    if (!helper) {
      if (!__all(sg.y == epoch && sg.w == epoch)) {
#pragma unroll 1
        for (u32 spins = 0; spins < SPIN_CAP; ++spins) {
          __builtin_amdgcn_s_sleep(4);
          asm volatile("" ::: "memory");
          sg = __builtin_amdgcn_raw_buffer_load_b128(rsWS, sto, 0, SC1);
          if (__all(sg.y == epoch && sg.w == epoch)) break;
        }
      }
      *(LAS v2u*)(L + LD_STAT + (32 * w + r32) * 8) = v2u{sg.x, sg.z};
    }
    poll_flags<4>(fw2p, epoch, fw2);
    set_progress(L, wv, 1u);
    TS(5);
    u32 fxa = __hip_atomic_load(fxnp, RLX_AGENT);
    v4u aw[7][4];
#pragma unroll
    for (int c = 0; c < 12; ++c) AW_LOAD(c >> 2, c & 3);
    SCHED();
    wait_progress(L, wv ^ 1, 1u);
    {
      f32x4 v[4];
#pragma unroll
      for (int i = 0; i < 4; ++i) v[i] = *(LAS f32x4*)(L + LD_CX + (w ^ 1) * 4096 + i * 1024 + lane * 16);
#pragma unroll
      for (int i = 0; i < 15; ++i) {
        kacc[i] = sadd(kacc[i], v[i >> 2][i & 3]);
      }
    }
    {
      const bool odd = r32 & 1, up = hh != 0;
      float e[4][9];
      const int ia[4][9] = {{0, 1, 2, 3, 4, 5, 6, 7, 8}, {9, 9, 9, 9, -1, 10, 10, 10, 10}, {11, 11, 11, 11, -1, 12, 12, 12, 12}, {13, 13, 13, 13, -1, 14, 14, 14, 14}};
      const int ib[4][9] = {{0, 0, 1, 2, 2, 2, 3, 4, 4}, {5, 5, 5, 6, 6, 6, 7, 7, 7}, {8, 8, 8, 9, 9, 9, 10, 10, 10}, {11, 11, 11, 11, -1, 12, 12, 12, 12}};
#pragma unroll
      for (int u = 0; u < 4; ++u)
#pragma unroll
        for (int i = 0; i < 9; ++i) {
          const float va = ia[u][i] >= 0 ? kacc[ia[u][i]] : (odd ? kacc[ia[u][5]] : kacc[ia[u][3]]);
          const float vb = ib[u][i] >= 0 ? kacc[ib[u][i]] : (odd ? kacc[ib[u][5]] : kacc[ib[u][3]]);
          e[u][i] = up ? vb : va;
        }
      LAS char* const KC = PT + hw * 64 + (255 - r32) * 16;
#pragma unroll
      for (int u = 0; u < 4; ++u) {
        v4u d;
        if (hw) { d.x = pk2(e[u][8], e[u][7]); d.y = pk2(e[u][6], e[u][5]); d.z = pk2(e[u][4], e[u][3]); d.w = pk2(e[u][2], e[u][1]); }
        else    { d.x = pk2(e[u][7], e[u][6]); d.y = pk2(e[u][5], e[u][4]); d.z = pk2(e[u][3], e[u][2]); d.w = pk2(e[u][1], e[u][0]); }
        const int sa = u == 0 ? 0 : u + 3, sb = u == 3 ? 7 : u + 1;
        *(LAS v4u*)(KC - (up ? sb : sa) * 512) = d;
      }
      if (lane < 2) *(LAS v4u*)(PT + hw * 64 + 4096 + lane * 16) = v4u{(hw && lane == 0) ? pk2(kacc[0], 0.f) : 0u, 0u, 0u, 0u};
    }
    SCHED();
    TC(13);
    asm volatile("" : "+v"(fxa));
    poll_flags<8>(fxnp, epoch, fxa);
    const v4u u0 = __builtin_amdgcn_raw_buffer_load_b128(rsWS, lane * 8192, WS_XNT + (128 * hw) * 8192 + ch * 16, SC1);
    const v4u u1 = __builtin_amdgcn_raw_buffer_load_b128(rsWS, lane * 8192, WS_XNT + (128 * hw + 64) * 8192 + ch * 16, SC1);
#pragma unroll
    for (int c = 12; c < 20; ++c) AW_LOAD(c >> 2, c & 3);
    SCHED();
    LAS char* const UB = L + LD_U + cw * 4608;
    if (hw == 0 && lane < 32) *(LAS v4u*)(UB + lane * 16) = v4u{0u, 0u, 0u, 0u};
    *(LAS v4u*)(UB + 512 + (128 * hw + lane) * 16) = u0;
    *(LAS v4u*)(UB + 512 + (128 * hw + 64 + lane) * 16) = u1;
    set_progress(L, wv, 2u);
    SCHED();
#pragma unroll
    for (int c = 20; c < 28; ++c) AW_LOAD(c >> 2, c & 3);
    {
      const u32 po = (u32)(WS_W2T + (cg * 56 + 7 * w) * 4096 + ((tg & 7) * 28 + min(lane, 27)) * 128);
      asm volatile("s_mov_b32 m0, %0\n\ts_nop 0\n\tglobal_load_lds_dword %1, %2 sc1" ::"s"((u32)LD_DUMMY), "v"(po), "s"(ws) : "memory");
    }
    SCHED();
    TS(6);
    wait_progress(L, wv ^ 1, 2u);
    wait_progress(L, w, 4u);
    TS(7);
    TC(14);
    const float dskip = bf2f(pDs);
    const int sgm = 1 - (r16 & 1);
    LAS char* const ka = L + LD_KREG + (2 * cw + sgm) * 8192 + sgm * 64 + 2 * (2031 - r16 + 8 * q + sgm) - hw * 256 - 7 * 512 - 3 * 64;
    LAS char* const ub = UB + 2 * (256 - 16 + 16 * r16 + 8 * q) - hw * 256 - 3 * 64;
    f32x4 y[8];
#pragma unroll
    for (int i = 0; i < 8; ++i) y[i] = f32x4{0.f, 0.f, 0.f, 0.f};
    v4u af[4][8], bfv[4][8];
    u32 kb[4];
#pragma unroll
    for (int i = 0; i < 4; ++i) { kb[i] = (u32)(size_t)ka + i * 1024; asm volatile("" : "+v"(kb[i])); }
#define CV_READ(rr, g)                                                                                   \
  {                                                                                                      \
    LAS u32* pa = (LAS u32*)(size_t)(kb[(7 - (g)) >> 1] + ((7 - (g)) & 1) * 512 + (3 - (rr)) * 64);      \
    af[rr][g] = v4u{pa[0], pa[1], pa[2], pa[3]};                                                         \
    bfv[rr][g] = *(LAS v4u*)(ub + (g) * 512 + (3 - (rr)) * 64);                                          \
  }
    CV_READ(0, 0) CV_READ(0, 1) CV_READ(0, 2) CV_READ(0, 3) CV_READ(0, 4)
    SCHED();
#pragma unroll
    for (int rr = 0; rr < 4; ++rr) {
#pragma unroll
      for (int k = 0; k < 8; ++k) {
        if (k + 5 < 8) CV_READ(rr, k + 5)
        else if (rr < 3) CV_READ(rr + 1, k + 5 - 8)
#pragma unroll
        for (int o = 0; o <= k; ++o)
          if (k + o <= 7) {
            y[k + o] = mfma16(af[rr][k], bfv[rr][o], y[k + o]);
            if (o < k) y[k + o] = mfma16(af[rr][o], bfv[rr][k], y[k + o]);
          }
        SCHED();
      }
    }
    TC(15);
    const u32 fh0 = __hip_atomic_load(ctl + WS_FH / 4 + (tg * 32 + ((7 * wv) >> 1) + (lane & 3)) * FL, RLX_AGENT);
    SCHED();
    f32x4 mine[4];
    if (hw) {
#pragma unroll
      for (int i = 0; i < 4; ++i) { *(LAS f32x4*)(L + LD_CX + w * 4096 + i * 1024 + lane * 16) = y[i]; mine[i] = y[4 + i]; }
    } else {
#pragma unroll
      for (int i = 0; i < 4; ++i) { *(LAS f32x4*)(L + LD_CX + w * 4096 + i * 1024 + lane * 16) = y[4 + i]; mine[i] = y[i]; }
    }
    set_progress(L, wv, 3u);
    wait_progress(L, wv ^ 1, 3u);
    f32x4 oy[4];
    v2u xv[4];
#pragma unroll
    for (int i = 0; i < 4; ++i) {
      oy[i] = *(LAS f32x4*)(L + LD_CX + (w ^ 1) * 4096 + i * 1024 + lane * 16);
      xv[i] = *(LAS v2u*)(UB + 512 + (256 * (4 * hw + i) + 16 * r16 + 4 * q) * 2);
    }
    set_progress(L, wv, 4u);
#pragma unroll
    for (int i = 0; i < 4; ++i) {
      const u32 ya = pk2(sadd(mine[i].x, oy[i].x), sadd(mine[i].y, oy[i].y)), yb = pk2(sadd(mine[i].z, oy[i].z), sadd(mine[i].w, oy[i].w));
      const u32 da = pk2(smul(bflo(xv[i].x), dskip), smul(bfhi(xv[i].x), dskip)), db = pk2(smul(bflo(xv[i].y), dskip), smul(bfhi(xv[i].y), dskip));
      v2u o;
      o.x = pk2(sadd(bflo(ya), bflo(da)), sadd(bfhi(ya), bfhi(da)));
      o.y = pk2(sadd(bflo(yb), bflo(db)), sadd(bfhi(yb), bfhi(db)));
      __builtin_amdgcn_raw_buffer_store_b64(o, rsWS, (16 * r16 + 4 * q) * 2 + WS_ST + ch * 4096 + (4 * hw + i) * 512, 0, SC1);
    }
    TS(8);
    __builtin_amdgcn_s_setprio(0);
    gemm2_tail(aw, epoch, ctl + WS_FS / 4 + (hw * 512 + ch) * FL, fh0);
  } else {
    if (!helper) __builtin_amdgcn_s_setprio(3);
    const int po = ch * 128 + hw * 64 + lane;
    const u32 pLd = LD16(log_dt, ch), pAr = LD16(A_re, po), pAi = LD16(A_im, po);
    const u32 pBr = LD16(B_re, po), pBi = LD16(B_im, po), pCr = LD16(C_re, po), pCi = LD16(C_im, po);
    const u32 pB1 = LD16(b1, (helper ? 0 : nt * 128) + (tid & 127));
    const int uks = 2 * (helper ? 0 : idx) + (w >> 1), upp = w & 1;
    v4u w2a, w2b;
    {
      const GAS v4u* src = (const GAS v4u*)(w2 + (uks * 32 + (lane >> 2)) * 512 + xcd * 64 + upp * 32 + (lane & 3) * 8);
      asm volatile("" : "+v"(src));
      w2a = src[0];
      w2b = src[16 * 64];
    }
    if (lane == 0) *(LAS u32*)(L + LD_PS + wv * 4) = 0u;
    SCHED();
    TS(1);
    const int mi = w >> 1, ni = w & 1;
    f32x4 acc[4][4], csum[4];
#pragma unroll
    for (int i = 0; i < 4; ++i) {
#pragma unroll
      for (int j = 0; j < 4; ++j) { acc[i][j] = f32x4{0.f, 0.f, 0.f, 0.f}; asm volatile("" : "+v"(acc[i][j])); }
      csum[i] = f32x4{0.f, 0.f, 0.f, 0.f};
      asm volatile("" : "+v"(csum[i]));
    }
    u32 xb[2][2], wb[2][4][2];
#pragma unroll
    for (int h = 0; h < 2; ++h) {
#pragma unroll
      for (int k = 0; k < 2; ++k) {
        xb[h][k] = (u32)(((64 * mi + r16) * 128 + ((q ^ (r16 & 7)) << 4)) ^ (k * 64)) + h * 65536;
        asm volatile("" : "+v"(xb[h][k]));
      }
#pragma unroll
      for (int j = 0; j < 4; ++j)
#pragma unroll
        for (int t = 0; t < 2; ++t) {
          wb[h][j][t] = (u32)(16384 + (8 * q + 4 * t + qq) * 256 + (((2 * (4 * ni + j) + (p4 >> 1)) ^ ((qq << 2) | ((2 * q + t) & 3))) << 4) + 8 * (p4 & 1)) + h * 65536;
          asm volatile("" : "+v"(wb[h][j][t]));
        }
    }
    v4u onev = ones;
    asm volatile("" : "+v"(onev));
    SCHED();
    TC(0);
    u32 epoch;
    asm volatile("s_waitcnt lgkmcnt(0)\n\ts_add_u32 %0, %1, 1" : "=s"(epoch) : "s"(eraw) : "memory", "scc");
    SCHED();
    {
      const float dt = bfr(__builtin_amdgcn_exp2f(bf2f(pLd) * 1.4426950408889634f));
      const float la = dt * bf2f(pAr) * 1.4426950408889634f;
      const float th = dt * bf2f(pAi) * 0.15915494309189535f;
      const f32x2 dBc = {dt * bf2f(pBr), -dt * bf2f(pBi)};
      const f32x2 Ccc = {bf2f(pCr), -bf2f(pCi)};
      const f32x2 z8 = zpow(la, th, 8.f), z16 = zpow(la, th, 16.f);
      const f32x2 y1 = zpow(la, -th, 1.f), y256 = zpow(la, -th, 256.f), y8 = {z8.x, -z8.y};
      const f32x2 y2 = cmul(y1, y1), y4 = cmul(y2, y2);
#define PROW(h, g) (((g) & 3) + 8 * ((g) >> 2) + 4 * (h))
#define PW(h, g, v) *(LAS u32*)(PT + (lane >> 2) * 464 + (lane & 3) * 4 + PROW(h, g) * 16) = pk2v(v)
      f32x2 W[8];
      W[0] = cmul(Ccc, dBc);
#pragma unroll
      for (int c = 1; c < 8; ++c) W[c] = cmul(W[c - 1], y256);
      {
        f32x2 te = W[0], to = cmul(W[0], y1), t1 = W[1];
        PW(0, 0, te); PW(0, 1, to); PW(1, 0, t1);
#pragma unroll
        for (int a = 1; a <= 4; ++a) {
          te = cmul(te, y2); PW(0, 2 * a, te);
          if (a < 4) { to = cmul(to, y2); PW(0, 2 * a + 1, to); }
          t1 = cmul(t1, y2); PW(1, a, t1);
        }
      }
      PW(1, 5, W[2]); PW(1, 6, cmul(W[2], y4)); PW(1, 7, cmul(W[2], y8));
      PW(1, 8, W[3]); PW(1, 9, cmul(W[3], y4)); PW(1, 10, cmul(W[3], y8));
      PW(0, 9, W[4]); PW(0, 10, cmul(W[4], y8));
      PW(0, 11, W[5]); PW(0, 12, cmul(W[5], y8));
      PW(0, 13, W[6]); PW(0, 14, cmul(W[6], y8));
      PW(1, 11, W[7]); PW(1, 12, cmul(W[7], y8));
      SCHED();
      TC(2);
      LAS char* const QA = L + LD_Q + w * 8192;
      *(LAS v4u*)(QA + lane * 16) = w2a;
      *(LAS v4u*)(QA + 1024 + lane * 16) = w2b;
      v2u wt[2][2];
#pragma unroll
      for (int e = 0; e < 2; ++e)
#pragma unroll
        for (int t = 0; t < 2; ++t) wt[e][t] = trread(QA + (16 * t + 4 * q + qq) * 64 + 16 * p4 + 8 * e);
      asm volatile("" ::: "memory");
      TC(3);
      f32x2 qa = {1.f, 0.f}, qb = z8;
#pragma unroll
      for (int i = 0; i < 16; ++i) {
        TW(QA + 2 * i * 256, 2 * i, pk2v(qa));
        TW(QA + (2 * i + 1) * 256, 2 * i + 1, pk2v(qb));
        if (i < 15) { qa = (i == 0) ? z16 : cmul(qa, z16); qb = cmul(qb, z16); }
        if (i == 1) {
#pragma unroll
      for (int e = 0; e < 2; ++e)
        __builtin_amdgcn_raw_buffer_store_b128(v4u{wt[e][0].x, wt[e][0].y, wt[e][1].x, wt[e][1].y}, rsWS,
                                               lane * 16 + (helper ? WS_END : WS_W2T) + ((xcd * 56 + uks) * 4 + 2 * upp + e) * 1024, 0, SC1);
        }
      }
    }
    *(LAS float*)(L + LD_B1 + (tid & 127) * 4) = bf2f(pB1);
    TC(4);
    TS(2);
    v4u aw[7][4];
    if (!helper) {
      v4u xf[2][4], wf[2][4];
#define G1_READ(n)                                                                                            \
  _Pragma("unroll") for (int i = 0; i < 4; ++i) {                                                             \
    const v2u lo = trread(L + (((n) >> 1) & 1) * 32768 + ((n) & 1) * 8192 + wb[((n) >> 2) & 1][i][0]);        \
    const v2u hi = trread(L + (((n) >> 1) & 1) * 32768 + ((n) & 1) * 8192 + wb[((n) >> 2) & 1][i][1]);        \
    wf[(n) & 1][i] = v4u{lo.x, lo.y, hi.x, hi.y};                                                             \
    xf[(n) & 1][i] = *(LAS v4u*)(L + (((n) >> 1) & 1) * 32768 + i * 2048 + xb[((n) >> 2) & 1][(n) & 1]);      \
  }
      __builtin_amdgcn_s_setprio(0);
      TC(5);
      BAR();
      TC(6);
      TS(3);
      G1_READ(0)
      SCHED();
#pragma unroll 1
      for (int it = 0; it < 2; ++it) {
#pragma unroll
        for (int n = 0; n < 8; ++n) {
          if (n & 1) {
            if (n < 4 || it == 0) BAR();
            if (n == 3 && it == 0) {
              __builtin_amdgcn_s_setprio(3);
              publish(ctl + WS_FW2 / 4 + ((xcd * 56 + uks) * 2 + upp) * FL, epoch, lane);
              __builtin_amdgcn_s_setprio(0);
            }
            SCHED();
          }
          G1_READ(n + 1)
#pragma unroll
          for (int j = 0; j < 4; ++j) {
#pragma unroll
            for (int i = 0; i < 4; ++i) acc[j][i] = mfma16(wf[n & 1][j], xf[n & 1][i], acc[j][i]);
            csum[j] = mfma16(wf[n & 1][j], onev, csum[j]);
          }
#pragma unroll
          for (int k = 0; k < 12; ++k) {
            __builtin_amdgcn_sched_group_barrier(0x008, 1, 0);
            __builtin_amdgcn_sched_group_barrier(0x100, 1, 0);
          }
          __builtin_amdgcn_sched_group_barrier(0x008, 8, 0);
          SCHED();
        }
      }
      TS(4);
      __builtin_amdgcn_s_setprio(3);
      wait_progress(L, 4 + (lane & 3), 1u);
      TS(5);
      float rs[4], rm[4];
      f32x4 bb[4];
#pragma unroll
      for (int i = 0; i < 4; ++i) {
        const f32x2 st = *(LAS f32x2*)(L + LD_STAT + (64 * mi + 16 * i + r16) * 8);
        rs[i] = st.x; rm[i] = -st.y;
        asm volatile("" : "+v"(rm[i]));
        bb[i] = *(LAS f32x4*)(L + LD_B1 + (64 * ni + 16 * i + 4 * q) * 4);
      }
      SCHED();
#pragma unroll
      for (int jp = 0; jp < 2; ++jp) {
#pragma unroll
        for (int i = 0; i < 4; ++i) {
          TC(8 + jp * 4 + i);
          f32x2 pre[4], ee[4];
#pragma unroll
          for (int k = 0; k < 4; ++k) {
            const f32x4 a4 = acc[2 * jp + (k >> 1)][i], c4 = csum[2 * jp + (k >> 1)], b4 = bb[2 * jp + (k >> 1)];
            const f32x2 a2 = (k & 1) ? f32x2{a4.z, a4.w} : f32x2{a4.x, a4.y};
            const f32x2 c2 = (k & 1) ? f32x2{c4.z, c4.w} : f32x2{c4.x, c4.y};
            const f32x2 b2v = (k & 1) ? f32x2{b4.z, b4.w} : f32x2{b4.x, b4.y};
            pre[k] = a2 * rs[i] + (c2 * rm[i] + b2v);
            f32x2 arg = pre[k] * (pre[k] * pre[k] * -0.1029432397f + -2.302208198f);
            asm("" : "+v"(arg));
            ee[k] = f32x2{__builtin_amdgcn_exp2f(arg.x), __builtin_amdgcn_exp2f(arg.y)};
          }
          u32 o[4];
#pragma unroll
          for (int k = 0; k < 4; ++k) {
            f32x2 dn = ee[k] + 1.0f;
            asm("" : "+v"(dn));
            o[k] = pk2v(pre[k] * f32x2{__builtin_amdgcn_rcpf(dn.x), __builtin_amdgcn_rcpf(dn.y)});
          }
          __builtin_amdgcn_raw_buffer_store_b128(v4u{o[0], o[1], o[2], o[3]}, rsWS,
                                                 lane * 16 + ABL_HT + (((2 * mt + mi) * 56 + nt * 4 + 2 * ni + jp) * 4 + i) * 1024, 0, SC1);
          SCHED();
        }
      }
      set_progress(L, wv, 4u);
      __builtin_amdgcn_s_setprio(0);
      TS(6);
#pragma unroll
      for (int c = 0; c < 8; ++c) AW_LOAD(c >> 2, c & 3);
      SCHED();
      __builtin_amdgcn_s_setprio(3);
      WAITV(8);
      if (lane == 0) __hip_atomic_store(ctl + WS_FH / 4 + ((2 * mt + mi) * 32 + 2 * nt + ni) * FL, epoch, RLX_AGENT);
      __builtin_amdgcn_s_setprio(0);
      SCHED();
      TS(7);
#pragma unroll
      for (int c = 8; c < 28; ++c) AW_LOAD(c >> 2, c & 3);
      SCHED();
    } else {
      __builtin_amdgcn_s_setprio(0);
      BAR();
      set_progress(L, wv, 4u);
      wait_progress(L, 4 + (lane & 3), 1u);
#pragma unroll
      for (int s = 0; s < 7; ++s)
#pragma unroll
        for (int c = 0; c < 4; ++c) AW_LOAD(s, c);
      SCHED();
    }
    TS(8);
    gemm2_tail(aw, epoch, nullptr, epoch + 1u);
  }
}

extern "C" void kernel_launch(void* const* d_in, const int* in_sizes, int n_in, void* d_out, int out_size, void* d_ws,
                              size_t ws_size, hipStream_t stream) {
  (void)in_sizes; (void)n_in; (void)out_size; (void)ws_size;
  (void)hipFuncSetAttribute((const void*)s4_mixer_kernel, hipFuncAttributeMaxDynamicSharedMemorySize, LDS_BYTES);
  s4_mixer_kernel<<<256, 512, LDS_BYTES, stream>>>(
      (const u16*)d_in[0], 2097152u, 0x00020000u, (const u16*)d_in[10], 1835008u, 0x00020000u, (const u16*)d_in[12], (char*)d_ws, (const u16*)d_in[1], (const u16*)d_in[2],
      (const u16*)d_in[3], (const u16*)d_in[4], (const u16*)d_in[5], (const u16*)d_in[6], (const u16*)d_in[7],
      (const u16*)d_in[8], (const u16*)d_in[11], (const u16*)d_in[13], (u16*)d_out);
}
```

```cpp
#define STAMP
#include <hip/hip_runtime.h>
#include <hip/hip_bf16.h>

typedef unsigned int u32;
typedef unsigned short u16;
typedef u32 v4u __attribute__((ext_vector_type(4)));
typedef u32 v2u __attribute__((ext_vector_type(2)));
typedef float f32x2 __attribute__((ext_vector_type(2)));
typedef float f32x4 __attribute__((ext_vector_type(4)));
typedef float f32x16 __attribute__((ext_vector_type(16)));
typedef short s16x4 __attribute__((ext_vector_type(4)));
typedef short s16x8 __attribute__((ext_vector_type(8)));
typedef __bf16 bf16x2 __attribute__((ext_vector_type(2)));
#define GAS __attribute__((address_space(1)))
#define LAS __attribute__((address_space(3)))
#define DEV __device__ __forceinline__
#define RLX_AGENT __ATOMIC_RELAXED, __HIP_MEMORY_SCOPE_AGENT
#define SC1 16
#define TS(i) do { SCHED(); ts[i] = (u32)__builtin_amdgcn_s_memrealtime(); SCHED(); } while (0)
#define TC(i) do { SCHED(); unsigned long long tt_; asm volatile("s_memtime %0\n\ts_waitcnt lgkmcnt(0)" : "=s"(tt_)::"memory"); tc[i] = (u32)tt_; SCHED(); } while (0)

constexpr int WS_E = 0;
constexpr int FL = 32;
constexpr int WS_STAT = 4096;
constexpr int WS_FXN = 65536;
constexpr int WS_FW2 = WS_FXN + 256 * 128;
constexpr int WS_FH = WS_FW2 + 1024 * 128;
constexpr int WS_FS = WS_FH + 1024 * 128;
constexpr int WS_XNT = 524288;
constexpr int WS_W2T = WS_XNT + 2097152;
constexpr int WS_HT = WS_W2T + 1835008;
constexpr int WS_ST = WS_HT + 7340032;
constexpr int WS_END = WS_ST + 2097152;
constexpr int LD_Q = 98304;
constexpr int LD_KREG = 131072;
constexpr int LD_PS = LD_KREG + 7424;
constexpr int LD_DUMMY = LD_KREG + 7680;
constexpr int LD_B1 = LD_KREG + 8192 + 7424;
constexpr int LD_HST = LD_KREG + 3 * 8192 + 7424;
constexpr int LD_STAT = 12288;
constexpr int LD_U = 0;
constexpr int LD_CX = 16384;
constexpr int LD_HX = 32768;
constexpr int LD_RED = 0;
constexpr int LD_ST = 114688;
constexpr int ABL_HT = WS_HT;
constexpr int LDS_BYTES = 163840;
constexpr u32 SPIN_CAP = 1u << 16;
constexpr u32 CODE_BYTES = $$$$$$$$$$$$$$$$$$$$$$$$$$$$$$$$$$$$$$$$$$$$$$$$;

DEV float bf2f(u32 h) { return __builtin_bit_cast(float, h << 16); }
DEV float bflo(u32 d) { return __builtin_bit_cast(float, d << 16); }
DEV float bfhi(u32 d) { return __builtin_bit_cast(float, d & 0xffff0000u); }
DEV u32 pk2(float lo, float hi) {
  f32x2 v = {lo, hi};
  return __builtin_bit_cast(u32, __builtin_convertvector(v, bf16x2));
}
DEV u32 pk2v(f32x2 v) { return __builtin_bit_cast(u32, __builtin_convertvector(v, bf16x2)); }
DEV float bfr(float v) { return bflo(pk2(v, v)); }
DEV f32x4 mfma16(v4u a, v4u b, f32x4 c) {
  return __builtin_amdgcn_mfma_f32_16x16x32_bf16(__builtin_bit_cast(s16x8, a), __builtin_bit_cast(s16x8, b), c, 0, 0, 0);
}
DEV f32x16 mfma32(v4u a, v4u b, f32x16 c) {
  return __builtin_amdgcn_mfma_f32_32x32x16_bf16(__builtin_bit_cast(s16x8, a), __builtin_bit_cast(s16x8, b), c, 0, 0, 0);
}
DEV v2u trread(LAS char* p) { return __builtin_bit_cast(v2u, __builtin_amdgcn_ds_read_tr16_b64_v4i16((LAS s16x4*)p)); }
DEV v4u mk_rsrc(const void* p, u32 bytes, u32 fmt) {
  const unsigned long long a = (unsigned long long)p;
  v4u r = {(u32)a, (u32)(a >> 32), bytes, fmt};
  return r;
}
DEV void dma16(u32 voff, v4u rs, u32 soff, u32 lds) {
  asm volatile("s_mov_b32 m0, %0\n\ts_nop 0\n\tbuffer_load_dwordx4 %1, %2, %3 offen lds" ::"s"(lds), "v"(voff), "s"(rs), "s"(soff) : "memory");
}
#define WAITV(n) asm volatile("s_waitcnt vmcnt(" #n ")" ::: "memory")
#define WAITL() asm volatile("s_waitcnt lgkmcnt(0)" ::: "memory")
#define SCHED() __builtin_amdgcn_sched_barrier(0)
#define BAR() do { asm volatile("s_waitcnt lgkmcnt(0)" ::: "memory"); __builtin_amdgcn_s_barrier(); asm volatile("" ::: "memory"); SCHED(); } while (0)
#define ORDER_BEHIND_POLL() __builtin_amdgcn_fence(__ATOMIC_ACQUIRE, "wavefront")

DEV f32x2 cmul(f32x2 a, f32x2 z) {
  const f32x2 zr = {-z.y, z.x};
  f32x2 t, r;
  asm("v_pk_mul_f32 %0, %1, %2 op_sel:[1,0] op_sel_hi:[1,1]" : "=v"(t) : "v"(a), "v"(zr));
  asm("v_pk_fma_f32 %0, %1, %2, %3 op_sel_hi:[0,1,1]" : "=v"(r) : "v"(a), "v"(z), "v"(t));
  return r;
}
DEV float sadd(float a, float b) { float r; asm("v_add_f32 %0, %1, %2" : "=v"(r) : "v"(a), "v"(b)); return r; }
DEV float smul(float a, float b) { float r; asm("v_mul_f32 %0, %1, %2" : "=v"(r) : "v"(a), "v"(b)); return r; }
DEV f32x2 zpow(float la, float th, float k) {
  const float mag = __builtin_amdgcn_exp2f(la * k);
  const float fr = __builtin_amdgcn_fractf(th * k);
  f32x2 r = {mag * __builtin_amdgcn_cosf(fr), mag * __builtin_amdgcn_sinf(fr)};
  return r;
}
template <int SLEEP>
DEV void poll_flags(GAS u32* p, u32 epoch, u32 first) {
  if (!__all(first == epoch)) {
#pragma unroll 1
    for (u32 spins = 0; spins < SPIN_CAP; ++spins) {
      const u32 v = __hip_atomic_load(p, RLX_AGENT);
      if (__all(v == epoch)) break;
      __builtin_amdgcn_s_sleep(SLEEP);
    }
  }
  ORDER_BEHIND_POLL();
}
DEV void publish(GAS u32* p, u32 epoch, int lane) {
  WAITV(0);
  if (lane == 0) __hip_atomic_store(p, epoch, RLX_AGENT);
}
DEV void set_progress(LAS char* L, int wv, u32 k) {
  WAITL();
  *(volatile LAS u32*)(L + LD_PS + wv * 4) = k;
}
DEV void wait_progress(LAS char* L, int word, u32 k) {
#pragma unroll 1
  for (u32 spins = 0; spins < (1u << 22); ++spins) {
    const u32 pv = *(volatile LAS u32*)(L + LD_PS + word * 4);
    if (__all(pv >= k)) break;
    __builtin_amdgcn_s_sleep(1);
  }
  asm volatile("" ::: "memory");
}
DEV void ln_stats(float s1, float s2, float& mean, float& var, float& den1) {
  mean = s1 * (1.0f / 512.0f);
  var = fmaxf(s2 * (1.0f / 512.0f) - mean * mean, 0.0f);
  den1 = bfr(__builtin_amdgcn_sqrtf(bfr(bfr(var) + 1e-5f)));
}
DEV float pick4(f32x4 g, int k) { return k == 0 ? g.x : k == 1 ? g.y : k == 2 ? g.z : g.w; }
#define LD16(p, i) ((u32)__hip_atomic_load((const GAS u16*)(p) + (i), __ATOMIC_RELAXED, __HIP_MEMORY_SCOPE_WAVEFRONT))

__global__ void __launch_bounds__(512, 2)
s4_mixer_kernel(const u16* __restrict__ x, u32 x_bytes, u32 x_fmt, const u16* __restrict__ w1, u32 w1_bytes, u32 w1_fmt,
                const u16* __restrict__ w2, char* __restrict__ ws,
                const u16* __restrict__ A_re, const u16* __restrict__ A_im, const u16* __restrict__ B_re,
                const u16* __restrict__ B_im, const u16* __restrict__ C_re, const u16* __restrict__ C_im,
                const u16* __restrict__ log_dt, const u16* __restrict__ Dskip, const u16* __restrict__ b1,
                const u16* __restrict__ b2, u16* __restrict__ out) {
  extern __shared__ __attribute__((aligned(16))) char smem[];
  LAS char* const L = (LAS char*)smem;
  unsigned long long pc;
  asm volatile("s_getpc_b64 %0" : "=s"(pc));
  {
    u32 coff = ((((threadIdx.x & 63) << 3) | (threadIdx.x >> 6)) * 32u + (blockIdx.x >> 3)) * 64u;
    coff = coff < CODE_BYTES ? coff : 0u;
    asm volatile("s_mov_b32 m0, %0\n\ts_nop 0\n\tglobal_load_lds_dword %1, %2" ::"s"((u32)LD_DUMMY), "v"(coff), "s"(pc) : "memory");
    SCHED();
  }
  const int tid = threadIdx.x, lane = tid & 63;
  const int wv = __builtin_amdgcn_readfirstlane(tid >> 6);
  const int w = wv & 3;
  const bool swave = wv >= 4;
  const int b = blockIdx.x, xcd = b & 7, idx = b >> 3;
  const int r16 = lane & 15, q = lane >> 4, qq = r16 >> 2, p4 = lane & 3;
  const bool helper = idx >= 28;
  u32 ts[16], tc[16];
#pragma unroll
  for (int i = 0; i < 16; ++i) { ts[i] = 0; tc[i] = 0; }
  TS(0);
  const u32 cyc0 = (u32)__builtin_amdgcn_s_memtime();
  const v4u rsX = mk_rsrc(x, x_bytes, x_fmt);
  const v4u rsW1 = mk_rsrc(w1, w1_bytes, w1_fmt);
  asm volatile("" ::"s"(rsX), "s"(rsW1), "s"(w2), "s"(ws), "s"(A_re), "s"(A_im), "s"(B_re), "s"(B_im), "s"(C_re), "s"(C_im), "s"(log_dt), "s"(Dskip), "s"(b1));
  u32 eraw;
  asm volatile("s_load_dword %0, %1, 0x0" : "=s"(eraw) : "s"(ws));
  const int mq = (idx * 37) >> 8, mr = idx - 7 * mq;
  const int mt = (xcd >> 1) * 4 + mq, nt = (xcd & 1) * 7 + mr;
  const int hid = xcd * 4 + (idx - 28);
  const int tg = (xcd >> 1) * 8 + (idx >> 2), cg = (xcd & 1) * 4 + (idx & 3);
  const int cw = w >> 1, hw = w & 1;
  const int ch = 64 * xcd + 2 * idx + cw;
  const __amdgpu_buffer_rsrc_t rsWS = __builtin_amdgcn_make_buffer_rsrc((void*)ws, 0, WS_END, 0x00020000);
  GAS u32* const ctl = (GAS u32*)ws;
  const v4u ones = {0x3f803f80u, 0x3f803f80u, 0x3f803f80u, 0x3f803f80u};
  int rot2 = (tg & 7) + 2 * (cg & 3);
  rot2 -= rot2 >= 7 ? 7 : 0;
#define KS2(s) (7 * wv + ((s) + rot2 >= 7 ? (s) + rot2 - 7 : (s) + rot2))
#define AW_LOAD(s, c) \
  aw[s][c] = __builtin_amdgcn_raw_buffer_load_b128(rsWS, lane * 16 + ((((c) + 2 * (wv & 1)) & 3) << 10), WS_W2T + (cg * 56 + KS2(s)) * 4096, SC1)
  const u32 wo = (u32)lane * 4u;
#define TW(rowp, row, val) *(LAS u32*)((rowp) + (wo ^ (u32)(((row) & 15) << 4))) = (val)
  LAS char* const PT = L + LD_KREG + w * 8192;

  auto gemm2_tail = [&](v4u (&aw)[7][4], const u32 epoch, GAS u32* const pend, u32 fh0) __attribute__((always_inline)) {
  {
    TS(9);
    int tz = threadIdx.x;
    asm volatile("" : "+v"(tz));
    const int lane = tz & 63, r16 = lane & 15, q = lane >> 4, qq = r16 >> 2, p4 = lane & 3;
    asm volatile("" : "+v"(fh0));
    poll_flags<2>(ctl + WS_FH / 4 + (tg * 32 + ((7 * wv) >> 1) + (lane & 3)) * FL, epoch, fh0);
    TS(10);
    constexpr int WIN = 4;
    v4u hf[7][4];
#define H_LOAD(s) _Pragma("unroll") for (int j = 0; j < 4; ++j) \
      hf[s][j] = __builtin_amdgcn_raw_buffer_load_b128(rsWS, lane * 16 + (((j + (wv >> 1)) & 3) << 10), WS_HT + (tg * 56 + KS2(s)) * 4096, SC1);
#pragma unroll
    for (int s = 0; s < WIN; ++s) { H_LOAD(s) SCHED(); }
    if (pend) {
      WAITV(16);
      if (lane == 0) __hip_atomic_store(pend, epoch, RLX_AGENT);
      SCHED();
    }
    GAS u32* const fsp = ctl + WS_FS / 4 + ((tg >> 4) * 512 + cg * 64 + wv * 8 + (lane >> 3)) * FL;
    const int tok = tg * 64 + 16 * (wv >> 1) + r16;
    const int c8 = cg * 64 + 32 * (wv & 1) + 8 * q;
    u32 fs, fe0 = epoch, fe1 = epoch;
    bool early;
    v4u xv, bv, sv;
    f32x4 acc[4][4];
#pragma unroll
    for (int i = 0; i < 4; ++i)
#pragma unroll
      for (int j = 0; j < 4; ++j) acc[i][j] = f32x4{0.f, 0.f, 0.f, 0.f};
#pragma unroll
    for (int s = 0; s < 7; ++s) {
#pragma unroll
      for (int j = 0; j < 4; ++j)
#pragma unroll
        for (int c = 0; c < 4; ++c) acc[c][j] = mfma16(aw[s][c], hf[s][j], acc[c][j]);
      SCHED();
      if (s == 2) fs = __hip_atomic_load(fsp, RLX_AGENT);
      if (s + WIN < 7) { H_LOAD(s + WIN) }
      if (s == 2) {
        xv = *(const GAS v4u*)(x + tok * 512 + c8);
        bv = *(const GAS v4u*)(b2 + c8);
      }
      if (s == 5) {
        early = __all(fs == epoch);
        ORDER_BEHIND_POLL();
        sv = __builtin_amdgcn_raw_buffer_load_b128(rsWS, (lane >> 3) * 4096 + (lane & 7) * 16, early ? WS_ST + (cg * 64 + wv * 8) * 4096 + tg * 128 : WS_END, SC1);
        if (b == 0) {
          fe0 = __hip_atomic_load(ctl + WS_FS / 4 + tz * FL, RLX_AGENT);
          fe1 = __hip_atomic_load(ctl + WS_FS / 4 + (512 + tz) * FL, RLX_AGENT);
        }
      }
      SCHED();
    }
    TS(11);
    wait_progress(L, lane & 7, 4u);
#pragma unroll
    for (int j = 0; j < 4; ++j)
#pragma unroll
      for (int cp = 0; cp < 2; ++cp)
        if (j + cp > 0) {
          const int d = (((j + (wv >> 1)) & 3) << 1) | ((cp + wv) & 1);
#pragma unroll
          for (int e = 0; e < 2; ++e)
            *(LAS f32x4*)(L + LD_RED + (d * 7 + 2 * j + cp - 1) * 2048 + e * 1024 + lane * 16) = acc[2 * cp + e][j];
        }
    if (!early) {
      poll_flags<2>(fsp, epoch, epoch + 1u);
      sv = __builtin_amdgcn_raw_buffer_load_b128(rsWS, (lane >> 3) * 4096 + (lane & 7) * 16, WS_ST + (cg * 64 + wv * 8) * 4096 + tg * 128, SC1);
    }
    *(LAS v4u*)(L + LD_ST + wv * 1024 + lane * 16) = sv;
    TS(12);
    asm volatile("" : "+v"(fe0), "+v"(fe1));
    poll_flags<2>(ctl + WS_FS / 4 + tz * FL, epoch, fe0);
    poll_flags<2>(ctl + WS_FS / 4 + (512 + tz) * FL, epoch, fe1);
    BAR();
    if (b == 0 && tz == 0) __hip_atomic_store(ctl + WS_E / 4, epoch, RLX_AGENT);
    TS(13);
    f32x4 part[2][7];
    v2u sq[2];
#pragma unroll
    for (int e = 0; e < 2; ++e) {
#pragma unroll
      for (int j = 0; j < 7; ++j) part[e][j] = *(LAS f32x4*)(L + LD_RED + (wv * 7 + j) * 2048 + e * 1024 + lane * 16);
      sq[e] = trread(L + LD_ST + (32 * (wv & 1) + 8 * q + 4 * e + qq) * 128 + (16 * (wv >> 1) + 4 * p4) * 2);
    }
    SCHED();
    u32 o[4];
#pragma unroll
    for (int e = 0; e < 2; ++e) {
      const u32 ba = bv[2 * e], bb = bv[2 * e + 1], xa = xv[2 * e], xb = xv[2 * e + 1];
      f32x4 m = acc[e][0] + f32x4{bflo(ba), bfhi(ba), bflo(bb), bfhi(bb)};
#pragma unroll
      for (int j = 0; j < 7; ++j) m += part[e][j];
      f32x4 g;
#pragma unroll
      for (int k = 0; k < 4; ++k) g[k] = m[k] * __builtin_amdgcn_rcpf(1.0f + __builtin_amdgcn_exp2f(m[k] * -1.4426950408889634f));
      o[2 * e] = pk2(bflo(xa) + bflo(sq[e].x) * g.x, bfhi(xa) + bfhi(sq[e].x) * g.y);
      o[2 * e + 1] = pk2(bflo(xb) + bflo(sq[e].y) * g.z, bfhi(xb) + bfhi(sq[e].y) * g.w);
    }
    *(GAS v4u*)(out + tok * 512 + c8) = v4u{o[0], o[1], o[2], o[3]};
  }
  TS(14);
  WAITV(0);
  TS(15);
  const u32 cyc1 = (u32)__builtin_amdgcn_s_memtime();
  if (b == 40 && wv == 0) { const u32 X = 0u; const u32 te = ts[15] + 4u * X; while ((int)((u32)__builtin_amdgcn_s_memrealtime() - te) < 0) __builtin_amdgcn_s_sleep(2); }
  if (lane == 0) *(GAS u32*)(ws + (33 << 20) + (b * 8 + wv) * 4) = cyc1 - cyc0;
  if (lane == 0) {
#pragma unroll
    for (int i = 0; i < 16; ++i) *(GAS u32*)(ws + (32 << 20) + ((b * 8 + wv) * 16 + i) * 4) = ts[i];
#pragma unroll
    for (int i = 0; i < 16; ++i) *(GAS u32*)(ws + (34 << 20) + ((b * 8 + wv) * 16 + i) * 4) = tc[i];
  }
  __builtin_amdgcn_endpgm();
  };

  if (helper) {
    LAS char* const HX = L + LD_HX + wv * 8192;
#pragma unroll
    for (int p = 0; p < 8; ++p)
      dma16((u32)(wv * 8192 + ((lane ^ (((p & 3) << 2) | (p >> 2))) << 4)), rsX, (u32)((hid * 64 + p) * 1024), (u32)(LD_HX + (wv * 8 + p) * 1024));
    u32 epoch;
    asm volatile("s_waitcnt vmcnt(0) lgkmcnt(0)\n\ts_add_u32 %0, %1, 1" : "=s"(epoch) : "s"(eraw) : "memory", "scc");
    if (!swave) TS(3);
    f32x4 gram[2], sumx[2];
#pragma unroll
    for (int i = 0; i < 2; ++i) { gram[i] = f32x4{0.f, 0.f, 0.f, 0.f}; sumx[i] = gram[i]; }
    const int r8 = lane & 7, fr = ((r8 & 3) << 2) | (r8 >> 2);
    v4u xr[8];
#pragma unroll
    for (int s = 0; s < 8; ++s) xr[s] = *(LAS v4u*)(HX + r8 * 1024 + (lane & 8) * 64 + (((4 * s + q) ^ fr) << 4));
    v2u d[8][2];
#pragma unroll
    for (int s = 0; s < 8; ++s)
#pragma unroll
      for (int t = 0; t < 2; ++t)
        d[s][t] = trread(HX + (4 * t + qq) * 1024 + (((8 * s + 2 * q + (p4 >> 1)) ^ ((qq << 2) | t)) << 4) + 8 * (p4 & 1));
    SCHED();
#pragma unroll
    for (int s = 0; s < 8; ++s) {
      gram[s & 1] = mfma16(xr[s], xr[s], gram[s & 1]);
      sumx[s & 1] = mfma16(ones, xr[s], sumx[s & 1]);
    }
    float s1 = sumx[0].x + sumx[1].x, s2 = pick4(gram[0] + gram[1], r16 & 3);
    s1 += __shfl(s1, (lane + 40) & 63);
    s2 += __shfl(s2, (lane + 40) & 63);
    float mean, var, den1;
    ln_stats(s1, s2, mean, var, den1);
    const float rd = __builtin_amdgcn_rcpf(den1);
    const float den2 = bfr(__builtin_amdgcn_sqrtf(bfr(bfr(var * rd * rd) + 1e-5f)));
    const float R = __builtin_amdgcn_rcpf(den1 * den2);
    if (q == qq && r16 < 8) {
      *(LAS f32x2*)(L + LD_HST + (8 * wv + r16) * 8) = f32x2{rd, -bfr(mean) * rd};
      __builtin_amdgcn_raw_buffer_store_b128(v4u{__builtin_bit_cast(u32, R), epoch, __builtin_bit_cast(u32, R * mean), epoch}, rsWS,
                                             WS_STAT + (hid * 64 + 8 * wv + r16) * 16, 0, SC1);
    }
    WAITL();
    if (!swave) TS(4);
    f32x4 sv[4];
#pragma unroll
    for (int i = 0; i < 4; ++i) sv[i] = *(LAS f32x4*)(L + LD_HST + wv * 64 + i * 16);
#pragma unroll
    for (int cb = 0; cb < 8; ++cb) {
      v4u o;
      o.x = pk2(bflo(d[cb][0].x) * sv[0].x + sv[0].y, bfhi(d[cb][0].x) * sv[0].z + sv[0].w);
      o.y = pk2(bflo(d[cb][0].y) * sv[1].x + sv[1].y, bfhi(d[cb][0].y) * sv[1].z + sv[1].w);
      o.z = pk2(bflo(d[cb][1].x) * sv[2].x + sv[2].y, bfhi(d[cb][1].x) * sv[2].z + sv[2].w);
      o.w = pk2(bflo(d[cb][1].y) * sv[3].x + sv[3].y, bfhi(d[cb][1].y) * sv[3].z + sv[3].w);
      __builtin_amdgcn_raw_buffer_store_b128(o, rsWS, lane * 16 + WS_XNT + (hid * 8 + wv) * 8192 + cb * 1024, 0, SC1);
    }
    if (!swave) TS(5);
    WAITV(0);
    BAR();
    if (tid == 0) __hip_atomic_store(ctl + WS_FXN / 4 + hid * FL, epoch, RLX_AGENT);
    if (!swave) TS(6);
    SCHED();
  }

  if (swave) {
    __builtin_amdgcn_s_setprio(2);
    const u32 voX = (u32)((32 * w + (lane >> 3)) * 1024 + (((lane & 7) ^ (lane >> 3)) << 4));
    const u32 voW = (u32)((4 * w + q) * 3584 + ((r16 ^ ((q << 2) | w)) << 4));
    const int rot = (mr + 2 * mq) & 7;
#define G1_STAGE(st) G1_PART(st, 0, 4)
#define G1_PART(st, p0, p1)                                                                                  \
  {                                                                                                          \
    const int sa_ = ((st) + rot) & 7;                                                                        \
    _Pragma("unroll") for (int p = p0; p < p1; ++p) {                                                        \
      dma16(voX, rsX, (u32)(mt * 131072 + p * 8192 + sa_ * 128), (u32)(((st) & 3) * 32768 + w * 4096 + p * 1024)); \
      dma16(voW, rsW1, (u32)((sa_ * 64 + 16 * p) * 3584 + nt * 256), (u32)(((st) & 3) * 32768 + 16384 + p * 4096 + w * 1024)); \
    }                                                                                                        \
  }
    if (!helper) { G1_STAGE(0) G1_STAGE(1) G1_PART(2, 0, 2) }
    if (lane == 0) *(LAS u32*)(L + LD_PS + wv * 4) = 0u;
    TS(1);
    WAITV(12);
    u32 epoch;
    asm volatile("s_waitcnt lgkmcnt(0)\n\ts_add_u32 %0, %1, 1" : "=s"(epoch) : "s"(eraw) : "memory", "scc");
    TS(2);
    BAR();
    TS(3);
    TC(0);
    if (!helper) G1_PART(2, 2, 4)
    const int r32 = lane & 31, hh = lane >> 5;
    v4u pf[8], qf[8];
    {
#pragma unroll
      for (int s = 0; s < 8; ++s) {
        qf[s] = *(LAS v4u*)(L + LD_Q + w * 8192 + r32 * 256 + (((2 * s + hh) ^ (r32 & 15)) << 4));
        pf[s] = *(LAS v4u*)(PT + (2 * s + hh) * 464 + r32 * 16);
      }
    }
    SCHED();
    TC(1);
    if (!helper) {
      WAITV(8); BAR(); TC(2); G1_STAGE(3) G1_PART(4, 0, 2) TC(3);
      WAITV(12); BAR(); TC(4); G1_PART(4, 2, 4) G1_STAGE(5) TC(5);
      WAITV(16); BAR(); TC(6);
      G1_STAGE(6) TC(7);
      WAITV(16); BAR(); TC(8); G1_STAGE(7) TC(9);
      WAITV(16); BAR();
    }
    TC(10);
    GAS u32* const fxnp = ctl + WS_FXN / 4 + (hw * 16 + r16) * FL;
    GAS u32* const fw2p = ctl + WS_FW2 / 4 + ((cg * 56 + 7 * (w + 4 * ((lane >> 4) & 1))) * 2 + min(r16, 13)) * FL;
    const int sto = WS_STAT + ((helper ? 0 : mt) * 128 + 32 * w + r32) * 16;
    v4u sg = __builtin_amdgcn_raw_buffer_load_b128(rsWS, sto, 0, SC1);
    const u32 fw2 = __hip_atomic_load(fw2p, RLX_AGENT);
    const u32 pDs = LD16(Dskip, ch);
    SCHED();
    f32x16 kacc;
#pragma unroll
    for (int i = 0; i < 16; ++i) kacc[i] = 0.f;
#pragma unroll
    for (int s = 0; s < 8; ++s) kacc = mfma32(pf[s], qf[s], kacc);
    SCHED();
    TC(11);
    if (!helper) { WAITV(3); BAR(); }
    TC(12);
    TS(4);
#pragma unroll
    for (int i = 0; i < 4; ++i)
      *(LAS f32x4*)(L + LD_CX + w * 4096 + i * 1024 + lane * 16) = f32x4{kacc[4 * i], kacc[4 * i + 1], kacc[4 * i + 2], kacc[4 * i + 3]};
    if (!helper) {
      if (!__all(sg.y == epoch && sg.w == epoch)) {
#pragma unroll 1
        for (u32 spins = 0; spins < SPIN_CAP; ++spins) {
          __builtin_amdgcn_s_sleep(4);
          asm volatile("" ::: "memory");
          sg = __builtin_amdgcn_raw_buffer_load_b128(rsWS, sto, 0, SC1);
          if (__all(sg.y == epoch && sg.w == epoch)) break;
        }
      }
      *(LAS v2u*)(L + LD_STAT + (32 * w + r32) * 8) = v2u{sg.x, sg.z};
    }
    poll_flags<4>(fw2p, epoch, fw2);
    set_progress(L, wv, 1u);
    TS(5);
    u32 fxa = __hip_atomic_load(fxnp, RLX_AGENT);
    v4u aw[7][4];
#pragma unroll
    for (int c = 0; c < 12; ++c) AW_LOAD(c >> 2, c & 3);
    SCHED();
    wait_progress(L, wv ^ 1, 1u);
    {
      f32x4 v[4];
#pragma unroll
      for (int i = 0; i < 4; ++i) v[i] = *(LAS f32x4*)(L + LD_CX + (w ^ 1) * 4096 + i * 1024 + lane * 16);
#pragma unroll
      for (int i = 0; i < 15; ++i) {
        kacc[i] = sadd(kacc[i], v[i >> 2][i & 3]);
      }
    }
    {
      const bool odd = r32 & 1, up = hh != 0;
      float e[4][9];
      const int ia[4][9] = {{0, 1, 2, 3, 4, 5, 6, 7, 8}, {9, 9, 9, 9, -1, 10, 10, 10, 10}, {11, 11, 11, 11, -1, 12, 12, 12, 12}, {13, 13, 13, 13, -1, 14, 14, 14, 14}};
      const int ib[4][9] = {{0, 0, 1, 2, 2, 2, 3, 4, 4}, {5, 5, 5, 6, 6, 6, 7, 7, 7}, {8, 8, 8, 9, 9, 9, 10, 10, 10}, {11, 11, 11, 11, -1, 12, 12, 12, 12}};
#pragma unroll
      for (int u = 0; u < 4; ++u)
#pragma unroll
        for (int i = 0; i < 9; ++i) {
          const float va = ia[u][i] >= 0 ? kacc[ia[u][i]] : (odd ? kacc[ia[u][5]] : kacc[ia[u][3]]);
          const float vb = ib[u][i] >= 0 ? kacc[ib[u][i]] : (odd ? kacc[ib[u][5]] : kacc[ib[u][3]]);
          e[u][i] = up ? vb : va;
        }
      LAS char* const KC = PT + hw * 64 + (255 - r32) * 16;
#pragma unroll
      for (int u = 0; u < 4; ++u) {
        v4u d;
        if (hw) { d.x = pk2(e[u][8], e[u][7]); d.y = pk2(e[u][6], e[u][5]); d.z = pk2(e[u][4], e[u][3]); d.w = pk2(e[u][2], e[u][1]); }
        else    { d.x = pk2(e[u][7], e[u][6]); d.y = pk2(e[u][5], e[u][4]); d.z = pk2(e[u][3], e[u][2]); d.w = pk2(e[u][1], e[u][0]); }
        const int sa = u == 0 ? 0 : u + 3, sb = u == 3 ? 7 : u + 1;
        *(LAS v4u*)(KC - (up ? sb : sa) * 512) = d;
      }
      if (lane < 2) *(LAS v4u*)(PT + hw * 64 + 4096 + lane * 16) = v4u{(hw && lane == 0) ? pk2(kacc[0], 0.f) : 0u, 0u, 0u, 0u};
    }
    SCHED();
    TC(13);
    asm volatile("" : "+v"(fxa));
    poll_flags<8>(fxnp, epoch, fxa);
    const v4u u0 = __builtin_amdgcn_raw_buffer_load_b128(rsWS, lane * 8192, WS_XNT + (128 * hw) * 8192 + ch * 16, SC1);
    const v4u u1 = __builtin_amdgcn_raw_buffer_load_b128(rsWS, lane * 8192, WS_XNT + (128 * hw + 64) * 8192 + ch * 16, SC1);
#pragma unroll
    for (int c = 12; c < 20; ++c) AW_LOAD(c >> 2, c & 3);
    SCHED();
    LAS char* const UB = L + LD_U + cw * 4608;
    if (hw == 0 && lane < 32) *(LAS v4u*)(UB + lane * 16) = v4u{0u, 0u, 0u, 0u};
    *(LAS v4u*)(UB + 512 + (128 * hw + lane) * 16) = u0;
    *(LAS v4u*)(UB + 512 + (128 * hw + 64 + lane) * 16) = u1;
    set_progress(L, wv, 2u);
    SCHED();
#pragma unroll
    for (int c = 20; c < 28; ++c) AW_LOAD(c >> 2, c & 3);
    {
      const u32 po = (u32)(WS_W2T + (cg * 56 + 7 * w) * 4096 + ((tg & 7) * 28 + min(lane, 27)) * 128);
      asm volatile("s_mov_b32 m0, %0\n\ts_nop 0\n\tglobal_load_lds_dword %1, %2 sc1" ::"s"((u32)LD_DUMMY), "v"(po), "s"(ws) : "memory");
    }
    SCHED();
    TS(6);
    wait_progress(L, wv ^ 1, 2u);
    wait_progress(L, w, 4u);
    TS(7);
    TC(14);
    const float dskip = bf2f(pDs);
    const int sgm = 1 - (r16 & 1);
    LAS char* const ka = L + LD_KREG + (2 * cw + sgm) * 8192 + sgm * 64 + 2 * (2031 - r16 + 8 * q + sgm) - hw * 256 - 7 * 512 - 3 * 64;
    LAS char* const ub = UB + 2 * (256 - 16 + 16 * r16 + 8 * q) - hw * 256 - 3 * 64;
    f32x4 y[8];
#pragma unroll
    for (int i = 0; i < 8; ++i) y[i] = f32x4{0.f, 0.f, 0.f, 0.f};
    v4u af[4][8], bfv[4][8];
    u32 kb[4];
#pragma unroll
    for (int i = 0; i < 4; ++i) { kb[i] = (u32)(size_t)ka + i * 1024; asm volatile("" : "+v"(kb[i])); }
#define CV_READ(rr, g)                                                                                   \
  {                                                                                                      \
    LAS u32* pa = (LAS u32*)(size_t)(kb[(7 - (g)) >> 1] + ((7 - (g)) & 1) * 512 + (3 - (rr)) * 64);      \
    af[rr][g] = v4u{pa[0], pa[1], pa[2], pa[3]};                                                         \
    bfv[rr][g] = *(LAS v4u*)(ub + (g) * 512 + (3 - (rr)) * 64);                                          \
  }
    CV_READ(0, 0) CV_READ(0, 1) CV_READ(0, 2) CV_READ(0, 3) CV_READ(0, 4)
    SCHED();
#pragma unroll
    for (int rr = 0; rr < 4; ++rr) {
#pragma unroll
      for (int k = 0; k < 8; ++k) {
        if (k + 5 < 8) CV_READ(rr, k + 5)
        else if (rr < 3) CV_READ(rr + 1, k + 5 - 8)
#pragma unroll
        for (int o = 0; o <= k; ++o)
          if (k + o <= 7) {
            y[k + o] = mfma16(af[rr][k], bfv[rr][o], y[k + o]);
            if (o < k) y[k + o] = mfma16(af[rr][o], bfv[rr][k], y[k + o]);
          }
        SCHED();
      }
    }
    TC(15);
    const u32 fh0 = __hip_atomic_load(ctl + WS_FH / 4 + (tg * 32 + ((7 * wv) >> 1) + (lane & 3)) * FL, RLX_AGENT);
    SCHED();
    f32x4 mine[4];
    if (hw) {
#pragma unroll
      for (int i = 0; i < 4; ++i) { *(LAS f32x4*)(L + LD_CX + w * 4096 + i * 1024 + lane * 16) = y[i]; mine[i] = y[4 + i]; }
    } else {
#pragma unroll
      for (int i = 0; i < 4; ++i) { *(LAS f32x4*)(L + LD_CX + w * 4096 + i * 1024 + lane * 16) = y[4 + i]; mine[i] = y[i]; }
    }
    set_progress(L, wv, 3u);
    wait_progress(L, wv ^ 1, 3u);
    f32x4 oy[4];
    v2u xv[4];
#pragma unroll
    for (int i = 0; i < 4; ++i) {
      oy[i] = *(LAS f32x4*)(L + LD_CX + (w ^ 1) * 4096 + i * 1024 + lane * 16);
      xv[i] = *(LAS v2u*)(UB + 512 + (256 * (4 * hw + i) + 16 * r16 + 4 * q) * 2);
    }
    set_progress(L, wv, 4u);
#pragma unroll
    for (int i = 0; i < 4; ++i) {
      const u32 ya = pk2(sadd(mine[i].x, oy[i].x), sadd(mine[i].y, oy[i].y)), yb = pk2(sadd(mine[i].z, oy[i].z), sadd(mine[i].w, oy[i].w));
      const u32 da = pk2(smul(bflo(xv[i].x), dskip), smul(bfhi(xv[i].x), dskip)), db = pk2(smul(bflo(xv[i].y), dskip), smul(bfhi(xv[i].y), dskip));
      v2u o;
      o.x = pk2(sadd(bflo(ya), bflo(da)), sadd(bfhi(ya), bfhi(da)));
      o.y = pk2(sadd(bflo(yb), bflo(db)), sadd(bfhi(yb), bfhi(db)));
      __builtin_amdgcn_raw_buffer_store_b64(o, rsWS, (16 * r16 + 4 * q) * 2 + WS_ST + ch * 4096 + (4 * hw + i) * 512, 0, SC1);
    }
    TS(8);
    __builtin_amdgcn_s_setprio(0);
    gemm2_tail(aw, epoch, ctl + WS_FS / 4 + (hw * 512 + ch) * FL, fh0);
  } else {
    if (!helper) __builtin_amdgcn_s_setprio(3);
    const int po = ch * 128 + hw * 64 + lane;
    const u32 pLd = LD16(log_dt, ch), pAr = LD16(A_re, po), pAi = LD16(A_im, po);
    const u32 pBr = LD16(B_re, po), pBi = LD16(B_im, po), pCr = LD16(C_re, po), pCi = LD16(C_im, po);
    const u32 pB1 = LD16(b1, (helper ? 0 : nt * 128) + (tid & 127));
    const int uks = 2 * (helper ? 0 : idx) + (w >> 1), upp = w & 1;
    v4u w2a, w2b;
    {
      const GAS v4u* src = (const GAS v4u*)(w2 + (uks * 32 + (lane >> 2)) * 512 + xcd * 64 + upp * 32 + (lane & 3) * 8);
      asm volatile("" : "+v"(src));
      w2a = src[0];
      w2b = src[16 * 64];
    }
    if (lane == 0) *(LAS u32*)(L + LD_PS + wv * 4) = 0u;
    SCHED();
    TS(1);
    const int mi = w >> 1, ni = w & 1;
    f32x4 acc[4][4], csum[4];
#pragma unroll
    for (int i = 0; i < 4; ++i) {
#pragma unroll
      for (int j = 0; j < 4; ++j) { acc[i][j] = f32x4{0.f, 0.f, 0.f, 0.f}; asm volatile("" : "+v"(acc[i][j])); }
      csum[i] = f32x4{0.f, 0.f, 0.f, 0.f};
      asm volatile("" : "+v"(csum[i]));
    }
    u32 xb[2][2], wb[2][4][2];
#pragma unroll
    for (int h = 0; h < 2; ++h) {
#pragma unroll
      for (int k = 0; k < 2; ++k) {
        xb[h][k] = (u32)(((64 * mi + r16) * 128 + ((q ^ (r16 & 7)) << 4)) ^ (k * 64)) + h * 65536;
        asm volatile("" : "+v"(xb[h][k]));
      }
#pragma unroll
      for (int j = 0; j < 4; ++j)
#pragma unroll
        for (int t = 0; t < 2; ++t) {
          wb[h][j][t] = (u32)(16384 + (8 * q + 4 * t + qq) * 256 + (((2 * (4 * ni + j) + (p4 >> 1)) ^ ((qq << 2) | ((2 * q + t) & 3))) << 4) + 8 * (p4 & 1)) + h * 65536;
          asm volatile("" : "+v"(wb[h][j][t]));
        }
    }
    v4u onev = ones;
    asm volatile("" : "+v"(onev));
    SCHED();
    TC(0);
    u32 epoch;
    asm volatile("s_waitcnt lgkmcnt(0)\n\ts_add_u32 %0, %1, 1" : "=s"(epoch) : "s"(eraw) : "memory", "scc");
    SCHED();
    {
      const float dt = bfr(__builtin_amdgcn_exp2f(bf2f(pLd) * 1.4426950408889634f));
      const float la = dt * bf2f(pAr) * 1.4426950408889634f;
      const float th = dt * bf2f(pAi) * 0.15915494309189535f;
      const f32x2 dBc = {dt * bf2f(pBr), -dt * bf2f(pBi)};
      const f32x2 Ccc = {bf2f(pCr), -bf2f(pCi)};
      const f32x2 z8 = zpow(la, th, 8.f), z16 = zpow(la, th, 16.f);
      const f32x2 y1 = zpow(la, -th, 1.f), y256 = zpow(la, -th, 256.f), y8 = {z8.x, -z8.y};
      const f32x2 y2 = cmul(y1, y1), y4 = cmul(y2, y2);
#define PROW(h, g) (((g) & 3) + 8 * ((g) >> 2) + 4 * (h))
#define PW(h, g, v) *(LAS u32*)(PT + (lane >> 2) * 464 + (lane & 3) * 4 + PROW(h, g) * 16) = pk2v(v)
      f32x2 W[8];
      W[0] = cmul(Ccc, dBc);
#pragma unroll
      for (int c = 1; c < 8; ++c) W[c] = cmul(W[c - 1], y256);
      {
        f32x2 te = W[0], to = cmul(W[0], y1), t1 = W[1];
        PW(0, 0, te); PW(0, 1, to); PW(1, 0, t1);
#pragma unroll
        for (int a = 1; a <= 4; ++a) {
          te = cmul(te, y2); PW(0, 2 * a, te);
          if (a < 4) { to = cmul(to, y2); PW(0, 2 * a + 1, to); }
          t1 = cmul(t1, y2); PW(1, a, t1);
        }
      }
      PW(1, 5, W[2]); PW(1, 6, cmul(W[2], y4)); PW(1, 7, cmul(W[2], y8));
      PW(1, 8, W[3]); PW(1, 9, cmul(W[3], y4)); PW(1, 10, cmul(W[3], y8));
      PW(0, 9, W[4]); PW(0, 10, cmul(W[4], y8));
      PW(0, 11, W[5]); PW(0, 12, cmul(W[5], y8));
      PW(0, 13, W[6]); PW(0, 14, cmul(W[6], y8));
      PW(1, 11, W[7]); PW(1, 12, cmul(W[7], y8));
      SCHED();
      TC(2);
      LAS char* const QA = L + LD_Q + w * 8192;
      *(LAS v4u*)(QA + lane * 16) = w2a;
      *(LAS v4u*)(QA + 1024 + lane * 16) = w2b;
      v2u wt[2][2];
#pragma unroll
      for (int e = 0; e < 2; ++e)
#pragma unroll
        for (int t = 0; t < 2; ++t) wt[e][t] = trread(QA + (16 * t + 4 * q + qq) * 64 + 16 * p4 + 8 * e);
      asm volatile("" ::: "memory");
      TC(3);
      f32x2 qa = {1.f, 0.f}, qb = z8;
#pragma unroll
      for (int i = 0; i < 16; ++i) {
        TW(QA + 2 * i * 256, 2 * i, pk2v(qa));
        TW(QA + (2 * i + 1) * 256, 2 * i + 1, pk2v(qb));
        if (i < 15) { qa = (i == 0) ? z16 : cmul(qa, z16); qb = cmul(qb, z16); }
        if (i == 1) {
#pragma unroll
      for (int e = 0; e < 2; ++e)
        __builtin_amdgcn_raw_buffer_store_b128(v4u{wt[e][0].x, wt[e][0].y, wt[e][1].x, wt[e][1].y}, rsWS,
                                               lane * 16 + (helper ? WS_END : WS_W2T) + ((xcd * 56 + uks) * 4 + 2 * upp + e) * 1024, 0, SC1);
        }
      }
    }
    *(LAS float*)(L + LD_B1 + (tid & 127) * 4) = bf2f(pB1);
    TC(4);
    TS(2);
    v4u aw[7][4];
    if (!helper) {
      v4u xf[2][4], wf[2][4];
#define G1_READ(n)                                                                                            \
  _Pragma("unroll") for (int i = 0; i < 4; ++i) {                                                             \
    const v2u lo = trread(L + (((n) >> 1) & 1) * 32768 + ((n) & 1) * 8192 + wb[((n) >> 2) & 1][i][0]);        \
    const v2u hi = trread(L + (((n) >> 1) & 1) * 32768 + ((n) & 1) * 8192 + wb[((n) >> 2) & 1][i][1]);        \
    wf[(n) & 1][i] = v4u{lo.x, lo.y, hi.x, hi.y};                                                             \
    xf[(n) & 1][i] = *(LAS v4u*)(L + (((n) >> 1) & 1) * 32768 + i * 2048 + xb[((n) >> 2) & 1][(n) & 1]);      \
  }
      __builtin_amdgcn_s_setprio(0);
      TC(5);
      BAR();
      TC(6);
      TS(3);
      G1_READ(0)
      SCHED();
#pragma unroll 1
      for (int it = 0; it < 2; ++it) {
#pragma unroll
        for (int n = 0; n < 8; ++n) {
          if (n & 1) {
            if (n < 4 || it == 0) BAR();
            if (n == 3 && it == 0) {
              __builtin_amdgcn_s_setprio(3);
              publish(ctl + WS_FW2 / 4 + ((xcd * 56 + uks) * 2 + upp) * FL, epoch, lane);
              __builtin_amdgcn_s_setprio(0);
            }
            SCHED();
          }
          G1_READ(n + 1)
#pragma unroll
          for (int j = 0; j < 4; ++j) {
#pragma unroll
            for (int i = 0; i < 4; ++i) acc[j][i] = mfma16(wf[n & 1][j], xf[n & 1][i], acc[j][i]);
            csum[j] = mfma16(wf[n & 1][j], onev, csum[j]);
          }
#pragma unroll
          for (int k = 0; k < 12; ++k) {
            __builtin_amdgcn_sched_group_barrier(0x008, 1, 0);
            __builtin_amdgcn_sched_group_barrier(0x100, 1, 0);
          }
          __builtin_amdgcn_sched_group_barrier(0x008, 8, 0);
          SCHED();
        }
      }
      TS(4);
      __builtin_amdgcn_s_setprio(3);
      wait_progress(L, 4 + (lane & 3), 1u);
      TS(5);
      float rs[4], rm[4];
      f32x4 bb[4];
#pragma unroll
      for (int i = 0; i < 4; ++i) {
        const f32x2 st = *(LAS f32x2*)(L + LD_STAT + (64 * mi + 16 * i + r16) * 8);
        rs[i] = st.x; rm[i] = -st.y;
        asm volatile("" : "+v"(rm[i]));
        bb[i] = *(LAS f32x4*)(L + LD_B1 + (64 * ni + 16 * i + 4 * q) * 4);
      }
      SCHED();
#pragma unroll
      for (int jp = 0; jp < 2; ++jp) {
#pragma unroll
        for (int i = 0; i < 4; ++i) {
          TC(8 + jp * 4 + i);
          f32x2 pre[4], ee[4];
#pragma unroll
          for (int k = 0; k < 4; ++k) {
            const f32x4 a4 = acc[2 * jp + (k >> 1)][i], c4 = csum[2 * jp + (k >> 1)], b4 = bb[2 * jp + (k >> 1)];
            const f32x2 a2 = (k & 1) ? f32x2{a4.z, a4.w} : f32x2{a4.x, a4.y};
            const f32x2 c2 = (k & 1) ? f32x2{c4.z, c4.w} : f32x2{c4.x, c4.y};
            const f32x2 b2v = (k & 1) ? f32x2{b4.z, b4.w} : f32x2{b4.x, b4.y};
            pre[k] = a2 * rs[i] + (c2 * rm[i] + b2v);
            f32x2 arg = pre[k] * (pre[k] * pre[k] * -0.1029432397f + -2.302208198f);
            asm("" : "+v"(arg));
            ee[k] = f32x2{__builtin_amdgcn_exp2f(arg.x), __builtin_amdgcn_exp2f(arg.y)};
          }
          u32 o[4];
#pragma unroll
          for (int k = 0; k < 4; ++k) {
            f32x2 dn = ee[k] + 1.0f;
            asm("" : "+v"(dn));
            o[k] = pk2v(pre[k] * f32x2{__builtin_amdgcn_rcpf(dn.x), __builtin_amdgcn_rcpf(dn.y)});
          }
          __builtin_amdgcn_raw_buffer_store_b128(v4u{o[0], o[1], o[2], o[3]}, rsWS,
                                                 lane * 16 + ABL_HT + (((2 * mt + mi) * 56 + nt * 4 + 2 * ni + jp) * 4 + i) * 1024, 0, SC1);
          SCHED();
        }
      }
      set_progress(L, wv, 4u);
      __builtin_amdgcn_s_setprio(0);
      TS(6);
#pragma unroll
      for (int c = 0; c < 8; ++c) AW_LOAD(c >> 2, c & 3);
      SCHED();
      __builtin_amdgcn_s_setprio(3);
      WAITV(8);
      if (lane == 0) __hip_atomic_store(ctl + WS_FH / 4 + ((2 * mt + mi) * 32 + 2 * nt + ni) * FL, epoch, RLX_AGENT);
      __builtin_amdgcn_s_setprio(0);
      SCHED();
      TS(7);
#pragma unroll
      for (int c = 8; c < 28; ++c) AW_LOAD(c >> 2, c & 3);
      SCHED();
    } else {
      __builtin_amdgcn_s_setprio(0);
      BAR();
      set_progress(L, wv, 4u);
      wait_progress(L, 4 + (lane & 3), 1u);
#pragma unroll
      for (int s = 0; s < 7; ++s)
#pragma unroll
        for (int c = 0; c < 4; ++c) AW_LOAD(s, c);
      SCHED();
    }
    TS(8);
    gemm2_tail(aw, epoch, nullptr, epoch + 1u);
  }
}

extern "C" void kernel_launch(void* const* d_in, const int* in_sizes, int n_in, void* d_out, int out_size, void* d_ws,
                              size_t ws_size, hipStream_t stream) {
  (void)in_sizes; (void)n_in; (void)out_size; (void)ws_size;
  (void)hipFuncSetAttribute((const void*)s4_mixer_kernel, hipFuncAttributeMaxDynamicSharedMemorySize, LDS_BYTES);
  s4_mixer_kernel<<<256, 512, LDS_BYTES, stream>>>(
      (const u16*)d_in[0], 2097152u, 0x00020000u, (const u16*)d_in[10], 1835008u, 0x00020000u, (const u16*)d_in[12], (char*)d_ws, (const u16*)d_in[1], (const u16*)d_in[2],
      (const u16*)d_in[3], (const u16*)d_in[4], (const u16*)d_in[5], (const u16*)d_in[6], (const u16*)d_in[7],
      (const u16*)d_in[8], (const u16*)d_in[11], (const u16*)d_in[13], (u16*)d_out);
}
```

```cpp
#define STAMP
#include <hip/hip_runtime.h>
#include <hip/hip_bf16.h>

typedef unsigned int u32;
typedef unsigned short u16;
typedef u32 v4u __attribute__((ext_vector_type(4)));
typedef u32 v2u __attribute__((ext_vector_type(2)));
typedef float f32x2 __attribute__((ext_vector_type(2)));
typedef float f32x4 __attribute__((ext_vector_type(4)));
typedef float f32x16 __attribute__((ext_vector_type(16)));
typedef short s16x4 __attribute__((ext_vector_type(4)));
typedef short s16x8 __attribute__((ext_vector_type(8)));
typedef __bf16 bf16x2 __attribute__((ext_vector_type(2)));
#define GAS __attribute__((address_space(1)))
#define LAS __attribute__((address_space(3)))
#define DEV __device__ __forceinline__
#define RLX_AGENT __ATOMIC_RELAXED, __HIP_MEMORY_SCOPE_AGENT
#define SC1 16
#define TS(i) do { SCHED(); ts[i] = (u32)__builtin_amdgcn_s_memrealtime(); SCHED(); } while (0)
#define TC(i) do { SCHED(); unsigned long long tt_; asm volatile("s_memtime %0\n\ts_waitcnt lgkmcnt(0)" : "=s"(tt_)::"memory"); tc[i] = (u32)tt_; SCHED(); } while (0)

constexpr int WS_E = 0;
constexpr int FL = 32;
constexpr int WS_STAT = 4096;
constexpr int WS_FXN = 65536;
constexpr int WS_FW2 = WS_FXN + 256 * 128;
constexpr int WS_FH = WS_FW2 + 1024 * 128;
constexpr int WS_FS = WS_FH + 1024 * 128;
constexpr int WS_XNT = 524288;
constexpr int WS_W2T = WS_XNT + 2097152;
constexpr int WS_HT = WS_W2T + 1835008;
constexpr int WS_ST = WS_HT + 7340032;
constexpr int WS_END = WS_ST + 2097152;
constexpr int LD_Q = 98304;
constexpr int LD_KREG = 131072;
constexpr int LD_PS = LD_KREG + 7424;
constexpr int LD_DUMMY = LD_KREG + 7680;
constexpr int LD_B1 = LD_KREG + 8192 + 7424;
constexpr int LD_HST = LD_KREG + 3 * 8192 + 7424;
constexpr int LD_STAT = 12288;
constexpr int LD_U = 0;
constexpr int LD_CX = 16384;
constexpr int LD_HX = 32768;
constexpr int LD_RED = 0;
constexpr int LD_ST = 114688;
constexpr int ABL_HT = WS_HT;
constexpr int LDS_BYTES = 163840;
constexpr u32 SPIN_CAP = 1u << 16;
constexpr u32 CODE_BYTES = $$$$$$$$$$$$$$$$$$$$$$$$$$$$$$$$$$$$$$$$$$$$$$;

DEV float bf2f(u32 h) { return __builtin_bit_cast(float, h << 16); }
DEV float bflo(u32 d) { return __builtin_bit_cast(float, d << 16); }
DEV float bfhi(u32 d) { return __builtin_bit_cast(float, d & 0xffff0000u); }
DEV u32 pk2(float lo, float hi) {
  f32x2 v = {lo, hi};
  return __builtin_bit_cast(u32, __builtin_convertvector(v, bf16x2));
}
DEV u32 pk2v(f32x2 v) { return __builtin_bit_cast(u32, __builtin_convertvector(v, bf16x2)); }
DEV float bfr(float v) { return bflo(pk2(v, v)); }
DEV f32x4 mfma16(v4u a, v4u b, f32x4 c) {
  return __builtin_amdgcn_mfma_f32_16x16x32_bf16(__builtin_bit_cast(s16x8, a), __builtin_bit_cast(s16x8, b), c, 0, 0, 0);
}
DEV f32x16 mfma32(v4u a, v4u b, f32x16 c) {
  return __builtin_amdgcn_mfma_f32_32x32x16_bf16(__builtin_bit_cast(s16x8, a), __builtin_bit_cast(s16x8, b), c, 0, 0, 0);
}
DEV v2u trread(LAS char* p) { return __builtin_bit_cast(v2u, __builtin_amdgcn_ds_read_tr16_b64_v4i16((LAS s16x4*)p)); }
DEV v4u mk_rsrc(const void* p, u32 bytes, u32 fmt) {
  const unsigned long long a = (unsigned long long)p;
  v4u r = {(u32)a, (u32)(a >> 32), bytes, fmt};
  return r;
}
DEV void dma16(u32 voff, v4u rs, u32 soff, u32 lds) {
  asm volatile("s_mov_b32 m0, %0\n\ts_nop 0\n\tbuffer_load_dwordx4 %1, %2, %3 offen lds" ::"s"(lds), "v"(voff), "s"(rs), "s"(soff) : "memory");
}
#define WAITV(n) asm volatile("s_waitcnt vmcnt(" #n ")" ::: "memory")
#define WAITL() asm volatile("s_waitcnt lgkmcnt(0)" ::: "memory")
#define SCHED() __builtin_amdgcn_sched_barrier(0)
#define BAR() do { asm volatile("s_waitcnt lgkmcnt(0)" ::: "memory"); __builtin_amdgcn_s_barrier(); asm volatile("" ::: "memory"); SCHED(); } while (0)
#define ORDER_BEHIND_POLL() __builtin_amdgcn_fence(__ATOMIC_ACQUIRE, "wavefront")

DEV f32x2 cmul(f32x2 a, f32x2 z) {
  const f32x2 zr = {-z.y, z.x};
  f32x2 t, r;
  asm("v_pk_mul_f32 %0, %1, %2 op_sel:[1,0] op_sel_hi:[1,1]" : "=v"(t) : "v"(a), "v"(zr));
  asm("v_pk_fma_f32 %0, %1, %2, %3 op_sel_hi:[0,1,1]" : "=v"(r) : "v"(a), "v"(z), "v"(t));
  return r;
}
DEV float sadd(float a, float b) { float r; asm("v_add_f32 %0, %1, %2" : "=v"(r) : "v"(a), "v"(b)); return r; }
DEV float smul(float a, float b) { float r; asm("v_mul_f32 %0, %1, %2" : "=v"(r) : "v"(a), "v"(b)); return r; }
DEV f32x2 zpow(float la, float th, float k) {
  const float mag = __builtin_amdgcn_exp2f(la * k);
  const float fr = __builtin_amdgcn_fractf(th * k);
  f32x2 r = {mag * __builtin_amdgcn_cosf(fr), mag * __builtin_amdgcn_sinf(fr)};
  return r;
}
template <int SLEEP>
DEV void poll_flags(GAS u32* p, u32 epoch, u32 first) {
  if (!__all(first == epoch)) {
#pragma unroll 1
    for (u32 spins = 0; spins < SPIN_CAP; ++spins) {
      const u32 v = __hip_atomic_load(p, RLX_AGENT);
      if (__all(v == epoch)) break;
      __builtin_amdgcn_s_sleep(SLEEP);
    }
  }
  ORDER_BEHIND_POLL();
}
DEV void publish(GAS u32* p, u32 epoch, int lane) {
  WAITV(0);
  if (lane == 0) __hip_atomic_store(p, epoch, RLX_AGENT);
}
DEV void set_progress(LAS char* L, int wv, u32 k) {
  WAITL();
  *(volatile LAS u32*)(L + LD_PS + wv * 4) = k;
}
DEV void wait_progress(LAS char* L, int word, u32 k) {
#pragma unroll 1
  for (u32 spins = 0; spins < (1u << 22); ++spins) {
    const u32 pv = *(volatile LAS u32*)(L + LD_PS + word * 4);
    if (__all(pv >= k)) break;
    __builtin_amdgcn_s_sleep(1);
  }
  asm volatile("" ::: "memory");
}
DEV void ln_stats(float s1, float s2, float& mean, float& var, float& den1) {
  mean = s1 * (1.0f / 512.0f);
  var = fmaxf(s2 * (1.0f / 512.0f) - mean * mean, 0.0f);
  den1 = bfr(__builtin_amdgcn_sqrtf(bfr(bfr(var) + 1e-5f)));
}
DEV float pick4(f32x4 g, int k) { return k == 0 ? g.x : k == 1 ? g.y : k == 2 ? g.z : g.w; }
#define LD16(p, i) ((u32)__hip_atomic_load((const GAS u16*)(p) + (i), __ATOMIC_RELAXED, __HIP_MEMORY_SCOPE_WAVEFRONT))

__global__ void __launch_bounds__(512, 2)
s4_mixer_kernel(const u16* __restrict__ x, u32 x_bytes, u32 x_fmt, const u16* __restrict__ w1, u32 w1_bytes, u32 w1_fmt,
                const u16* __restrict__ w2, char* __restrict__ ws,
                const u16* __restrict__ A_re, const u16* __restrict__ A_im, const u16* __restrict__ B_re,
                const u16* __restrict__ B_im, const u16* __restrict__ C_re, const u16* __restrict__ C_im,
                const u16* __restrict__ log_dt, const u16* __restrict__ Dskip, const u16* __restrict__ b1,
                const u16* __restrict__ b2, u16* __restrict__ out) {
  extern __shared__ __attribute__((aligned(16))) char smem[];
  LAS char* const L = (LAS char*)smem;
  unsigned long long pc;
  asm volatile("s_getpc_b64 %0" : "=s"(pc));
  {
    u32 coff = ((((threadIdx.x & 63) << 3) | (threadIdx.x >> 6)) * 32u + (blockIdx.x >> 3)) * 64u;
    coff = coff < CODE_BYTES ? coff : 0u;
    asm volatile("s_mov_b32 m0, %0\n\ts_nop 0\n\tglobal_load_lds_dword %1, %2" ::"s"((u32)LD_DUMMY), "v"(coff), "s"(pc) : "memory");
    SCHED();
  }
  const int tid = threadIdx.x, lane = tid & 63;
  const int wv = __builtin_amdgcn_readfirstlane(tid >> 6);
  const int w = wv & 3;
  const bool swave = wv >= 4;
  const int b = blockIdx.x, xcd = b & 7, idx = b >> 3;
  const int r16 = lane & 15, q = lane >> 4, qq = r16 >> 2, p4 = lane & 3;
  const bool helper = idx >= 28;
  u32 ts[16], tc[16];
#pragma unroll
  for (int i = 0; i < 16; ++i) { ts[i] = 0; tc[i] = 0; }
  TS(0);
  const u32 cyc0 = (u32)__builtin_amdgcn_s_memtime();
  const v4u rsX = mk_rsrc(x, x_bytes, x_fmt);
  const v4u rsW1 = mk_rsrc(w1, w1_bytes, w1_fmt);
  asm volatile("" ::"s"(rsX), "s"(rsW1), "s"(w2), "s"(ws), "s"(A_re), "s"(A_im), "s"(B_re), "s"(B_im), "s"(C_re), "s"(C_im), "s"(log_dt), "s"(Dskip), "s"(b1));
  u32 eraw;
  asm volatile("s_load_dword %0, %1, 0x0" : "=s"(eraw) : "s"(ws));
  const int mq = (idx * 37) >> 8, mr = idx - 7 * mq;
  const int mt = (xcd >> 1) * 4 + mq, nt = (xcd & 1) * 7 + mr;
  const int hid = xcd * 4 + (idx - 28);
  const int tg = (xcd >> 1) * 8 + (idx >> 2), cg = (xcd & 1) * 4 + (idx & 3);
  const int cw = w >> 1, hw = w & 1;
  const int ch = 64 * xcd + 2 * idx + cw;
  const __amdgpu_buffer_rsrc_t rsWS = __builtin_amdgcn_make_buffer_rsrc((void*)ws, 0, WS_END, 0x00020000);
  GAS u32* const ctl = (GAS u32*)ws;
  const v4u ones = {0x3f803f80u, 0x3f803f80u, 0x3f803f80u, 0x3f803f80u};
  int rot2 = (tg & 7) + 2 * (cg & 3);
  rot2 -= rot2 >= 7 ? 7 : 0;
#define KS2(s) (7 * wv + ((s) + rot2 >= 7 ? (s) + rot2 - 7 : (s) + rot2))
#define AW_LOAD(s, c) \
  aw[s][c] = __builtin_amdgcn_raw_buffer_load_b128(rsWS, lane * 16 + ((((c) + 2 * (wv & 1)) & 3) << 10), WS_W2T + (cg * 56 + KS2(s)) * 4096, SC1)
  const u32 wo = (u32)lane * 4u;
#define TW(rowp, row, val) *(LAS u32*)((rowp) + (wo ^ (u32)(((row) & 15) << 4))) = (val)
  LAS char* const PT = L + LD_KREG + w * 8192;

  auto gemm2_tail = [&](v4u (&aw)[7][4], const u32 epoch, GAS u32* const pend, u32 fh0) __attribute__((always_inline)) {
  {
    TS(9);
    int tz = threadIdx.x;
    asm volatile("" : "+v"(tz));
    const int lane = tz & 63, r16 = lane & 15, q = lane >> 4, qq = r16 >> 2, p4 = lane & 3;
    asm volatile("" : "+v"(fh0));
    poll_flags<2>(ctl + WS_FH / 4 + (tg * 32 + ((7 * wv) >> 1) + (lane & 3)) * FL, epoch, fh0);
    TS(10);
    constexpr int WIN = 4;
    v4u hf[7][4];
#define H_LOAD(s) _Pragma("unroll") for (int j = 0; j < 4; ++j) \
      hf[s][j] = __builtin_amdgcn_raw_buffer_load_b128(rsWS, lane * 16 + (((j + (wv >> 1)) & 3) << 10), WS_HT + (tg * 56 + KS2(s)) * 4096, SC1);
#pragma unroll
    for (int s = 0; s < WIN; ++s) { H_LOAD(s) SCHED(); }
    if (pend) {
      WAITV(16);
      if (lane == 0) __hip_atomic_store(pend, epoch, RLX_AGENT);
      SCHED();
    }
    GAS u32* const fsp = ctl + WS_FS / 4 + ((tg >> 4) * 512 + cg * 64 + wv * 8 + (lane >> 3)) * FL;
    const int tok = tg * 64 + 16 * (wv >> 1) + r16;
    const int c8 = cg * 64 + 32 * (wv & 1) + 8 * q;
    u32 fs, fe0 = epoch, fe1 = epoch;
    bool early;
    v4u xv, bv, sv;
    f32x4 acc[4][4];
#pragma unroll
    for (int i = 0; i < 4; ++i)
#pragma unroll
      for (int j = 0; j < 4; ++j) acc[i][j] = f32x4{0.f, 0.f, 0.f, 0.f};
#pragma unroll
    for (int s = 0; s < 7; ++s) {
#pragma unroll
      for (int j = 0; j < 4; ++j)
#pragma unroll
        for (int c = 0; c < 4; ++c) acc[c][j] = mfma16(aw[s][c], hf[s][j], acc[c][j]);
      SCHED();
      if (s == 2) fs = __hip_atomic_load(fsp, RLX_AGENT);
      if (s + WIN < 7) { H_LOAD(s + WIN) }
      if (s == 2) {
        xv = *(const GAS v4u*)(x + tok * 512 + c8);
        bv = *(const GAS v4u*)(b2 + c8);
      }
      if (s == 5) {
        early = __all(fs == epoch);
        ORDER_BEHIND_POLL();
        sv = __builtin_amdgcn_raw_buffer_load_b128(rsWS, (lane >> 3) * 4096 + (lane & 7) * 16, early ? WS_ST + (cg * 64 + wv * 8) * 4096 + tg * 128 : WS_END, SC1);
        if (b == 0) {
          fe0 = __hip_atomic_load(ctl + WS_FS / 4 + tz * FL, RLX_AGENT);
          fe1 = __hip_atomic_load(ctl + WS_FS / 4 + (512 + tz) * FL, RLX_AGENT);
        }
      }
      SCHED();
    }
    TS(11);
    wait_progress(L, lane & 7, 4u);
#pragma unroll
    for (int j = 0; j < 4; ++j)
#pragma unroll
      for (int cp = 0; cp < 2; ++cp)
        if (j + cp > 0) {
          const int d = (((j + (wv >> 1)) & 3) << 1) | ((cp + wv) & 1);
#pragma unroll
          for (int e = 0; e < 2; ++e)
            *(LAS f32x4*)(L + LD_RED + (d * 7 + 2 * j + cp - 1) * 2048 + e * 1024 + lane * 16) = acc[2 * cp + e][j];
        }
    if (!early) {
      poll_flags<2>(fsp, epoch, epoch + 1u);
      sv = __builtin_amdgcn_raw_buffer_load_b128(rsWS, (lane >> 3) * 4096 + (lane & 7) * 16, WS_ST + (cg * 64 + wv * 8) * 4096 + tg * 128, SC1);
    }
    *(LAS v4u*)(L + LD_ST + wv * 1024 + lane * 16) = sv;
    TS(12);
    asm volatile("" : "+v"(fe0), "+v"(fe1));
    poll_flags<2>(ctl + WS_FS / 4 + tz * FL, epoch, fe0);
    poll_flags<2>(ctl + WS_FS / 4 + (512 + tz) * FL, epoch, fe1);
    BAR();
    if (b == 0 && tz == 0) __hip_atomic_store(ctl + WS_E / 4, epoch, RLX_AGENT);
    TS(13);
    f32x4 part[2][7];
    v2u sq[2];
#pragma unroll
    for (int e = 0; e < 2; ++e) {
#pragma unroll
      for (int j = 0; j < 7; ++j) part[e][j] = *(LAS f32x4*)(L + LD_RED + (wv * 7 + j) * 2048 + e * 1024 + lane * 16);
      sq[e] = trread(L + LD_ST + (32 * (wv & 1) + 8 * q + 4 * e + qq) * 128 + (16 * (wv >> 1) + 4 * p4) * 2);
    }
    SCHED();
    u32 o[4];
#pragma unroll
    for (int e = 0; e < 2; ++e) {
      const u32 ba = bv[2 * e], bb = bv[2 * e + 1], xa = xv[2 * e], xb = xv[2 * e + 1];
      f32x4 m = acc[e][0] + f32x4{bflo(ba), bfhi(ba), bflo(bb), bfhi(bb)};
#pragma unroll
      for (int j = 0; j < 7; ++j) m += part[e][j];
      f32x4 g;
#pragma unroll
      for (int k = 0; k < 4; ++k) g[k] = m[k] * __builtin_amdgcn_rcpf(1.0f + __builtin_amdgcn_exp2f(m[k] * -1.4426950408889634f));
      o[2 * e] = pk2(bflo(xa) + bflo(sq[e].x) * g.x, bfhi(xa) + bfhi(sq[e].x) * g.y);
      o[2 * e + 1] = pk2(bflo(xb) + bflo(sq[e].y) * g.z, bfhi(xb) + bfhi(sq[e].y) * g.w);
    }
    *(GAS v4u*)(out + tok * 512 + c8) = v4u{o[0], o[1], o[2], o[3]};
  }
  TS(14);
  WAITV(0);
  TS(15);
  const u32 cyc1 = (u32)__builtin_amdgcn_s_memtime();
  if (b == 40 && wv == 0) { u32 X = 0u; X = X > 5000u ? 0u : X; const u32 te = ts[15] + 4u * X; while ((int)((u32)__builtin_amdgcn_s_memrealtime() - te) < 0) __builtin_amdgcn_s_sleep(2); }
  if (lane == 0) *(GAS u32*)(ws + (33 << 20) + (b * 8 + wv) * 4) = cyc1 - cyc0;
  if (lane == 0) {
#pragma unroll
    for (int i = 0; i < 16; ++i) *(GAS u32*)(ws + (32 << 20) + ((b * 8 + wv) * 16 + i) * 4) = ts[i];
#pragma unroll
    for (int i = 0; i < 16; ++i) *(GAS u32*)(ws + (34 << 20) + ((b * 8 + wv) * 16 + i) * 4) = tc[i];
  }
  __builtin_amdgcn_endpgm();
  };

  if (helper) {
    LAS char* const HX = L + LD_HX + wv * 8192;
#pragma unroll
    for (int p = 0; p < 8; ++p)
      dma16((u32)(wv * 8192 + ((lane ^ (((p & 3) << 2) | (p >> 2))) << 4)), rsX, (u32)((hid * 64 + p) * 1024), (u32)(LD_HX + (wv * 8 + p) * 1024));
    u32 epoch;
    asm volatile("s_waitcnt vmcnt(0) lgkmcnt(0)\n\ts_add_u32 %0, %1, 1" : "=s"(epoch) : "s"(eraw) : "memory", "scc");
    if (!swave) TS(3);
    f32x4 gram[2], sumx[2];
#pragma unroll
    for (int i = 0; i < 2; ++i) { gram[i] = f32x4{0.f, 0.f, 0.f, 0.f}; sumx[i] = gram[i]; }
    const int r8 = lane & 7, fr = ((r8 & 3) << 2) | (r8 >> 2);
    v4u xr[8];
#pragma unroll
    for (int s = 0; s < 8; ++s) xr[s] = *(LAS v4u*)(HX + r8 * 1024 + (lane & 8) * 64 + (((4 * s + q) ^ fr) << 4));
    v2u d[8][2];
#pragma unroll
    for (int s = 0; s < 8; ++s)
#pragma unroll
      for (int t = 0; t < 2; ++t)
        d[s][t] = trread(HX + (4 * t + qq) * 1024 + (((8 * s + 2 * q + (p4 >> 1)) ^ ((qq << 2) | t)) << 4) + 8 * (p4 & 1));
    SCHED();
#pragma unroll
    for (int s = 0; s < 8; ++s) {
      gram[s & 1] = mfma16(xr[s], xr[s], gram[s & 1]);
      sumx[s & 1] = mfma16(ones, xr[s], sumx[s & 1]);
    }
    float s1 = sumx[0].x + sumx[1].x, s2 = pick4(gram[0] + gram[1], r16 & 3);
    s1 += __shfl(s1, (lane + 40) & 63);
    s2 += __shfl(s2, (lane + 40) & 63);
    float mean, var, den1;
    ln_stats(s1, s2, mean, var, den1);
    const float rd = __builtin_amdgcn_rcpf(den1);
    const float den2 = bfr(__builtin_amdgcn_sqrtf(bfr(bfr(var * rd * rd) + 1e-5f)));
    const float R = __builtin_amdgcn_rcpf(den1 * den2);
    if (q == qq && r16 < 8) {
      *(LAS f32x2*)(L + LD_HST + (8 * wv + r16) * 8) = f32x2{rd, -bfr(mean) * rd};
      __builtin_amdgcn_raw_buffer_store_b128(v4u{__builtin_bit_cast(u32, R), epoch, __builtin_bit_cast(u32, R * mean), epoch}, rsWS,
                                             WS_STAT + (hid * 64 + 8 * wv + r16) * 16, 0, SC1);
    }
    WAITL();
    if (!swave) TS(4);
    f32x4 sv[4];
#pragma unroll
    for (int i = 0; i < 4; ++i) sv[i] = *(LAS f32x4*)(L + LD_HST + wv * 64 + i * 16);
#pragma unroll
    for (int cb = 0; cb < 8; ++cb) {
      v4u o;
      o.x = pk2(bflo(d[cb][0].x) * sv[0].x + sv[0].y, bfhi(d[cb][0].x) * sv[0].z + sv[0].w);
      o.y = pk2(bflo(d[cb][0].y) * sv[1].x + sv[1].y, bfhi(d[cb][0].y) * sv[1].z + sv[1].w);
      o.z = pk2(bflo(d[cb][1].x) * sv[2].x + sv[2].y, bfhi(d[cb][1].x) * sv[2].z + sv[2].w);
      o.w = pk2(bflo(d[cb][1].y) * sv[3].x + sv[3].y, bfhi(d[cb][1].y) * sv[3].z + sv[3].w);
      __builtin_amdgcn_raw_buffer_store_b128(o, rsWS, lane * 16 + WS_XNT + (hid * 8 + wv) * 8192 + cb * 1024, 0, SC1);
    }
    if (!swave) TS(5);
    WAITV(0);
    BAR();
    if (tid == 0) __hip_atomic_store(ctl + WS_FXN / 4 + hid * FL, epoch, RLX_AGENT);
    if (!swave) TS(6);
    SCHED();
  }

  if (swave) {
    __builtin_amdgcn_s_setprio(2);
    const u32 voX = (u32)((32 * w + (lane >> 3)) * 1024 + (((lane & 7) ^ (lane >> 3)) << 4));
    const u32 voW = (u32)((4 * w + q) * 3584 + ((r16 ^ ((q << 2) | w)) << 4));
    const int rot = (mr + 2 * mq) & 7;
#define G1_STAGE(st) G1_PART(st, 0, 4)
#define G1_PART(st, p0, p1)                                                                                  \
  {                                                                                                          \
    const int sa_ = ((st) + rot) & 7;                                                                        \
    _Pragma("unroll") for (int p = p0; p < p1; ++p) {                                                        \
      dma16(voX, rsX, (u32)(mt * 131072 + p * 8192 + sa_ * 128), (u32)(((st) & 3) * 32768 + w * 4096 + p * 1024)); \
      dma16(voW, rsW1, (u32)((sa_ * 64 + 16 * p) * 3584 + nt * 256), (u32)(((st) & 3) * 32768 + 16384 + p * 4096 + w * 1024)); \
    }                                                                                                        \
  }
    if (!helper) { G1_STAGE(0) G1_STAGE(1) G1_PART(2, 0, 2) }
    if (lane == 0) *(LAS u32*)(L + LD_PS + wv * 4) = 0u;
    TS(1);
    WAITV(12);
    u32 epoch;
    asm volatile("s_waitcnt lgkmcnt(0)\n\ts_add_u32 %0, %1, 1" : "=s"(epoch) : "s"(eraw) : "memory", "scc");
    TS(2);
    BAR();
    TS(3);
    TC(0);
    if (!helper) G1_PART(2, 2, 4)
    const int r32 = lane & 31, hh = lane >> 5;
    v4u pf[8], qf[8];
    {
#pragma unroll
      for (int s = 0; s < 8; ++s) {
        qf[s] = *(LAS v4u*)(L + LD_Q + w * 8192 + r32 * 256 + (((2 * s + hh) ^ (r32 & 15)) << 4));
        pf[s] = *(LAS v4u*)(PT + (2 * s + hh) * 464 + r32 * 16);
      }
    }
    SCHED();
    TC(1);
    if (!helper) {
      WAITV(8); BAR(); TC(2); G1_STAGE(3) G1_PART(4, 0, 2) TC(3);
      WAITV(12); BAR(); TC(4); G1_PART(4, 2, 4) G1_STAGE(5) TC(5);
      WAITV(16); BAR(); TC(6);
      G1_STAGE(6) TC(7);
      WAITV(16); BAR(); TC(8); G1_STAGE(7) TC(9);
      WAITV(16); BAR();
    }
    TC(10);
    GAS u32* const fxnp = ctl + WS_FXN / 4 + (hw * 16 + r16) * FL;
    GAS u32* const fw2p = ctl + WS_FW2 / 4 + ((cg * 56 + 7 * (w + 4 * ((lane >> 4) & 1))) * 2 + min(r16, 13)) * FL;
    const int sto = WS_STAT + ((helper ? 0 : mt) * 128 + 32 * w + r32) * 16;
    v4u sg = __builtin_amdgcn_raw_buffer_load_b128(rsWS, sto, 0, SC1);
    const u32 fw2 = __hip_atomic_load(fw2p, RLX_AGENT);
    const u32 pDs = LD16(Dskip, ch);
    SCHED();
    f32x16 kacc;
#pragma unroll
    for (int i = 0; i < 16; ++i) kacc[i] = 0.f;
#pragma unroll
    for (int s = 0; s < 8; ++s) kacc = mfma32(pf[s], qf[s], kacc);
    SCHED();
    TC(11);
    if (!helper) { WAITV(3); BAR(); }
    TC(12);
    TS(4);
#pragma unroll
    for (int i = 0; i < 4; ++i)
      *(LAS f32x4*)(L + LD_CX + w * 4096 + i * 1024 + lane * 16) = f32x4{kacc[4 * i], kacc[4 * i + 1], kacc[4 * i + 2], kacc[4 * i + 3]};
    if (!helper) {
      if (!__all(sg.y == epoch && sg.w == epoch)) {
#pragma unroll 1
        for (u32 spins = 0; spins < SPIN_CAP; ++spins) {
          __builtin_amdgcn_s_sleep(4);
          asm volatile("" ::: "memory");
          sg = __builtin_amdgcn_raw_buffer_load_b128(rsWS, sto, 0, SC1);
          if (__all(sg.y == epoch && sg.w == epoch)) break;
        }
      }
      *(LAS v2u*)(L + LD_STAT + (32 * w + r32) * 8) = v2u{sg.x, sg.z};
    }
    poll_flags<4>(fw2p, epoch, fw2);
    set_progress(L, wv, 1u);
    TS(5);
    u32 fxa = __hip_atomic_load(fxnp, RLX_AGENT);
    v4u aw[7][4];
#pragma unroll
    for (int c = 0; c < 12; ++c) AW_LOAD(c >> 2, c & 3);
    SCHED();
    wait_progress(L, wv ^ 1, 1u);
    {
      f32x4 v[4];
#pragma unroll
      for (int i = 0; i < 4; ++i) v[i] = *(LAS f32x4*)(L + LD_CX + (w ^ 1) * 4096 + i * 1024 + lane * 16);
#pragma unroll
      for (int i = 0; i < 15; ++i) {
        kacc[i] = sadd(kacc[i], v[i >> 2][i & 3]);
      }
    }
    {
      const bool odd = r32 & 1, up = hh != 0;
      float e[4][9];
      const int ia[4][9] = {{0, 1, 2, 3, 4, 5, 6, 7, 8}, {9, 9, 9, 9, -1, 10, 10, 10, 10}, {11, 11, 11, 11, -1, 12, 12, 12, 12}, {13, 13, 13, 13, -1, 14, 14, 14, 14}};
      const int ib[4][9] = {{0, 0, 1, 2, 2, 2, 3, 4, 4}, {5, 5, 5, 6, 6, 6, 7, 7, 7}, {8, 8, 8, 9, 9, 9, 10, 10, 10}, {11, 11, 11, 11, -1, 12, 12, 12, 12}};
#pragma unroll
      for (int u = 0; u < 4; ++u)
#pragma unroll
        for (int i = 0; i < 9; ++i) {
          const float va = ia[u][i] >= 0 ? kacc[ia[u][i]] : (odd ? kacc[ia[u][5]] : kacc[ia[u][3]]);
          const float vb = ib[u][i] >= 0 ? kacc[ib[u][i]] : (odd ? kacc[ib[u][5]] : kacc[ib[u][3]]);
          e[u][i] = up ? vb : va;
        }
      LAS char* const KC = PT + hw * 64 + (255 - r32) * 16;
#pragma unroll
      for (int u = 0; u < 4; ++u) {
        v4u d;
        if (hw) { d.x = pk2(e[u][8], e[u][7]); d.y = pk2(e[u][6], e[u][5]); d.z = pk2(e[u][4], e[u][3]); d.w = pk2(e[u][2], e[u][1]); }
        else    { d.x = pk2(e[u][7], e[u][6]); d.y = pk2(e[u][5], e[u][4]); d.z = pk2(e[u][3], e[u][2]); d.w = pk2(e[u][1], e[u][0]); }
        const int sa = u == 0 ? 0 : u + 3, sb = u == 3 ? 7 : u + 1;
        *(LAS v4u*)(KC - (up ? sb : sa) * 512) = d;
      }
      if (lane < 2) *(LAS v4u*)(PT + hw * 64 + 4096 + lane * 16) = v4u{(hw && lane == 0) ? pk2(kacc[0], 0.f) : 0u, 0u, 0u, 0u};
    }
    SCHED();
    TC(13);
    asm volatile("" : "+v"(fxa));
    poll_flags<8>(fxnp, epoch, fxa);
    const v4u u0 = __builtin_amdgcn_raw_buffer_load_b128(rsWS, lane * 8192, WS_XNT + (128 * hw) * 8192 + ch * 16, SC1);
    const v4u u1 = __builtin_amdgcn_raw_buffer_load_b128(rsWS, lane * 8192, WS_XNT + (128 * hw + 64) * 8192 + ch * 16, SC1);
#pragma unroll
    for (int c = 12; c < 20; ++c) AW_LOAD(c >> 2, c & 3);
    SCHED();
    LAS char* const UB = L + LD_U + cw * 4608;
    if (hw == 0 && lane < 32) *(LAS v4u*)(UB + lane * 16) = v4u{0u, 0u, 0u, 0u};
    *(LAS v4u*)(UB + 512 + (128 * hw + lane) * 16) = u0;
    *(LAS v4u*)(UB + 512 + (128 * hw + 64 + lane) * 16) = u1;
    set_progress(L, wv, 2u);
    SCHED();
#pragma unroll
    for (int c = 20; c < 28; ++c) AW_LOAD(c >> 2, c & 3);
    {
      const u32 po = (u32)(WS_W2T + (cg * 56 + 7 * w) * 4096 + ((tg & 7) * 28 + min(lane, 27)) * 128);
      asm volatile("s_mov_b32 m0, %0\n\ts_nop 0\n\tglobal_load_lds_dword %1, %2 sc1" ::"s"((u32)LD_DUMMY), "v"(po), "s"(ws) : "memory");
    }
    SCHED();
    TS(6);
    wait_progress(L, wv ^ 1, 2u);
    wait_progress(L, w, 4u);
    TS(7);
    TC(14);
    const float dskip = bf2f(pDs);
    const int sgm = 1 - (r16 & 1);
    LAS char* const ka = L + LD_KREG + (2 * cw + sgm) * 8192 + sgm * 64 + 2 * (2031 - r16 + 8 * q + sgm) - hw * 256 - 7 * 512 - 3 * 64;
    LAS char* const ub = UB + 2 * (256 - 16 + 16 * r16 + 8 * q) - hw * 256 - 3 * 64;
    f32x4 y[8];
#pragma unroll
    for (int i = 0; i < 8; ++i) y[i] = f32x4{0.f, 0.f, 0.f, 0.f};
    v4u af[4][8], bfv[4][8];
    u32 kb[4];
#pragma unroll
    for (int i = 0; i < 4; ++i) { kb[i] = (u32)(size_t)ka + i * 1024; asm volatile("" : "+v"(kb[i])); }
#define CV_READ(rr, g)                                                                                   \
  {                                                                                                      \
    LAS u32* pa = (LAS u32*)(size_t)(kb[(7 - (g)) >> 1] + ((7 - (g)) & 1) * 512 + (3 - (rr)) * 64);      \
    af[rr][g] = v4u{pa[0], pa[1], pa[2], pa[3]};                                                         \
    bfv[rr][g] = *(LAS v4u*)(ub + (g) * 512 + (3 - (rr)) * 64);                                          \
  }
    CV_READ(0, 0) CV_READ(0, 1) CV_READ(0, 2) CV_READ(0, 3) CV_READ(0, 4)
    SCHED();
#pragma unroll
    for (int rr = 0; rr < 4; ++rr) {
#pragma unroll
      for (int k = 0; k < 8; ++k) {
        if (k + 5 < 8) CV_READ(rr, k + 5)
        else if (rr < 3) CV_READ(rr + 1, k + 5 - 8)
#pragma unroll
        for (int o = 0; o <= k; ++o)
          if (k + o <= 7) {
            y[k + o] = mfma16(af[rr][k], bfv[rr][o], y[k + o]);
            if (o < k) y[k + o] = mfma16(af[rr][o], bfv[rr][k], y[k + o]);
          }
        SCHED();
      }
    }
    TC(15);
    const u32 fh0 = __hip_atomic_load(ctl + WS_FH / 4 + (tg * 32 + ((7 * wv) >> 1) + (lane & 3)) * FL, RLX_AGENT);
    SCHED();
    f32x4 mine[4];
    if (hw) {
#pragma unroll
      for (int i = 0; i < 4; ++i) { *(LAS f32x4*)(L + LD_CX + w * 4096 + i * 1024 + lane * 16) = y[i]; mine[i] = y[4 + i]; }
    } else {
#pragma unroll
      for (int i = 0; i < 4; ++i) { *(LAS f32x4*)(L + LD_CX + w * 4096 + i * 1024 + lane * 16) = y[4 + i]; mine[i] = y[i]; }
    }
    set_progress(L, wv, 3u);
    wait_progress(L, wv ^ 1, 3u);
    f32x4 oy[4];
    v2u xv[4];
#pragma unroll
    for (int i = 0; i < 4; ++i) {
      oy[i] = *(LAS f32x4*)(L + LD_CX + (w ^ 1) * 4096 + i * 1024 + lane * 16);
      xv[i] = *(LAS v2u*)(UB + 512 + (256 * (4 * hw + i) + 16 * r16 + 4 * q) * 2);
    }
    set_progress(L, wv, 4u);
#pragma unroll
    for (int i = 0; i < 4; ++i) {
      const u32 ya = pk2(sadd(mine[i].x, oy[i].x), sadd(mine[i].y, oy[i].y)), yb = pk2(sadd(mine[i].z, oy[i].z), sadd(mine[i].w, oy[i].w));
      const u32 da = pk2(smul(bflo(xv[i].x), dskip), smul(bfhi(xv[i].x), dskip)), db = pk2(smul(bflo(xv[i].y), dskip), smul(bfhi(xv[i].y), dskip));
      v2u o;
      o.x = pk2(sadd(bflo(ya), bflo(da)), sadd(bfhi(ya), bfhi(da)));
      o.y = pk2(sadd(bflo(yb), bflo(db)), sadd(bfhi(yb), bfhi(db)));
      __builtin_amdgcn_raw_buffer_store_b64(o, rsWS, (16 * r16 + 4 * q) * 2 + WS_ST + ch * 4096 + (4 * hw + i) * 512, 0, SC1);
    }
    TS(8);
    __builtin_amdgcn_s_setprio(0);
    gemm2_tail(aw, epoch, ctl + WS_FS / 4 + (hw * 512 + ch) * FL, fh0);
  } else {
    if (!helper) __builtin_amdgcn_s_setprio(3);
    const int po = ch * 128 + hw * 64 + lane;
    const u32 pLd = LD16(log_dt, ch), pAr = LD16(A_re, po), pAi = LD16(A_im, po);
    const u32 pBr = LD16(B_re, po), pBi = LD16(B_im, po), pCr = LD16(C_re, po), pCi = LD16(C_im, po);
    const u32 pB1 = LD16(b1, (helper ? 0 : nt * 128) + (tid & 127));
    const int uks = 2 * (helper ? 0 : idx) + (w >> 1), upp = w & 1;
    v4u w2a, w2b;
    {
      const GAS v4u* src = (const GAS v4u*)(w2 + (uks * 32 + (lane >> 2)) * 512 + xcd * 64 + upp * 32 + (lane & 3) * 8);
      asm volatile("" : "+v"(src));
      w2a = src[0];
      w2b = src[16 * 64];
    }
    if (lane == 0) *(LAS u32*)(L + LD_PS + wv * 4) = 0u;
    SCHED();
    TS(1);
    const int mi = w >> 1, ni = w & 1;
    f32x4 acc[4][4], csum[4];
#pragma unroll
    for (int i = 0; i < 4; ++i) {
#pragma unroll
      for (int j = 0; j < 4; ++j) { acc[i][j] = f32x4{0.f, 0.f, 0.f, 0.f}; asm volatile("" : "+v"(acc[i][j])); }
      csum[i] = f32x4{0.f, 0.f, 0.f, 0.f};
      asm volatile("" : "+v"(csum[i]));
    }
    u32 xb[2][2], wb[2][4][2];
#pragma unroll
    for (int h = 0; h < 2; ++h) {
#pragma unroll
      for (int k = 0; k < 2; ++k) {
        xb[h][k] = (u32)(((64 * mi + r16) * 128 + ((q ^ (r16 & 7)) << 4)) ^ (k * 64)) + h * 65536;
        asm volatile("" : "+v"(xb[h][k]));
      }
#pragma unroll
      for (int j = 0; j < 4; ++j)
#pragma unroll
        for (int t = 0; t < 2; ++t) {
          wb[h][j][t] = (u32)(16384 + (8 * q + 4 * t + qq) * 256 + (((2 * (4 * ni + j) + (p4 >> 1)) ^ ((qq << 2) | ((2 * q + t) & 3))) << 4) + 8 * (p4 & 1)) + h * 65536;
          asm volatile("" : "+v"(wb[h][j][t]));
        }
    }
    v4u onev = ones;
    asm volatile("" : "+v"(onev));
    SCHED();
    TC(0);
    u32 epoch;
    asm volatile("s_waitcnt lgkmcnt(0)\n\ts_add_u32 %0, %1, 1" : "=s"(epoch) : "s"(eraw) : "memory", "scc");
    SCHED();
    {
      const float dt = bfr(__builtin_amdgcn_exp2f(bf2f(pLd) * 1.4426950408889634f));
      const float la = dt * bf2f(pAr) * 1.4426950408889634f;
      const float th = dt * bf2f(pAi) * 0.15915494309189535f;
      const f32x2 dBc = {dt * bf2f(pBr), -dt * bf2f(pBi)};
      const f32x2 Ccc = {bf2f(pCr), -bf2f(pCi)};
      const f32x2 z8 = zpow(la, th, 8.f), z16 = zpow(la, th, 16.f);
      const f32x2 y1 = zpow(la, -th, 1.f), y256 = zpow(la, -th, 256.f), y8 = {z8.x, -z8.y};
      const f32x2 y2 = cmul(y1, y1), y4 = cmul(y2, y2);
#define PROW(h, g) (((g) & 3) + 8 * ((g) >> 2) + 4 * (h))
#define PW(h, g, v) *(LAS u32*)(PT + (lane >> 2) * 464 + (lane & 3) * 4 + PROW(h, g) * 16) = pk2v(v)
      f32x2 W[8];
      W[0] = cmul(Ccc, dBc);
#pragma unroll
      for (int c = 1; c < 8; ++c) W[c] = cmul(W[c - 1], y256);
      {
        f32x2 te = W[0], to = cmul(W[0], y1), t1 = W[1];
        PW(0, 0, te); PW(0, 1, to); PW(1, 0, t1);
#pragma unroll
        for (int a = 1; a <= 4; ++a) {
          te = cmul(te, y2); PW(0, 2 * a, te);
          if (a < 4) { to = cmul(to, y2); PW(0, 2 * a + 1, to); }
          t1 = cmul(t1, y2); PW(1, a, t1);
        }
      }
      PW(1, 5, W[2]); PW(1, 6, cmul(W[2], y4)); PW(1, 7, cmul(W[2], y8));
      PW(1, 8, W[3]); PW(1, 9, cmul(W[3], y4)); PW(1, 10, cmul(W[3], y8));
      PW(0, 9, W[4]); PW(0, 10, cmul(W[4], y8));
      PW(0, 11, W[5]); PW(0, 12, cmul(W[5], y8));
      PW(0, 13, W[6]); PW(0, 14, cmul(W[6], y8));
      PW(1, 11, W[7]); PW(1, 12, cmul(W[7], y8));
      SCHED();
      TC(2);
      LAS char* const QA = L + LD_Q + w * 8192;
      *(LAS v4u*)(QA + lane * 16) = w2a;
      *(LAS v4u*)(QA + 1024 + lane * 16) = w2b;
      v2u wt[2][2];
#pragma unroll
      for (int e = 0; e < 2; ++e)
#pragma unroll
        for (int t = 0; t < 2; ++t) wt[e][t] = trread(QA + (16 * t + 4 * q + qq) * 64 + 16 * p4 + 8 * e);
      asm volatile("" ::: "memory");
      TC(3);
      f32x2 qa = {1.f, 0.f}, qb = z8;
#pragma unroll
      for (int i = 0; i < 16; ++i) {
        TW(QA + 2 * i * 256, 2 * i, pk2v(qa));
        TW(QA + (2 * i + 1) * 256, 2 * i + 1, pk2v(qb));
        if (i < 15) { qa = (i == 0) ? z16 : cmul(qa, z16); qb = cmul(qb, z16); }
        if (i == 1) {
#pragma unroll
      for (int e = 0; e < 2; ++e)
        __builtin_amdgcn_raw_buffer_store_b128(v4u{wt[e][0].x, wt[e][0].y, wt[e][1].x, wt[e][1].y}, rsWS,
                                               lane * 16 + (helper ? WS_END : WS_W2T) + ((xcd * 56 + uks) * 4 + 2 * upp + e) * 1024, 0, SC1);
        }
      }
    }
    *(LAS float*)(L + LD_B1 + (tid & 127) * 4) = bf2f(pB1);
    TC(4);
    TS(2);
    v4u aw[7][4];
    if (!helper) {
      v4u xf[2][4], wf[2][4];
#define G1_READ(n)                                                                                            \
  _Pragma("unroll") for (int i = 0; i < 4; ++i) {                                                             \
    const v2u lo = trread(L + (((n) >> 1) & 1) * 32768 + ((n) & 1) * 8192 + wb[((n) >> 2) & 1][i][0]);        \
    const v2u hi = trread(L + (((n) >> 1) & 1) * 32768 + ((n) & 1) * 8192 + wb[((n) >> 2) & 1][i][1]);        \
    wf[(n) & 1][i] = v4u{lo.x, lo.y, hi.x, hi.y};                                                             \
    xf[(n) & 1][i] = *(LAS v4u*)(L + (((n) >> 1) & 1) * 32768 + i * 2048 + xb[((n) >> 2) & 1][(n) & 1]);      \
  }
      __builtin_amdgcn_s_setprio(0);
      TC(5);
      BAR();
      TC(6);
      TS(3);
      G1_READ(0)
      SCHED();
#pragma unroll 1
      for (int it = 0; it < 2; ++it) {
#pragma unroll
        for (int n = 0; n < 8; ++n) {
          if (n & 1) {
            if (n < 4 || it == 0) BAR();
            if (n == 3 && it == 0) {
              __builtin_amdgcn_s_setprio(3);
              publish(ctl + WS_FW2 / 4 + ((xcd * 56 + uks) * 2 + upp) * FL, epoch, lane);
              __builtin_amdgcn_s_setprio(0);
            }
            SCHED();
          }
          G1_READ(n + 1)
#pragma unroll
          for (int j = 0; j < 4; ++j) {
#pragma unroll
            for (int i = 0; i < 4; ++i) acc[j][i] = mfma16(wf[n & 1][j], xf[n & 1][i], acc[j][i]);
            csum[j] = mfma16(wf[n & 1][j], onev, csum[j]);
          }
#pragma unroll
          for (int k = 0; k < 12; ++k) {
            __builtin_amdgcn_sched_group_barrier(0x008, 1, 0);
            __builtin_amdgcn_sched_group_barrier(0x100, 1, 0);
          }
          __builtin_amdgcn_sched_group_barrier(0x008, 8, 0);
          SCHED();
        }
      }
      TS(4);
      __builtin_amdgcn_s_setprio(3);
      wait_progress(L, 4 + (lane & 3), 1u);
      TS(5);
      float rs[4], rm[4];
      f32x4 bb[4];
#pragma unroll
      for (int i = 0; i < 4; ++i) {
        const f32x2 st = *(LAS f32x2*)(L + LD_STAT + (64 * mi + 16 * i + r16) * 8);
        rs[i] = st.x; rm[i] = -st.y;
        asm volatile("" : "+v"(rm[i]));
        bb[i] = *(LAS f32x4*)(L + LD_B1 + (64 * ni + 16 * i + 4 * q) * 4);
      }
      SCHED();
#pragma unroll
      for (int jp = 0; jp < 2; ++jp) {
#pragma unroll
        for (int i = 0; i < 4; ++i) {
          TC(8 + jp * 4 + i);
          f32x2 pre[4], ee[4];
#pragma unroll
          for (int k = 0; k < 4; ++k) {
            const f32x4 a4 = acc[2 * jp + (k >> 1)][i], c4 = csum[2 * jp + (k >> 1)], b4 = bb[2 * jp + (k >> 1)];
            const f32x2 a2 = (k & 1) ? f32x2{a4.z, a4.w} : f32x2{a4.x, a4.y};
            const f32x2 c2 = (k & 1) ? f32x2{c4.z, c4.w} : f32x2{c4.x, c4.y};
            const f32x2 b2v = (k & 1) ? f32x2{b4.z, b4.w} : f32x2{b4.x, b4.y};
            pre[k] = a2 * rs[i] + (c2 * rm[i] + b2v);
            f32x2 arg = pre[k] * (pre[k] * pre[k] * -0.1029432397f + -2.302208198f);
            asm("" : "+v"(arg));
            ee[k] = f32x2{__builtin_amdgcn_exp2f(arg.x), __builtin_amdgcn_exp2f(arg.y)};
          }
          u32 o[4];
#pragma unroll
          for (int k = 0; k < 4; ++k) {
            f32x2 dn = ee[k] + 1.0f;
            asm("" : "+v"(dn));
            o[k] = pk2v(pre[k] * f32x2{__builtin_amdgcn_rcpf(dn.x), __builtin_amdgcn_rcpf(dn.y)});
          }
          __builtin_amdgcn_raw_buffer_store_b128(v4u{o[0], o[1], o[2], o[3]}, rsWS,
                                                 lane * 16 + ABL_HT + (((2 * mt + mi) * 56 + nt * 4 + 2 * ni + jp) * 4 + i) * 1024, 0, SC1);
          SCHED();
        }
      }
      set_progress(L, wv, 4u);
      __builtin_amdgcn_s_setprio(0);
      TS(6);
#pragma unroll
      for (int c = 0; c < 8; ++c) AW_LOAD(c >> 2, c & 3);
      SCHED();
      __builtin_amdgcn_s_setprio(3);
      WAITV(8);
      if (lane == 0) __hip_atomic_store(ctl + WS_FH / 4 + ((2 * mt + mi) * 32 + 2 * nt + ni) * FL, epoch, RLX_AGENT);
      __builtin_amdgcn_s_setprio(0);
      SCHED();
      TS(7);
#pragma unroll
      for (int c = 8; c < 28; ++c) AW_LOAD(c >> 2, c & 3);
      SCHED();
    } else {
      __builtin_amdgcn_s_setprio(0);
      BAR();
      set_progress(L, wv, 4u);
      wait_progress(L, 4 + (lane & 3), 1u);
#pragma unroll
      for (int s = 0; s < 7; ++s)
#pragma unroll
        for (int c = 0; c < 4; ++c) AW_LOAD(s, c);
      SCHED();
    }
    TS(8);
    gemm2_tail(aw, epoch, nullptr, epoch + 1u);
  }
}

__global__ void k_pre(u32* p) { __hip_atomic_store((GAS u32*)p, (u32)__builtin_amdgcn_s_memrealtime(), RLX_AGENT); }
__global__ void k_post(u32* p, u32 mul) { const u32 now = (u32)__builtin_amdgcn_s_memrealtime(); u32 X = now - __hip_atomic_load((GAS u32*)p, RLX_AGENT); X = X > 5000u ? 0u : X; const u32 te = now + mul * X;
  while ((int)((u32)__builtin_amdgcn_s_memrealtime() - te) < 0) __builtin_amdgcn_s_sleep(2); }
extern "C" void kernel_launch(void* const* d_in, const int* in_sizes, int n_in, void* d_out, int out_size, void* d_ws,
                              size_t ws_size, hipStream_t stream) {
  (void)in_sizes; (void)n_in; (void)out_size; (void)ws_size;
  (void)hipFuncSetAttribute((const void*)s4_mixer_kernel, hipFuncAttributeMaxDynamicSharedMemorySize, LDS_BYTES);
  s4_mixer_kernel<<<256, 512, LDS_BYTES, stream>>>(
      (const u16*)d_in[0], 2097152u, 0x00020000u, (const u16*)d_in[10], 1835008u, 0x00020000u, (const u16*)d_in[12], (char*)d_ws, (const u16*)d_in[1], (const u16*)d_in[2],
      (const u16*)d_in[3], (const u16*)d_in[4], (const u16*)d_in[5], (const u16*)d_in[6], (const u16*)d_in[7],
      (const u16*)d_in[8], (const u16*)d_in[11], (const u16*)d_in[13], (u16*)d_out);
  k_post<<<1, 1, 0, stream>>>((u32*)((char*)d_ws + (32 << 20) + (40 * 8 * 16 + 15) * 4), 0u);
}
```

```cpp
#include <hip/hip_runtime.h>
#include <hip/hip_bf16.h>

typedef unsigned int u32;
typedef unsigned short u16;
typedef u32 v4u __attribute__((ext_vector_type(4)));
typedef u32 v2u __attribute__((ext_vector_type(2)));
typedef float f32x2 __attribute__((ext_vector_type(2)));
typedef float f32x4 __attribute__((ext_vector_type(4)));
typedef float f32x16 __attribute__((ext_vector_type(16)));
typedef short s16x4 __attribute__((ext_vector_type(4)));
typedef short s16x8 __attribute__((ext_vector_type(8)));
typedef __bf16 bf16x2 __attribute__((ext_vector_type(2)));
#define GAS __attribute__((address_space(1)))
#define LAS __attribute__((address_space(3)))
#define DEV __device__ __forceinline__
#define RLX_AGENT __ATOMIC_RELAXED, __HIP_MEMORY_SCOPE_AGENT
#define SC1 16
#define TS(i) do { } while (0)
#define TC(i) do { } while (0)

constexpr int WS_E = 0;
constexpr int FL = 32;
constexpr int WS_STAT = 4096;
constexpr int WS_FXN = 65536;
constexpr int WS_FW2 = WS_FXN + 256 * 128;
constexpr int WS_FH = WS_FW2 + 1024 * 128;
constexpr int WS_FS = WS_FH + 1024 * 128;
constexpr int WS_XNT = 524288;
constexpr int WS_W2T = WS_XNT + 2097152;
constexpr int WS_HT = WS_W2T + 1835008;
constexpr int WS_ST = WS_HT + 7340032;
constexpr int WS_END = WS_ST + 2097152;
constexpr int LD_Q = 98304;
constexpr int LD_KREG = 131072;
constexpr int LD_PS = LD_KREG + 7424;
constexpr int LD_DUMMY = LD_KREG + 7680;
constexpr int LD_B1 = LD_KREG + 8192 + 7424;
constexpr int LD_HST = LD_KREG + 3 * 8192 + 7424;
constexpr int LD_STAT = 12288;
constexpr int LD_U = 0;
constexpr int LD_CX = 16384;
constexpr int LD_HX = 32768;
constexpr int LD_RED = 0;
constexpr int LD_ST = 114688;
constexpr int ABL_HT = WS_HT;
constexpr int LDS_BYTES = 163840;
constexpr u32 SPIN_CAP = 1u << 16;
constexpr u32 CODE_BYTES = $$$$$$$$$$$$$$$$$$$$$$$$$$$$$$$$$$$$$$$$$$$$$$$$;

DEV float bf2f(u32 h) { return __builtin_bit_cast(float, h << 16); }
DEV float bflo(u32 d) { return __builtin_bit_cast(float, d << 16); }
DEV float bfhi(u32 d) { return __builtin_bit_cast(float, d & 0xffff0000u); }
DEV u32 pk2(float lo, float hi) {
  f32x2 v = {lo, hi};
  return __builtin_bit_cast(u32, __builtin_convertvector(v, bf16x2));
}
DEV u32 pk2v(f32x2 v) { return __builtin_bit_cast(u32, __builtin_convertvector(v, bf16x2)); }
DEV float bfr(float v) { return bflo(pk2(v, v)); }
DEV f32x4 mfma16(v4u a, v4u b, f32x4 c) {
  return __builtin_amdgcn_mfma_f32_16x16x32_bf16(__builtin_bit_cast(s16x8, a), __builtin_bit_cast(s16x8, b), c, 0, 0, 0);
}
DEV f32x16 mfma32(v4u a, v4u b, f32x16 c) {
  return __builtin_amdgcn_mfma_f32_32x32x16_bf16(__builtin_bit_cast(s16x8, a), __builtin_bit_cast(s16x8, b), c, 0, 0, 0);
}
DEV v2u trread(LAS char* p) { return __builtin_bit_cast(v2u, __builtin_amdgcn_ds_read_tr16_b64_v4i16((LAS s16x4*)p)); }
DEV v4u mk_rsrc(const void* p, u32 bytes, u32 fmt) {
  const unsigned long long a = (unsigned long long)p;
  v4u r = {(u32)a, (u32)(a >> 32), bytes, fmt};
  return r;
}
DEV void dma16(u32 voff, v4u rs, u32 soff, u32 lds) {
  asm volatile("s_mov_b32 m0, %0\n\ts_nop 0\n\tbuffer_load_dwordx4 %1, %2, %3 offen lds" ::"s"(lds), "v"(voff), "s"(rs), "s"(soff) : "memory");
}
#define WAITV(n) asm volatile("s_waitcnt vmcnt(" #n ")" ::: "memory")
#define WAITL() asm volatile("s_waitcnt lgkmcnt(0)" ::: "memory")
#define SCHED() __builtin_amdgcn_sched_barrier(0)
#define BAR() do { asm volatile("s_waitcnt lgkmcnt(0)" ::: "memory"); __builtin_amdgcn_s_barrier(); asm volatile("" ::: "memory"); SCHED(); } while (0)
#define ORDER_BEHIND_POLL() __builtin_amdgcn_fence(__ATOMIC_ACQUIRE, "wavefront")

DEV f32x2 cmul(f32x2 a, f32x2 z) {
  const f32x2 zr = {-z.y, z.x};
  f32x2 t, r;
  asm("v_pk_mul_f32 %0, %1, %2 op_sel:[1,0] op_sel_hi:[1,1]" : "=v"(t) : "v"(a), "v"(zr));
  asm("v_pk_fma_f32 %0, %1, %2, %3 op_sel_hi:[0,1,1]" : "=v"(r) : "v"(a), "v"(z), "v"(t));
  return r;
}
DEV float sadd(float a, float b) { float r; asm("v_add_f32 %0, %1, %2" : "=v"(r) : "v"(a), "v"(b)); return r; }
DEV float smul(float a, float b) { float r; asm("v_mul_f32 %0, %1, %2" : "=v"(r) : "v"(a), "v"(b)); return r; }
DEV f32x2 zpow(float la, float th, float k) {
  const float mag = __builtin_amdgcn_exp2f(la * k);
  const float fr = __builtin_amdgcn_fractf(th * k);
  f32x2 r = {mag * __builtin_amdgcn_cosf(fr), mag * __builtin_amdgcn_sinf(fr)};
  return r;
}
template <int SLEEP>
DEV void poll_flags(GAS u32* p, u32 epoch, u32 first) {
  if (!__all(first == epoch)) {
#pragma unroll 1
    for (u32 spins = 0; spins < SPIN_CAP; ++spins) {
      const u32 v = __hip_atomic_load(p, RLX_AGENT);
      if (__all(v == epoch)) break;
      __builtin_amdgcn_s_sleep(SLEEP);
    }
  }
  ORDER_BEHIND_POLL();
}
DEV void publish(GAS u32* p, u32 epoch, int lane) {
  WAITV(0);
  if (lane == 0) __hip_atomic_store(p, epoch, RLX_AGENT);
}
DEV void set_progress(LAS char* L, int wv, u32 k) {
  WAITL();
  *(volatile LAS u32*)(L + LD_PS + wv * 4) = k;
}
DEV void wait_progress(LAS char* L, int word, u32 k) {
#pragma unroll 1
  for (u32 spins = 0; spins < (1u << 22); ++spins) {
    const u32 pv = *(volatile LAS u32*)(L + LD_PS + word * 4);
    if (__all(pv >= k)) break;
    __builtin_amdgcn_s_sleep(1);
  }
  asm volatile("" ::: "memory");
}
DEV void ln_stats(float s1, float s2, float& mean, float& var, float& den1) {
  mean = s1 * (1.0f / 512.0f);
  var = fmaxf(s2 * (1.0f / 512.0f) - mean * mean, 0.0f);
  den1 = bfr(__builtin_amdgcn_sqrtf(bfr(bfr(var) + 1e-5f)));
}
DEV float pick4(f32x4 g, int k) { return k == 0 ? g.x : k == 1 ? g.y : k == 2 ? g.z : g.w; }
#define LD16(p, i) ((u32)__hip_atomic_load((const GAS u16*)(p) + (i), __ATOMIC_RELAXED, __HIP_MEMORY_SCOPE_WAVEFRONT))

__global__ void __launch_bounds__(512, 2)
s4_mixer_kernel(const u16* __restrict__ x, u32 x_bytes, u32 x_fmt, const u16* __restrict__ w1, u32 w1_bytes, u32 w1_fmt,
                const u16* __restrict__ w2, char* __restrict__ ws,
                const u16* __restrict__ A_re, const u16* __restrict__ A_im, const u16* __restrict__ B_re,
                const u16* __restrict__ B_im, const u16* __restrict__ C_re, const u16* __restrict__ C_im,
                const u16* __restrict__ log_dt, const u16* __restrict__ Dskip, const u16* __restrict__ b1,
                const u16* __restrict__ b2, u16* __restrict__ out) {
  extern __shared__ __attribute__((aligned(16))) char smem[];
  LAS char* const L = (LAS char*)smem;
  unsigned long long pc;
  asm volatile("s_getpc_b64 %0" : "=s"(pc));
  {
    u32 coff = ((((threadIdx.x & 63) << 3) | (threadIdx.x >> 6)) * 32u + (blockIdx.x >> 3)) * 64u;
    coff = coff < CODE_BYTES ? coff : 0u;
    asm volatile("s_mov_b32 m0, %0\n\ts_nop 0\n\tglobal_load_lds_dword %1, %2" ::"s"((u32)LD_DUMMY), "v"(coff), "s"(pc) : "memory");
    SCHED();
  }
  const int tid = threadIdx.x, lane = tid & 63;
  const int wv = __builtin_amdgcn_readfirstlane(tid >> 6);
  const int w = wv & 3;
  const bool swave = wv >= 4;
  const int b = blockIdx.x, xcd = b & 7, idx = b >> 3;
  const int r16 = lane & 15, q = lane >> 4, qq = r16 >> 2, p4 = lane & 3;
  const bool helper = idx >= 28;
  TS(0);
  const v4u rsX = mk_rsrc(x, x_bytes, x_fmt);
  const v4u rsW1 = mk_rsrc(w1, w1_bytes, w1_fmt);
  asm volatile("" ::"s"(rsX), "s"(rsW1), "s"(w2), "s"(ws), "s"(A_re), "s"(A_im), "s"(B_re), "s"(B_im), "s"(C_re), "s"(C_im), "s"(log_dt), "s"(Dskip), "s"(b1));
  u32 eraw;
  asm volatile("s_load_dword %0, %1, 0x0" : "=s"(eraw) : "s"(ws));
  const int mq = (idx * 37) >> 8, mr = idx - 7 * mq;
  const int mt = (xcd >> 1) * 4 + mq, nt = (xcd & 1) * 7 + mr;
  const int hid = xcd * 4 + (idx - 28);
  const int tg = (xcd >> 1) * 8 + (idx >> 2), cg = (xcd & 1) * 4 + (idx & 3);
  const int cw = w >> 1, hw = w & 1;
  const int ch = 64 * xcd + 2 * idx + cw;
  const __amdgpu_buffer_rsrc_t rsWS = __builtin_amdgcn_make_buffer_rsrc((void*)ws, 0, WS_END, 0x00020000);
  GAS u32* const ctl = (GAS u32*)ws;
  const v4u ones = {0x3f803f80u, 0x3f803f80u, 0x3f803f80u, 0x3f803f80u};
  int rot2 = (tg & 7) + 2 * (cg & 3);
  rot2 -= rot2 >= 7 ? 7 : 0;
#define KS2(s) (7 * wv + ((s) + rot2 >= 7 ? (s) + rot2 - 7 : (s) + rot2))
#define AW_LOAD(s, c) \
  aw[s][c] = __builtin_amdgcn_raw_buffer_load_b128(rsWS, lane * 16 + ((((c) + 2 * (wv & 1)) & 3) << 10), WS_W2T + (cg * 56 + KS2(s)) * 4096, SC1)
  const u32 wo = (u32)lane * 4u;
#define TW(rowp, row, val) *(LAS u32*)((rowp) + (wo ^ (u32)(((row) & 15) << 4))) = (val)
  LAS char* const PT = L + LD_KREG + w * 8192;

  auto gemm2_tail = [&](v4u (&aw)[7][4], const u32 epoch, GAS u32* const pend, u32 fh0) __attribute__((always_inline)) {
  {
    TS(9);
    int tz = threadIdx.x;
    asm volatile("" : "+v"(tz));
    const int lane = tz & 63, r16 = lane & 15, q = lane >> 4, qq = r16 >> 2, p4 = lane & 3;
    asm volatile("" : "+v"(fh0));
    poll_flags<2>(ctl + WS_FH / 4 + (tg * 32 + ((7 * wv) >> 1) + (lane & 3)) * FL, epoch, fh0);
    TS(10);
    constexpr int WIN = 4;
    v4u hf[7][4];
#define H_LOAD(s) _Pragma("unroll") for (int j = 0; j < 4; ++j) \
      hf[s][j] = __builtin_amdgcn_raw_buffer_load_b128(rsWS, lane * 16 + (((j + (wv >> 1)) & 3) << 10), WS_HT + (tg * 56 + KS2(s)) * 4096, SC1);
#pragma unroll
    for (int s = 0; s < WIN; ++s) { H_LOAD(s) SCHED(); }
    if (pend) {
      WAITV(16);
      if (lane == 0) __hip_atomic_store(pend, epoch, RLX_AGENT);
      SCHED();
    }
    GAS u32* const fsp = ctl + WS_FS / 4 + ((tg >> 4) * 512 + cg * 64 + wv * 8 + (lane >> 3)) * FL;
    const int tok = tg * 64 + 16 * (wv >> 1) + r16;
    const int c8 = cg * 64 + 32 * (wv & 1) + 8 * q;
    u32 fs, fe0 = epoch, fe1 = epoch;
    bool early;
    v4u xv, bv, sv;
    f32x4 acc[4][4];
#pragma unroll
    for (int i = 0; i < 4; ++i)
#pragma unroll
      for (int j = 0; j < 4; ++j) acc[i][j] = f32x4{0.f, 0.f, 0.f, 0.f};
#pragma unroll
    for (int s = 0; s < 7; ++s) {
#pragma unroll
      for (int j = 0; j < 4; ++j)
#pragma unroll
        for (int c = 0; c < 4; ++c) acc[c][j] = mfma16(aw[s][c], hf[s][j], acc[c][j]);
      SCHED();
      if (s == 2) fs = __hip_atomic_load(fsp, RLX_AGENT);
      if (s + WIN < 7) { H_LOAD(s + WIN) }
      if (s == 2) {
        xv = *(const GAS v4u*)(x + tok * 512 + c8);
        bv = *(const GAS v4u*)(b2 + c8);
      }
      if (s == 5) {
        early = __all(fs == epoch);
        ORDER_BEHIND_POLL();
        sv = __builtin_amdgcn_raw_buffer_load_b128(rsWS, (lane >> 3) * 4096 + (lane & 7) * 16, early ? WS_ST + (cg * 64 + wv * 8) * 4096 + tg * 128 : WS_END, SC1);
        if (b == 0) {
          fe0 = __hip_atomic_load(ctl + WS_FS / 4 + tz * FL, RLX_AGENT);
          fe1 = __hip_atomic_load(ctl + WS_FS / 4 + (512 + tz) * FL, RLX_AGENT);
        }
      }
      SCHED();
    }
    TS(11);
    wait_progress(L, lane & 7, 4u);
#pragma unroll
    for (int j = 0; j < 4; ++j)
#pragma unroll
      for (int cp = 0; cp < 2; ++cp)
        if (j + cp > 0) {
          const int d = (((j + (wv >> 1)) & 3) << 1) | ((cp + wv) & 1);
#pragma unroll
          for (int e = 0; e < 2; ++e)
            *(LAS f32x4*)(L + LD_RED + (d * 7 + 2 * j + cp - 1) * 2048 + e * 1024 + lane * 16) = acc[2 * cp + e][j];
        }
    if (!early) {
      poll_flags<2>(fsp, epoch, epoch + 1u);
      sv = __builtin_amdgcn_raw_buffer_load_b128(rsWS, (lane >> 3) * 4096 + (lane & 7) * 16, WS_ST + (cg * 64 + wv * 8) * 4096 + tg * 128, SC1);
    }
    *(LAS v4u*)(L + LD_ST + wv * 1024 + lane * 16) = sv;
    TS(12);
    asm volatile("" : "+v"(fe0), "+v"(fe1));
    poll_flags<2>(ctl + WS_FS / 4 + tz * FL, epoch, fe0);
    poll_flags<2>(ctl + WS_FS / 4 + (512 + tz) * FL, epoch, fe1);
    BAR();
    if (b == 0 && tz == 0) __hip_atomic_store(ctl + WS_E / 4, epoch, RLX_AGENT);
    TS(13);
    f32x4 part[2][7];
    v2u sq[2];
#pragma unroll
    for (int e = 0; e < 2; ++e) {
#pragma unroll
      for (int j = 0; j < 7; ++j) part[e][j] = *(LAS f32x4*)(L + LD_RED + (wv * 7 + j) * 2048 + e * 1024 + lane * 16);
      sq[e] = trread(L + LD_ST + (32 * (wv & 1) + 8 * q + 4 * e + qq) * 128 + (16 * (wv >> 1) + 4 * p4) * 2);
    }
    SCHED();
    u32 o[4];
#pragma unroll
    for (int e = 0; e < 2; ++e) {
      const u32 ba = bv[2 * e], bb = bv[2 * e + 1], xa = xv[2 * e], xb = xv[2 * e + 1];
      f32x4 m = acc[e][0] + f32x4{bflo(ba), bfhi(ba), bflo(bb), bfhi(bb)};
#pragma unroll
      for (int j = 0; j < 7; ++j) m += part[e][j];
      f32x4 g;
#pragma unroll
      for (int k = 0; k < 4; ++k) g[k] = m[k] * __builtin_amdgcn_rcpf(1.0f + __builtin_amdgcn_exp2f(m[k] * -1.4426950408889634f));
      o[2 * e] = pk2(bflo(xa) + bflo(sq[e].x) * g.x, bfhi(xa) + bfhi(sq[e].x) * g.y);
      o[2 * e + 1] = pk2(bflo(xb) + bflo(sq[e].y) * g.z, bfhi(xb) + bfhi(sq[e].y) * g.w);
    }
    *(LAS v4u*)(L + LD_KREG + (16 * (wv >> 1) + r16) * 144 + (wv & 1) * 64 + q * 16) = v4u{o[0], o[1], o[2], o[3]};
    BAR();
    const v4u ov = *(LAS v4u*)(L + LD_KREG + (8 * wv + (lane >> 3)) * 144 + (lane & 7) * 16);
    __builtin_amdgcn_raw_buffer_store_b128(ov, __builtin_amdgcn_make_buffer_rsrc((void*)out, 0, 2097152, 0x00020000), ((tg * 64 + 8 * wv + (lane >> 3)) * 512 + cg * 64 + (lane & 7) * 8) * 2, 0, 16);
  }
  TS(14);
  __builtin_amdgcn_endpgm();
  };

  if (helper) {
    LAS char* const HX = L + LD_HX + wv * 8192;
#pragma unroll
    for (int p = 0; p < 8; ++p)
      dma16((u32)(wv * 8192 + ((lane ^ (((p & 3) << 2) | (p >> 2))) << 4)), rsX, (u32)((hid * 64 + p) * 1024), (u32)(LD_HX + (wv * 8 + p) * 1024));
    u32 epoch;
    asm volatile("s_waitcnt vmcnt(0) lgkmcnt(0)\n\ts_add_u32 %0, %1, 1" : "=s"(epoch) : "s"(eraw) : "memory", "scc");
    if (!swave) TS(3);
    f32x4 gram[2], sumx[2];
#pragma unroll
    for (int i = 0; i < 2; ++i) { gram[i] = f32x4{0.f, 0.f, 0.f, 0.f}; sumx[i] = gram[i]; }
    const int r8 = lane & 7, fr = ((r8 & 3) << 2) | (r8 >> 2);
    v4u xr[8];
#pragma unroll
    for (int s = 0; s < 8; ++s) xr[s] = *(LAS v4u*)(HX + r8 * 1024 + (lane & 8) * 64 + (((4 * s + q) ^ fr) << 4));
    v2u d[8][2];
#pragma unroll
    for (int s = 0; s < 8; ++s)
#pragma unroll
      for (int t = 0; t < 2; ++t)
        d[s][t] = trread(HX + (4 * t + qq) * 1024 + (((8 * s + 2 * q + (p4 >> 1)) ^ ((qq << 2) | t)) << 4) + 8 * (p4 & 1));
    SCHED();
#pragma unroll
    for (int s = 0; s < 8; ++s) {
      gram[s & 1] = mfma16(xr[s], xr[s], gram[s & 1]);
      sumx[s & 1] = mfma16(ones, xr[s], sumx[s & 1]);
    }
    float s1 = sumx[0].x + sumx[1].x, s2 = pick4(gram[0] + gram[1], r16 & 3);
    s1 += __shfl(s1, (lane + 40) & 63);
    s2 += __shfl(s2, (lane + 40) & 63);
    float mean, var, den1;
    ln_stats(s1, s2, mean, var, den1);
    const float rd = __builtin_amdgcn_rcpf(den1);
    const float den2 = bfr(__builtin_amdgcn_sqrtf(bfr(bfr(var * rd * rd) + 1e-5f)));
    const float R = __builtin_amdgcn_rcpf(den1 * den2);
    if (q == qq && r16 < 8) {
      *(LAS f32x2*)(L + LD_HST + (8 * wv + r16) * 8) = f32x2{rd, -bfr(mean) * rd};
      __builtin_amdgcn_raw_buffer_store_b128(v4u{__builtin_bit_cast(u32, R), epoch, __builtin_bit_cast(u32, R * mean), epoch}, rsWS,
                                             WS_STAT + (hid * 64 + 8 * wv + r16) * 16, 0, SC1);
    }
    WAITL();
    if (!swave) TS(4);
    f32x4 sv[4];
#pragma unroll
    for (int i = 0; i < 4; ++i) sv[i] = *(LAS f32x4*)(L + LD_HST + wv * 64 + i * 16);
#pragma unroll
    for (int cb = 0; cb < 8; ++cb) {
      v4u o;
      o.x = pk2(bflo(d[cb][0].x) * sv[0].x + sv[0].y, bfhi(d[cb][0].x) * sv[0].z + sv[0].w);
      o.y = pk2(bflo(d[cb][0].y) * sv[1].x + sv[1].y, bfhi(d[cb][0].y) * sv[1].z + sv[1].w);
      o.z = pk2(bflo(d[cb][1].x) * sv[2].x + sv[2].y, bfhi(d[cb][1].x) * sv[2].z + sv[2].w);
      o.w = pk2(bflo(d[cb][1].y) * sv[3].x + sv[3].y, bfhi(d[cb][1].y) * sv[3].z + sv[3].w);
      __builtin_amdgcn_raw_buffer_store_b128(o, rsWS, lane * 16 + WS_XNT + (hid * 8 + wv) * 8192 + cb * 1024, 0, SC1);
    }
    if (!swave) TS(5);
    WAITV(0);
    BAR();
    if (tid == 0) __hip_atomic_store(ctl + WS_FXN / 4 + hid * FL, epoch, RLX_AGENT);
    if (!swave) TS(6);
    SCHED();
  }

  if (swave) {
    __builtin_amdgcn_s_setprio(2);
    const u32 voX = (u32)((32 * w + (lane >> 3)) * 1024 + (((lane & 7) ^ (lane >> 3)) << 4));
    const u32 voW = (u32)((4 * w + q) * 3584 + ((r16 ^ ((q << 2) | w)) << 4));
    const int rot = (mr + 2 * mq) & 7;
#define G1_STAGE(st) G1_PART(st, 0, 4)
#define G1_PART(st, p0, p1)                                                                                  \
  {                                                                                                          \
    const int sa_ = ((st) + rot) & 7;                                                                        \
    _Pragma("unroll") for (int p = p0; p < p1; ++p) {                                                        \
      dma16(voX, rsX, (u32)(mt * 131072 + p * 8192 + sa_ * 128), (u32)(((st) & 3) * 32768 + w * 4096 + p * 1024)); \
      dma16(voW, rsW1, (u32)((sa_ * 64 + 16 * p) * 3584 + nt * 256), (u32)(((st) & 3) * 32768 + 16384 + p * 4096 + w * 1024)); \
    }                                                                                                        \
  }
    if (!helper) { G1_STAGE(0) G1_STAGE(1) G1_PART(2, 0, 2) }
    if (lane == 0) *(LAS u32*)(L + LD_PS + wv * 4) = 0u;
    TS(1);
    WAITV(12);
    u32 epoch;
    asm volatile("s_waitcnt lgkmcnt(0)\n\ts_add_u32 %0, %1, 1" : "=s"(epoch) : "s"(eraw) : "memory", "scc");
    TS(2);
    BAR();
    TS(3);
    TC(0);
    if (!helper) G1_PART(2, 2, 4)
    const int r32 = lane & 31, hh = lane >> 5;
    v4u pf[8], qf[8];
    {
#pragma unroll
      for (int s = 0; s < 8; ++s) {
        qf[s] = *(LAS v4u*)(L + LD_Q + w * 8192 + r32 * 256 + (((2 * s + hh) ^ (r32 & 15)) << 4));
        pf[s] = *(LAS v4u*)(PT + (2 * s + hh) * 464 + r32 * 16);
      }
    }
    SCHED();
    TC(1);
    if (!helper) {
      WAITV(8); BAR(); TC(2); G1_STAGE(3) G1_PART(4, 0, 2) TC(3);
      WAITV(12); BAR(); TC(4); G1_PART(4, 2, 4) G1_STAGE(5) TC(5);
      WAITV(16); BAR(); TC(6);
      G1_STAGE(6) TC(7);
      WAITV(16); BAR(); TC(8); G1_STAGE(7) TC(9);
      WAITV(16); BAR();
    }
    TC(10);
    GAS u32* const fxnp = ctl + WS_FXN / 4 + (hw * 16 + r16) * FL;
    GAS u32* const fw2p = ctl + WS_FW2 / 4 + ((cg * 56 + 7 * (w + 4 * ((lane >> 4) & 1))) * 2 + min(r16, 13)) * FL;
    const int sto = WS_STAT + ((helper ? 0 : mt) * 128 + 32 * w + r32) * 16;
    v4u sg = __builtin_amdgcn_raw_buffer_load_b128(rsWS, sto, 0, SC1);
    const u32 fw2 = __hip_atomic_load(fw2p, RLX_AGENT);
    const u32 pDs = LD16(Dskip, ch);
    SCHED();
    f32x16 kacc;
#pragma unroll
    for (int i = 0; i < 16; ++i) kacc[i] = 0.f;
#pragma unroll
    for (int s = 0; s < 8; ++s) kacc = mfma32(pf[s], qf[s], kacc);
    SCHED();
    TC(11);
    if (!helper) { WAITV(3); BAR(); }
    TC(12);
    TS(4);
#pragma unroll
    for (int i = 0; i < 4; ++i)
      *(LAS f32x4*)(L + LD_CX + w * 4096 + i * 1024 + lane * 16) = f32x4{kacc[4 * i], kacc[4 * i + 1], kacc[4 * i + 2], kacc[4 * i + 3]};
    if (!helper) {
      if (!__all(sg.y == epoch && sg.w == epoch)) {
#pragma unroll 1
        for (u32 spins = 0; spins < SPIN_CAP; ++spins) {
          __builtin_amdgcn_s_sleep(4);
          asm volatile("" ::: "memory");
          sg = __builtin_amdgcn_raw_buffer_load_b128(rsWS, sto, 0, SC1);
          if (__all(sg.y == epoch && sg.w == epoch)) break;
        }
      }
      *(LAS v2u*)(L + LD_STAT + (32 * w + r32) * 8) = v2u{sg.x, sg.z};
    }
    poll_flags<4>(fw2p, epoch, fw2);
    set_progress(L, wv, 1u);
    TS(5);
    u32 fxa = __hip_atomic_load(fxnp, RLX_AGENT);
    v4u aw[7][4];
#pragma unroll
    for (int c = 0; c < 12; ++c) AW_LOAD(c >> 2, c & 3);
    SCHED();
    wait_progress(L, wv ^ 1, 1u);
    {
      f32x4 v[4];
#pragma unroll
      for (int i = 0; i < 4; ++i) v[i] = *(LAS f32x4*)(L + LD_CX + (w ^ 1) * 4096 + i * 1024 + lane * 16);
#pragma unroll
      for (int i = 0; i < 15; ++i) {
        kacc[i] = sadd(kacc[i], v[i >> 2][i & 3]);
      }
    }
    {
      const bool odd = r32 & 1, up = hh != 0;
      float e[4][9];
      const int ia[4][9] = {{0, 1, 2, 3, 4, 5, 6, 7, 8}, {9, 9, 9, 9, -1, 10, 10, 10, 10}, {11, 11, 11, 11, -1, 12, 12, 12, 12}, {13, 13, 13, 13, -1, 14, 14, 14, 14}};
      const int ib[4][9] = {{0, 0, 1, 2, 2, 2, 3, 4, 4}, {5, 5, 5, 6, 6, 6, 7, 7, 7}, {8, 8, 8, 9, 9, 9, 10, 10, 10}, {11, 11, 11, 11, -1, 12, 12, 12, 12}};
#pragma unroll
      for (int u = 0; u < 4; ++u)
#pragma unroll
        for (int i = 0; i < 9; ++i) {
          const float va = ia[u][i] >= 0 ? kacc[ia[u][i]] : (odd ? kacc[ia[u][5]] : kacc[ia[u][3]]);
          const float vb = ib[u][i] >= 0 ? kacc[ib[u][i]] : (odd ? kacc[ib[u][5]] : kacc[ib[u][3]]);
          e[u][i] = up ? vb : va;
        }
      LAS char* const KC = PT + hw * 64 + (255 - r32) * 16;
#pragma unroll
      for (int u = 0; u < 4; ++u) {
        v4u d;
        if (hw) { d.x = pk2(e[u][8], e[u][7]); d.y = pk2(e[u][6], e[u][5]); d.z = pk2(e[u][4], e[u][3]); d.w = pk2(e[u][2], e[u][1]); }
        else    { d.x = pk2(e[u][7], e[u][6]); d.y = pk2(e[u][5], e[u][4]); d.z = pk2(e[u][3], e[u][2]); d.w = pk2(e[u][1], e[u][0]); }
        const int sa = u == 0 ? 0 : u + 3, sb = u == 3 ? 7 : u + 1;
        *(LAS v4u*)(KC - (up ? sb : sa) * 512) = d;
      }
      if (lane < 2) *(LAS v4u*)(PT + hw * 64 + 4096 + lane * 16) = v4u{(hw && lane == 0) ? pk2(kacc[0], 0.f) : 0u, 0u, 0u, 0u};
    }
    SCHED();
    TC(13);
    asm volatile("" : "+v"(fxa));
    poll_flags<8>(fxnp, epoch, fxa);
    const v4u u0 = __builtin_amdgcn_raw_buffer_load_b128(rsWS, lane * 8192, WS_XNT + (128 * hw) * 8192 + ch * 16, SC1);
    const v4u u1 = __builtin_amdgcn_raw_buffer_load_b128(rsWS, lane * 8192, WS_XNT + (128 * hw + 64) * 8192 + ch * 16, SC1);
#pragma unroll
    for (int c = 12; c < 20; ++c) AW_LOAD(c >> 2, c & 3);
    SCHED();
    LAS char* const UB = L + LD_U + cw * 4608;
    if (hw == 0 && lane < 32) *(LAS v4u*)(UB + lane * 16) = v4u{0u, 0u, 0u, 0u};
    *(LAS v4u*)(UB + 512 + (128 * hw + lane) * 16) = u0;
    *(LAS v4u*)(UB + 512 + (128 * hw + 64 + lane) * 16) = u1;
    set_progress(L, wv, 2u);
    SCHED();
#pragma unroll
    for (int c = 20; c < 28; ++c) AW_LOAD(c >> 2, c & 3);
    {
      const u32 po = (u32)(WS_W2T + (cg * 56 + 7 * w) * 4096 + ((tg & 7) * 28 + min(lane, 27)) * 128);
      asm volatile("s_mov_b32 m0, %0\n\ts_nop 0\n\tglobal_load_lds_dword %1, %2 sc1" ::"s"((u32)LD_DUMMY), "v"(po), "s"(ws) : "memory");
    }
    SCHED();
    TS(6);
    wait_progress(L, wv ^ 1, 2u);
    wait_progress(L, w, 4u);
    TS(7);
    TC(14);
    const float dskip = bf2f(pDs);
    const int sgm = 1 - (r16 & 1);
    LAS char* const ka = L + LD_KREG + (2 * cw + sgm) * 8192 + sgm * 64 + 2 * (2031 - r16 + 8 * q + sgm) - hw * 256 - 7 * 512 - 3 * 64;
    LAS char* const ub = UB + 2 * (256 - 16 + 16 * r16 + 8 * q) - hw * 256 - 3 * 64;
    f32x4 y[8];
#pragma unroll
    for (int i = 0; i < 8; ++i) y[i] = f32x4{0.f, 0.f, 0.f, 0.f};
    v4u af[4][8], bfv[4][8];
    u32 kb[4];
#pragma unroll
    for (int i = 0; i < 4; ++i) { kb[i] = (u32)(size_t)ka + i * 1024; asm volatile("" : "+v"(kb[i])); }
#define CV_READ(rr, g)                                                                                   \
  {                                                                                                      \
    LAS u32* pa = (LAS u32*)(size_t)(kb[(7 - (g)) >> 1] + ((7 - (g)) & 1) * 512 + (3 - (rr)) * 64);      \
    af[rr][g] = v4u{pa[0], pa[1], pa[2], pa[3]};                                                         \
    bfv[rr][g] = *(LAS v4u*)(ub + (g) * 512 + (3 - (rr)) * 64);                                          \
  }
    CV_READ(0, 0) CV_READ(0, 1) CV_READ(0, 2) CV_READ(0, 3) CV_READ(0, 4)
    SCHED();
#pragma unroll
    for (int rr = 0; rr < 4; ++rr) {
#pragma unroll
      for (int k = 0; k < 8; ++k) {
        if (k + 5 < 8) CV_READ(rr, k + 5)
        else if (rr < 3) CV_READ(rr + 1, k + 5 - 8)
#pragma unroll
        for (int o = 0; o <= k; ++o)
          if (k + o <= 7) {
            y[k + o] = mfma16(af[rr][k], bfv[rr][o], y[k + o]);
            if (o < k) y[k + o] = mfma16(af[rr][o], bfv[rr][k], y[k + o]);
          }
        SCHED();
      }
    }
    TC(15);
    const u32 fh0 = __hip_atomic_load(ctl + WS_FH / 4 + (tg * 32 + ((7 * wv) >> 1) + (lane & 3)) * FL, RLX_AGENT);
    SCHED();
    f32x4 mine[4];
    if (hw) {
#pragma unroll
      for (int i = 0; i < 4; ++i) { *(LAS f32x4*)(L + LD_CX + w * 4096 + i * 1024 + lane * 16) = y[i]; mine[i] = y[4 + i]; }
    } else {
#pragma unroll
      for (int i = 0; i < 4; ++i) { *(LAS f32x4*)(L + LD_CX + w * 4096 + i * 1024 + lane * 16) = y[4 + i]; mine[i] = y[i]; }
    }
    set_progress(L, wv, 3u);
    wait_progress(L, wv ^ 1, 3u);
    f32x4 oy[4];
    v2u xv[4];
#pragma unroll
    for (int i = 0; i < 4; ++i) {
      oy[i] = *(LAS f32x4*)(L + LD_CX + (w ^ 1) * 4096 + i * 1024 + lane * 16);
      xv[i] = *(LAS v2u*)(UB + 512 + (256 * (4 * hw + i) + 16 * r16 + 4 * q) * 2);
    }
    set_progress(L, wv, 4u);
#pragma unroll
    for (int i = 0; i < 4; ++i) {
      const u32 ya = pk2(sadd(mine[i].x, oy[i].x), sadd(mine[i].y, oy[i].y)), yb = pk2(sadd(mine[i].z, oy[i].z), sadd(mine[i].w, oy[i].w));
      const u32 da = pk2(smul(bflo(xv[i].x), dskip), smul(bfhi(xv[i].x), dskip)), db = pk2(smul(bflo(xv[i].y), dskip), smul(bfhi(xv[i].y), dskip));
      v2u o;
      o.x = pk2(sadd(bflo(ya), bflo(da)), sadd(bfhi(ya), bfhi(da)));
      o.y = pk2(sadd(bflo(yb), bflo(db)), sadd(bfhi(yb), bfhi(db)));
      __builtin_amdgcn_raw_buffer_store_b64(o, rsWS, (16 * r16 + 4 * q) * 2 + WS_ST + ch * 4096 + (4 * hw + i) * 512, 0, SC1);
    }
    TS(8);
    __builtin_amdgcn_s_setprio(0);
    gemm2_tail(aw, epoch, ctl + WS_FS / 4 + (hw * 512 + ch) * FL, fh0);
  } else {
    if (!helper) __builtin_amdgcn_s_setprio(3);
    const int po = ch * 128 + hw * 64 + lane;
    const u32 pLd = LD16(log_dt, ch), pAr = LD16(A_re, po), pAi = LD16(A_im, po);
    const u32 pBr = LD16(B_re, po), pBi = LD16(B_im, po), pCr = LD16(C_re, po), pCi = LD16(C_im, po);
    const u32 pB1 = LD16(b1, (helper ? 0 : nt * 128) + (tid & 127));
    const int uks = 2 * (helper ? 0 : idx) + (w >> 1), upp = w & 1;
    v4u w2a, w2b;
    {
      const GAS v4u* src = (const GAS v4u*)(w2 + (uks * 32 + (lane >> 2)) * 512 + xcd * 64 + upp * 32 + (lane & 3) * 8);
      asm volatile("" : "+v"(src));
      w2a = src[0];
      w2b = src[16 * 64];
    }
    if (lane == 0) *(LAS u32*)(L + LD_PS + wv * 4) = 0u;
    SCHED();
    TS(1);
    const int mi = w >> 1, ni = w & 1;
    f32x4 acc[4][4], csum[4];
#pragma unroll
    for (int i = 0; i < 4; ++i) {
#pragma unroll
      for (int j = 0; j < 4; ++j) { acc[i][j] = f32x4{0.f, 0.f, 0.f, 0.f}; asm volatile("" : "+v"(acc[i][j])); }
      csum[i] = f32x4{0.f, 0.f, 0.f, 0.f};
      asm volatile("" : "+v"(csum[i]));
    }
    u32 xb[2][2], wb[2][4][2];
#pragma unroll
    for (int h = 0; h < 2; ++h) {
#pragma unroll
      for (int k = 0; k < 2; ++k) {
        xb[h][k] = (u32)(((64 * mi + r16) * 128 + ((q ^ (r16 & 7)) << 4)) ^ (k * 64)) + h * 65536;
        asm volatile("" : "+v"(xb[h][k]));
      }
#pragma unroll
      for (int j = 0; j < 4; ++j)
#pragma unroll
        for (int t = 0; t < 2; ++t) {
          wb[h][j][t] = (u32)(16384 + (8 * q + 4 * t + qq) * 256 + (((2 * (4 * ni + j) + (p4 >> 1)) ^ ((qq << 2) | ((2 * q + t) & 3))) << 4) + 8 * (p4 & 1)) + h * 65536;
          asm volatile("" : "+v"(wb[h][j][t]));
        }
    }
    v4u onev = ones;
    asm volatile("" : "+v"(onev));
    SCHED();
    TC(0);
    u32 epoch;
    asm volatile("s_waitcnt lgkmcnt(0)\n\ts_add_u32 %0, %1, 1" : "=s"(epoch) : "s"(eraw) : "memory", "scc");
    SCHED();
    {
      const float dt = bfr(__builtin_amdgcn_exp2f(bf2f(pLd) * 1.4426950408889634f));
      const float la = dt * bf2f(pAr) * 1.4426950408889634f;
      const float th = dt * bf2f(pAi) * 0.15915494309189535f;
      const f32x2 dBc = {dt * bf2f(pBr), -dt * bf2f(pBi)};
      const f32x2 Ccc = {bf2f(pCr), -bf2f(pCi)};
      const f32x2 z8 = zpow(la, th, 8.f), z16 = zpow(la, th, 16.f);
      const f32x2 y1 = zpow(la, -th, 1.f), y256 = zpow(la, -th, 256.f), y8 = {z8.x, -z8.y};
      const f32x2 y2 = cmul(y1, y1), y4 = cmul(y2, y2);
#define PROW(h, g) (((g) & 3) + 8 * ((g) >> 2) + 4 * (h))
#define PW(h, g, v) *(LAS u32*)(PT + (lane >> 2) * 464 + (lane & 3) * 4 + PROW(h, g) * 16) = pk2v(v)
      f32x2 W[8];
      W[0] = cmul(Ccc, dBc);
#pragma unroll
      for (int c = 1; c < 8; ++c) W[c] = cmul(W[c - 1], y256);
      {
        f32x2 te = W[0], to = cmul(W[0], y1), t1 = W[1];
        PW(0, 0, te); PW(0, 1, to); PW(1, 0, t1);
#pragma unroll
        for (int a = 1; a <= 4; ++a) {
          te = cmul(te, y2); PW(0, 2 * a, te);
          if (a < 4) { to = cmul(to, y2); PW(0, 2 * a + 1, to); }
          t1 = cmul(t1, y2); PW(1, a, t1);
        }
      }
      PW(1, 5, W[2]); PW(1, 6, cmul(W[2], y4)); PW(1, 7, cmul(W[2], y8));
      PW(1, 8, W[3]); PW(1, 9, cmul(W[3], y4)); PW(1, 10, cmul(W[3], y8));
      PW(0, 9, W[4]); PW(0, 10, cmul(W[4], y8));
      PW(0, 11, W[5]); PW(0, 12, cmul(W[5], y8));
      PW(0, 13, W[6]); PW(0, 14, cmul(W[6], y8));
      PW(1, 11, W[7]); PW(1, 12, cmul(W[7], y8));
      SCHED();
      TC(2);
      LAS char* const QA = L + LD_Q + w * 8192;
      *(LAS v4u*)(QA + lane * 16) = w2a;
      *(LAS v4u*)(QA + 1024 + lane * 16) = w2b;
      v2u wt[2][2];
#pragma unroll
      for (int e = 0; e < 2; ++e)
#pragma unroll
        for (int t = 0; t < 2; ++t) wt[e][t] = trread(QA + (16 * t + 4 * q + qq) * 64 + 16 * p4 + 8 * e);
      asm volatile("" ::: "memory");
      TC(3);
      f32x2 qa = {1.f, 0.f}, qb = z8;
#pragma unroll
      for (int i = 0; i < 16; ++i) {
        TW(QA + 2 * i * 256, 2 * i, pk2v(qa));
        TW(QA + (2 * i + 1) * 256, 2 * i + 1, pk2v(qb));
        if (i < 15) { qa = (i == 0) ? z16 : cmul(qa, z16); qb = cmul(qb, z16); }
        if (i == 1) {
#pragma unroll
      for (int e = 0; e < 2; ++e)
        __builtin_amdgcn_raw_buffer_store_b128(v4u{wt[e][0].x, wt[e][0].y, wt[e][1].x, wt[e][1].y}, rsWS,
                                               lane * 16 + (helper ? WS_END : WS_W2T) + ((xcd * 56 + uks) * 4 + 2 * upp + e) * 1024, 0, SC1);
        }
      }
    }
    *(LAS float*)(L + LD_B1 + (tid & 127) * 4) = bf2f(pB1);
    TC(4);
    TS(2);
    v4u aw[7][4];
    if (!helper) {
      v4u xf[2][4], wf[2][4];
#define G1_READ(n)                                                                                            \
  _Pragma("unroll") for (int i = 0; i < 4; ++i) {                                                             \
    const v2u lo = trread(L + (((n) >> 1) & 1) * 32768 + ((n) & 1) * 8192 + wb[((n) >> 2) & 1][i][0]);        \
    const v2u hi = trread(L + (((n) >> 1) & 1) * 32768 + ((n) & 1) * 8192 + wb[((n) >> 2) & 1][i][1]);        \
    wf[(n) & 1][i] = v4u{lo.x, lo.y, hi.x, hi.y};                                                             \
    xf[(n) & 1][i] = *(LAS v4u*)(L + (((n) >> 1) & 1) * 32768 + i * 2048 + xb[((n) >> 2) & 1][(n) & 1]);      \
  }
      __builtin_amdgcn_s_setprio(0);
      TC(5);
      BAR();
      TC(6);
      TS(3);
      G1_READ(0)
      SCHED();
#pragma unroll 1
      for (int it = 0; it < 2; ++it) {
#pragma unroll
        for (int n = 0; n < 8; ++n) {
          if (n & 1) {
            if (n < 4 || it == 0) BAR();
            if (n == 3 && it == 0) {
              __builtin_amdgcn_s_setprio(3);
              publish(ctl + WS_FW2 / 4 + ((xcd * 56 + uks) * 2 + upp) * FL, epoch, lane);
              __builtin_amdgcn_s_setprio(0);
            }
            SCHED();
          }
          G1_READ(n + 1)
#pragma unroll
          for (int j = 0; j < 4; ++j) {
#pragma unroll
            for (int i = 0; i < 4; ++i) acc[j][i] = mfma16(wf[n & 1][j], xf[n & 1][i], acc[j][i]);
            csum[j] = mfma16(wf[n & 1][j], onev, csum[j]);
          }
#pragma unroll
          for (int k = 0; k < 12; ++k) {
            __builtin_amdgcn_sched_group_barrier(0x008, 1, 0);
            __builtin_amdgcn_sched_group_barrier(0x100, 1, 0);
          }
          __builtin_amdgcn_sched_group_barrier(0x008, 8, 0);
          SCHED();
        }
      }
      TS(4);
      __builtin_amdgcn_s_setprio(3);
      wait_progress(L, 4 + (lane & 3), 1u);
      TS(5);
      float rs[4], rm[4];
      f32x4 bb[4];
#pragma unroll
      for (int i = 0; i < 4; ++i) {
        const f32x2 st = *(LAS f32x2*)(L + LD_STAT + (64 * mi + 16 * i + r16) * 8);
        rs[i] = st.x; rm[i] = -st.y;
        asm volatile("" : "+v"(rm[i]));
        bb[i] = *(LAS f32x4*)(L + LD_B1 + (64 * ni + 16 * i + 4 * q) * 4);
      }
      SCHED();
#pragma unroll
      for (int jp = 0; jp < 2; ++jp) {
#pragma unroll
        for (int i = 0; i < 4; ++i) {
          TC(8 + jp * 4 + i);
          f32x2 pre[4], ee[4];
#pragma unroll
          for (int k = 0; k < 4; ++k) {
            const f32x4 a4 = acc[2 * jp + (k >> 1)][i], c4 = csum[2 * jp + (k >> 1)], b4 = bb[2 * jp + (k >> 1)];
            const f32x2 a2 = (k & 1) ? f32x2{a4.z, a4.w} : f32x2{a4.x, a4.y};
            const f32x2 c2 = (k & 1) ? f32x2{c4.z, c4.w} : f32x2{c4.x, c4.y};
            const f32x2 b2v = (k & 1) ? f32x2{b4.z, b4.w} : f32x2{b4.x, b4.y};
            pre[k] = a2 * rs[i] + (c2 * rm[i] + b2v);
            f32x2 arg = pre[k] * (pre[k] * pre[k] * -0.1029432397f + -2.302208198f);
            asm("" : "+v"(arg));
            ee[k] = f32x2{__builtin_amdgcn_exp2f(arg.x), __builtin_amdgcn_exp2f(arg.y)};
          }
          u32 o[4];
#pragma unroll
          for (int k = 0; k < 4; ++k) {
            f32x2 dn = ee[k] + 1.0f;
            asm("" : "+v"(dn));
            o[k] = pk2v(pre[k] * f32x2{__builtin_amdgcn_rcpf(dn.x), __builtin_amdgcn_rcpf(dn.y)});
          }
          __builtin_amdgcn_raw_buffer_store_b128(v4u{o[0], o[1], o[2], o[3]}, rsWS,
                                                 lane * 16 + ABL_HT + (((2 * mt + mi) * 56 + nt * 4 + 2 * ni + jp) * 4 + i) * 1024, 0, SC1);
          SCHED();
        }
      }
      set_progress(L, wv, 4u);
      __builtin_amdgcn_s_setprio(0);
      TS(6);
#pragma unroll
      for (int c = 0; c < 8; ++c) AW_LOAD(c >> 2, c & 3);
      SCHED();
      __builtin_amdgcn_s_setprio(3);
      WAITV(8);
      if (lane == 0) __hip_atomic_store(ctl + WS_FH / 4 + ((2 * mt + mi) * 32 + 2 * nt + ni) * FL, epoch, RLX_AGENT);
      __builtin_amdgcn_s_setprio(0);
      SCHED();
      TS(7);
#pragma unroll
      for (int c = 8; c < 28; ++c) AW_LOAD(c >> 2, c & 3);
      SCHED();
    } else {
      __builtin_amdgcn_s_setprio(0);
      BAR();
      set_progress(L, wv, 4u);
      wait_progress(L, 4 + (lane & 3), 1u);
#pragma unroll
      for (int s = 0; s < 7; ++s)
#pragma unroll
        for (int c = 0; c < 4; ++c) AW_LOAD(s, c);
      SCHED();
    }
    TS(8);
    gemm2_tail(aw, epoch, nullptr, epoch + 1u);
  }
}

extern "C" void kernel_launch(void* const* d_in, const int* in_sizes, int n_in, void* d_out, int out_size, void* d_ws,
                              size_t ws_size, hipStream_t stream) {
  (void)in_sizes; (void)n_in; (void)out_size; (void)ws_size;
  (void)hipFuncSetAttribute((const void*)s4_mixer_kernel, hipFuncAttributeMaxDynamicSharedMemorySize, LDS_BYTES);
  s4_mixer_kernel<<<256, 512, LDS_BYTES, stream>>>(
      (const u16*)d_in[0], 2097152u, 0x00020000u, (const u16*)d_in[10], 1835008u, 0x00020000u, (const u16*)d_in[12], (char*)d_ws, (const u16*)d_in[1], (const u16*)d_in[2],
      (const u16*)d_in[3], (const u16*)d_in[4], (const u16*)d_in[5], (const u16*)d_in[6], (const u16*)d_in[7],
      (const u16*)d_in[8], (const u16*)d_in[11], (const u16*)d_in[13], (u16*)d_out);
}
```

```cpp
#include <hip/hip_runtime.h>
#include <hip/hip_bf16.h>

typedef unsigned int u32;
typedef unsigned short u16;
typedef u32 v4u __attribute__((ext_vector_type(4)));
typedef u32 v2u __attribute__((ext_vector_type(2)));
typedef float f32x2 __attribute__((ext_vector_type(2)));
typedef float f32x4 __attribute__((ext_vector_type(4)));
typedef float f32x16 __attribute__((ext_vector_type(16)));
typedef short s16x4 __attribute__((ext_vector_type(4)));
typedef short s16x8 __attribute__((ext_vector_type(8)));
typedef __bf16 bf16x2 __attribute__((ext_vector_type(2)));
#define GAS __attribute__((address_space(1)))
#define LAS __attribute__((address_space(3)))
#define DEV __device__ __forceinline__
#define RLX_AGENT __ATOMIC_RELAXED, __HIP_MEMORY_SCOPE_AGENT
#define SC1 16
#define TS(i) do { } while (0)
#define TC(i) do { } while (0)

constexpr int WS_E = 0;
constexpr int FL = 32;
constexpr int WS_STAT = 4096;
constexpr int WS_FXN = 65536;
constexpr int WS_FW2 = WS_FXN + 256 * 128;
constexpr int WS_FH = WS_FW2 + 1024 * 128;
constexpr int WS_FS = WS_FH + 1024 * 128;
constexpr int WS_XNT = 524288;
constexpr int WS_W2T = WS_XNT + 2097152;
constexpr int WS_HT = WS_W2T + 1835008;
constexpr int WS_ST = WS_HT + 7340032;
constexpr int WS_END = WS_ST + 2097152;
constexpr int LD_Q = 98304;
constexpr int LD_KREG = 131072;
constexpr int LD_PS = LD_KREG + 7424;
constexpr int LD_DUMMY = LD_KREG + 7680;
constexpr int LD_B1 = LD_KREG + 8192 + 7424;
constexpr int LD_HST = LD_KREG + 3 * 8192 + 7424;
constexpr int LD_STAT = 12288;
constexpr int LD_W2S = 90112;
constexpr int LD_U = 0;
constexpr int LD_CX = 16384;
constexpr int LD_HT = 0;
constexpr int LD_HX = 65536;
constexpr int LD_RED = 0;
constexpr int LD_ST = 114688;
constexpr int ABL_HT = WS_HT;
constexpr int LDS_BYTES = 163840;
constexpr u32 SPIN_CAP = 1u << 16;
constexpr u32 CODE_BYTES = $$$$$$$$$$$$$$$$$$$$$$$$$$$$$$$$$$$$$$$$$$$$$$$$$$$$$$$$$$$$$$$$$$$$$$$$$$;

DEV float bf2f(u32 h) { return __builtin_bit_cast(float, h << 16); }
DEV float bflo(u32 d) { return __builtin_bit_cast(float, d << 16); }
DEV float bfhi(u32 d) { return __builtin_bit_cast(float, d & 0xffff0000u); }
DEV u32 pk2(float lo, float hi) {
  f32x2 v = {lo, hi};
  return __builtin_bit_cast(u32, __builtin_convertvector(v, bf16x2));
}
DEV u32 pk2v(f32x2 v) { return __builtin_bit_cast(u32, __builtin_convertvector(v, bf16x2)); }
DEV float bfr(float v) { return bflo(pk2(v, v)); }
DEV f32x4 mfma16(v4u a, v4u b, f32x4 c) {
  return __builtin_amdgcn_mfma_f32_16x16x32_bf16(__builtin_bit_cast(s16x8, a), __builtin_bit_cast(s16x8, b), c, 0, 0, 0);
}
DEV f32x16 mfma32(v4u a, v4u b, f32x16 c) {
  return __builtin_amdgcn_mfma_f32_32x32x16_bf16(__builtin_bit_cast(s16x8, a), __builtin_bit_cast(s16x8, b), c, 0, 0, 0);
}
DEV v2u trread(LAS char* p) { return __builtin_bit_cast(v2u, __builtin_amdgcn_ds_read_tr16_b64_v4i16((LAS s16x4*)p)); }
DEV v4u mk_rsrc(const void* p, u32 bytes, u32 fmt) {
  const unsigned long long a = (unsigned long long)p;
  v4u r = {(u32)a, (u32)(a >> 32), bytes, fmt};
  return r;
}
DEV void dma16(u32 voff, v4u rs, u32 soff, u32 lds) {
  asm volatile("s_mov_b32 m0, %0\n\ts_nop 0\n\tbuffer_load_dwordx4 %1, %2, %3 offen lds" ::"s"(lds), "v"(voff), "s"(rs), "s"(soff) : "memory");
}
#define WAITV(n) asm volatile("s_waitcnt vmcnt(" #n ")" ::: "memory")
#define WAITL() asm volatile("s_waitcnt lgkmcnt(0)" ::: "memory")
#define SCHED() __builtin_amdgcn_sched_barrier(0)
#define BAR() do { asm volatile("s_waitcnt lgkmcnt(0)" ::: "memory"); __builtin_amdgcn_s_barrier(); asm volatile("" ::: "memory"); SCHED(); } while (0)
#define ORDER_BEHIND_POLL() __builtin_amdgcn_fence(__ATOMIC_ACQUIRE, "wavefront")

DEV f32x2 cmul(f32x2 a, f32x2 z) {
  const f32x2 zr = {-z.y, z.x};
  f32x2 r;
  asm("v_pk_mul_f32 %0, %1, %2 op_sel:[1,0] op_sel_hi:[1,1]\n\tv_pk_fma_f32 %0, %1, %3, %0 op_sel_hi:[0,1,1]" : "=&v"(r) : "v"(a), "v"(zr), "v"(z));
  return r;
}
DEV float sadd(float a, float b) { float r; asm("v_add_f32 %0, %1, %2" : "=v"(r) : "v"(a), "v"(b)); return r; }
DEV float smul(float a, float b) { float r; asm("v_mul_f32 %0, %1, %2" : "=v"(r) : "v"(a), "v"(b)); return r; }
DEV f32x2 zpow(float la, float th, float k) {
  const float mag = __builtin_amdgcn_exp2f(la * k);
  const float fr = __builtin_amdgcn_fractf(th * k);
  f32x2 r = {mag * __builtin_amdgcn_cosf(fr), mag * __builtin_amdgcn_sinf(fr)};
  return r;
}
template <int SLEEP>
DEV void poll_flags(GAS u32* p, u32 epoch, u32 first) {
  if (!__all(first == epoch)) {
#pragma unroll 1
    for (u32 spins = 0; spins < SPIN_CAP; ++spins) {
      const u32 v = __hip_atomic_load(p, RLX_AGENT);
      if (__all(v == epoch)) break;
      __builtin_amdgcn_s_sleep(SLEEP);
    }
  }
  ORDER_BEHIND_POLL();
}
DEV void publish(GAS u32* p, u32 epoch, int lane) {
  WAITV(0);
  if (lane == 0) __hip_atomic_store(p, epoch, RLX_AGENT);
}
DEV void set_progress(LAS char* L, int wv, u32 k) {
  WAITL();
  *(volatile LAS u32*)(L + LD_PS + wv * 4) = k;
}
DEV void wait_progress(LAS char* L, int word, u32 k) {
#pragma unroll 1
  for (u32 spins = 0; spins < (1u << 22); ++spins) {
    const u32 pv = *(volatile LAS u32*)(L + LD_PS + word * 4);
    if (__all(pv >= k)) break;
    __builtin_amdgcn_s_sleep(1);
  }
  asm volatile("" ::: "memory");
}
DEV void ln_stats(float s1, float s2, float& mean, float& var, float& den1) {
  mean = s1 * (1.0f / 512.0f);
  var = fmaxf(s2 * (1.0f / 512.0f) - mean * mean, 0.0f);
  den1 = bfr(__builtin_amdgcn_sqrtf(bfr(bfr(var) + 1e-5f)));
}
DEV float pick4(f32x4 g, int k) { return k == 0 ? g.x : k == 1 ? g.y : k == 2 ? g.z : g.w; }
#define LD16(p, i) ((u32)__hip_atomic_load((const GAS u16*)(p) + (i), __ATOMIC_RELAXED, __HIP_MEMORY_SCOPE_WAVEFRONT))

__global__ void __launch_bounds__(512, 2)
s4_mixer_kernel(const u16* __restrict__ x, u32 x_bytes, u32 x_fmt, const u16* __restrict__ w1, u32 w1_bytes, u32 w1_fmt,
                const u16* __restrict__ w2, char* __restrict__ ws,
                const u16* __restrict__ A_re, const u16* __restrict__ A_im, const u16* __restrict__ B_re,
                const u16* __restrict__ B_im, const u16* __restrict__ C_re, const u16* __restrict__ C_im,
                const u16* __restrict__ log_dt, const u16* __restrict__ Dskip, const u16* __restrict__ b1,
                const u16* __restrict__ b2, u16* __restrict__ out) {
  extern __shared__ __attribute__((aligned(16))) char smem[];
  LAS char* const L = (LAS char*)smem;
  unsigned long long pc;
  asm volatile("s_getpc_b64 %0" : "=s"(pc));
  {
    u32 coff = ((((threadIdx.x & 63) << 3) | (threadIdx.x >> 6)) * 32u + (blockIdx.x >> 3)) * 64u;
    coff = coff < CODE_BYTES ? coff : 0u;
    asm volatile("s_mov_b32 m0, %0\n\ts_nop 0\n\tglobal_load_lds_dword %1, %2" ::"s"((u32)LD_DUMMY), "v"(coff), "s"(pc) : "memory");
    SCHED();
  }
  const int tid = threadIdx.x, lane = tid & 63;
  const int wv = __builtin_amdgcn_readfirstlane(tid >> 6);
  const int w = wv & 3;
  const bool swave = wv >= 4;
  const int b = blockIdx.x, xcd = b & 7, idx = b >> 3;
  const int r16 = lane & 15, q = lane >> 4, qq = r16 >> 2, p4 = lane & 3;
  const bool helper = idx >= 28;
  TS(0);
  const v4u rsX = mk_rsrc(x, x_bytes, x_fmt);
  const v4u rsW1 = mk_rsrc(w1, w1_bytes, w1_fmt);
  asm volatile("" ::"s"(rsX), "s"(rsW1), "s"(w2), "s"(ws), "s"(A_re), "s"(A_im), "s"(B_re), "s"(B_im), "s"(C_re), "s"(C_im), "s"(log_dt), "s"(Dskip), "s"(b1));
  const u32 eraw = *(const __attribute__((address_space(4))) u32*)(ws + WS_E);
  const int mq = (idx * 37) >> 8, mr = idx - 7 * mq;
  const int mt = (xcd >> 1) * 4 + mq, nt = (xcd & 1) * 7 + mr;
  const int hid = xcd * 4 + (idx - 28);
  const int tg = xcd * 4 + (idx >> 3), cg = idx & 7;
  const int cw = w >> 1, hw = w & 1;
  const int ch = 64 * xcd + 2 * idx + cw;
  const __amdgpu_buffer_rsrc_t rsWS = __builtin_amdgcn_make_buffer_rsrc((void*)ws, 0, WS_END, 0x00020000);
  GAS u32* const ctl = (GAS u32*)ws;
  const v4u ones = {0x3f803f80u, 0x3f803f80u, 0x3f803f80u, 0x3f803f80u};
  int rot2 = cg + 2 * (tg & 3);
  rot2 -= rot2 >= 7 ? 7 : 0;
#define KS2(s) (7 * wv + ((s) + rot2 >= 7 ? (s) + rot2 - 7 : (s) + rot2))
#define AW_LOAD(s, c) \
  aw[s][c] = __builtin_amdgcn_raw_buffer_load_b128(rsWS, lane * 16 + ((((c) + 2 * (wv & 1)) & 3) << 10), WS_W2T + (cg * 56 + KS2(s)) * 4096, SC1)
  const u32 wo = (u32)lane * 4u;
#define TW(rowp, row, val) *(LAS u32*)((rowp) + (wo ^ (u32)(((row) & 15) << 4))) = (val)
  LAS char* const PT = L + LD_KREG + w * 8192;

  auto gemm2_tail = [&](v4u (&aw)[7][4], const u32 epoch, GAS u32* const pend, u32 fh0) __attribute__((always_inline)) {
  {
    TS(9);
    int tz = threadIdx.x;
    asm volatile("" : "+v"(tz));
    const int lane = tz & 63, r16 = lane & 15, q = lane >> 4, qq = r16 >> 2, p4 = lane & 3;
    asm volatile("" : "+v"(fh0));
    poll_flags<2>(ctl + WS_FH / 4 + (tg * 32 + ((7 * wv) >> 1) + (lane & 3)) * FL, epoch, fh0);
    TS(10);
#define G2WIN 6
    constexpr int WIN = G2WIN;
    v4u hf[28];
#define H_LOAD(n) hf[n] = __builtin_amdgcn_raw_buffer_load_b128(rsWS, lane * 16 + (((((n) & 3) + 1 + (wv >> 1)) & 3) << 10), WS_HT + (tg * 56 + KS2((n) >> 2)) * 4096, SC1)
#pragma unroll
    for (int n = 0; n < WIN; ++n) { H_LOAD(n); SCHED(); }
    if (pend) {
      asm volatile("s_waitcnt vmcnt(%0)" ::"n"(WIN) : "memory");
      if (lane == 0) __hip_atomic_store(pend, epoch, RLX_AGENT);
      __builtin_amdgcn_s_setprio(0);
      SCHED();
    }
    GAS u32* const fsp = ctl + WS_FS / 4 + ((tg >> 4) * 512 + cg * 64 + wv * 8 + (lane >> 3)) * FL;
    const int tok = tg * 64 + 16 * (wv >> 1) + r16;
    const int c8 = cg * 64 + 32 * (wv & 1) + 8 * q;
    u32 fs, fe0 = epoch, fe1 = epoch;
    bool early;
    v4u xv, bv, sv;
    f32x4 acc[4][4];
#pragma unroll
    for (int i = 0; i < 4; ++i)
#pragma unroll
      for (int j = 0; j < 4; ++j) acc[i][j] = f32x4{0.f, 0.f, 0.f, 0.f};
#define RED_WRITE(j)                                                                                                  \
  _Pragma("unroll") for (int cp = 0; cp < 2; ++cp) if ((j) < 3 || cp) {                                               \
    const int d = ((((j) + 1 + (wv >> 1)) & 3) << 1) | ((cp + wv) & 1);                                               \
    _Pragma("unroll") for (int e = 0; e < 2; ++e)                                                                     \
      *(LAS f32x4*)(L + LD_RED + (d * 7 + ((j) < 3 ? 2 * (j) + cp : 6)) * 2048 + e * 1024 + lane * 16) = acc[2 * cp + e][j]; \
  }
#pragma unroll
    for (int n = 0; n < 28; ++n) {
#pragma unroll
      for (int c = 0; c < 4; ++c) acc[c][n & 3] = mfma16(aw[n >> 2][c], hf[n], acc[c][n & 3]);
      SCHED();
      if (n == 22) wait_progress(L, lane & 7, 4u);
      if (n >= 25) RED_WRITE(n - 25)
      if (n == 24 - 2 * WIN + 4) fs = __hip_atomic_load(fsp, RLX_AGENT);
      if (n + WIN < 28) H_LOAD(n + WIN);
      if (n == 27 - WIN) {
        xv = *(const GAS v4u*)(x + tok * 512 + c8);
        bv = *(const GAS v4u*)(b2 + c8);
      }
      if (n == 28 - WIN) {
        early = __all(fs == epoch);
        ORDER_BEHIND_POLL();
        sv = __builtin_amdgcn_raw_buffer_load_b128(rsWS, (lane >> 3) * 4096 + (lane & 7) * 16, early ? WS_ST + (cg * 64 + wv * 8) * 4096 + tg * 128 : WS_END, SC1);
        if (b == 0) {
          fe0 = __hip_atomic_load(ctl + WS_FS / 4 + tz * FL, RLX_AGENT);
          fe1 = __hip_atomic_load(ctl + WS_FS / 4 + (512 + tz) * FL, RLX_AGENT);
        }
      }
      SCHED();
    }
    TS(11);
    RED_WRITE(3)
    if (!early) {
      poll_flags<2>(fsp, epoch, epoch + 1u);
      sv = __builtin_amdgcn_raw_buffer_load_b128(rsWS, (lane >> 3) * 4096 + (lane & 7) * 16, WS_ST + (cg * 64 + wv * 8) * 4096 + tg * 128, SC1);
    }
    *(LAS v4u*)(L + LD_ST + wv * 1024 + lane * 16) = sv;
    TS(12);
    asm volatile("" : "+v"(fe0), "+v"(fe1));
    poll_flags<2>(ctl + WS_FS / 4 + tz * FL, epoch, fe0);
    poll_flags<2>(ctl + WS_FS / 4 + (512 + tz) * FL, epoch, fe1);
    BAR();
    if (b == 0 && tz == 0) __hip_atomic_store(ctl + WS_E / 4, epoch, RLX_AGENT);
    TS(13);
    f32x4 part[2][7];
    v2u sq[2];
#pragma unroll
    for (int e = 0; e < 2; ++e) {
#pragma unroll
      for (int j = 0; j < 7; ++j) part[e][j] = *(LAS f32x4*)(L + LD_RED + (wv * 7 + j) * 2048 + e * 1024 + lane * 16);
      sq[e] = trread(L + LD_ST + (32 * (wv & 1) + 8 * q + 4 * e + qq) * 128 + (16 * (wv >> 1) + 4 * p4) * 2);
    }
    SCHED();
    u32 o[4];
#pragma unroll
    for (int e = 0; e < 2; ++e) {
      const u32 ba = bv[2 * e], bb = bv[2 * e + 1], xa = xv[2 * e], xb = xv[2 * e + 1];
      f32x4 m = acc[e][3] + f32x4{bflo(ba), bfhi(ba), bflo(bb), bfhi(bb)};
#pragma unroll
      for (int j = 0; j < 7; ++j) m += part[e][j];
      f32x4 g;
#pragma unroll
      for (int k = 0; k < 4; ++k) g[k] = m[k] * __builtin_amdgcn_rcpf(1.0f + __builtin_amdgcn_exp2f(m[k] * -1.4426950408889634f));
      o[2 * e] = pk2(bflo(xa) + bflo(sq[e].x) * g.x, bfhi(xa) + bfhi(sq[e].x) * g.y);
      o[2 * e + 1] = pk2(bflo(xb) + bflo(sq[e].y) * g.z, bfhi(xb) + bfhi(sq[e].y) * g.w);
    }
    *(LAS v4u*)(L + LD_KREG + (16 * (wv >> 1) + r16) * 144 + (wv & 1) * 64 + q * 16) = v4u{o[0], o[1], o[2], o[3]};
    BAR();
    const v4u ov = *(LAS v4u*)(L + LD_KREG + (8 * wv + (lane >> 3)) * 144 + (lane & 7) * 16);
    __builtin_amdgcn_raw_buffer_store_b128(ov, __builtin_amdgcn_make_buffer_rsrc((void*)out, 0, 2097152, 0x00020000), ((tg * 64 + 8 * wv + (lane >> 3)) * 512 + cg * 64 + (lane & 7) * 8) * 2, 0, 16);
  }
  TS(14);
  __builtin_amdgcn_endpgm();
  };

  if (helper) {
    LAS char* const HX = L + LD_HX + wv * 8192;
#pragma unroll
    for (int p = 0; p < 8; ++p)
      dma16((u32)(wv * 8192 + ((lane ^ (((p & 3) << 2) | (p >> 2))) << 4)), rsX, (u32)((hid * 64 + p) * 1024), (u32)(LD_HX + (wv * 8 + p) * 1024));
    u32 epoch;
    asm volatile("s_waitcnt vmcnt(0) lgkmcnt(0)\n\ts_add_u32 %0, %1, 1" : "=s"(epoch) : "s"(eraw) : "memory", "scc");
    if (!swave) TS(3);
    f32x4 gram[2], sumx[2];
#pragma unroll
    for (int i = 0; i < 2; ++i) { gram[i] = f32x4{0.f, 0.f, 0.f, 0.f}; sumx[i] = gram[i]; }
    const int r8 = lane & 7, fr = ((r8 & 3) << 2) | (r8 >> 2);
    v4u xr[8];
#pragma unroll
    for (int s = 0; s < 8; ++s) xr[s] = *(LAS v4u*)(HX + r8 * 1024 + (lane & 8) * 64 + (((4 * s + q) ^ fr) << 4));
    v2u d[8][2];
#pragma unroll
    for (int s = 0; s < 8; ++s)
#pragma unroll
      for (int t = 0; t < 2; ++t)
        d[s][t] = trread(HX + (4 * t + qq) * 1024 + (((8 * s + 2 * q + (p4 >> 1)) ^ ((qq << 2) | t)) << 4) + 8 * (p4 & 1));
    SCHED();
#pragma unroll
    for (int s = 0; s < 8; ++s) {
      gram[s & 1] = mfma16(xr[s], xr[s], gram[s & 1]);
      sumx[s & 1] = mfma16(ones, xr[s], sumx[s & 1]);
    }
    float s1 = sumx[0].x + sumx[1].x, s2 = pick4(gram[0] + gram[1], r16 & 3);
    s1 += __shfl(s1, (lane + 40) & 63);
    s2 += __shfl(s2, (lane + 40) & 63);
    float mean, var, den1;
    ln_stats(s1, s2, mean, var, den1);
    const float rd = __builtin_amdgcn_rcpf(den1);
    const float den2 = bfr(__builtin_amdgcn_sqrtf(bfr(bfr(var * rd * rd) + 1e-5f)));
    const float R = __builtin_amdgcn_rcpf(den1 * den2);
    if (q == qq && r16 < 8) {
      *(LAS f32x2*)(L + LD_HST + (8 * wv + r16) * 8) = f32x2{rd, -bfr(mean) * rd};
      __builtin_amdgcn_raw_buffer_store_b128(v4u{__builtin_bit_cast(u32, R), epoch, __builtin_bit_cast(u32, R * mean), epoch}, rsWS,
                                             WS_STAT + (hid * 64 + 8 * wv + r16) * 16, 0, SC1);
    }
    WAITL();
    if (!swave) TS(4);
    f32x4 sv[4];
#pragma unroll
    for (int i = 0; i < 4; ++i) sv[i] = *(LAS f32x4*)(L + LD_HST + wv * 64 + i * 16);
#pragma unroll
    for (int cb = 0; cb < 8; ++cb) {
      v4u o;
      o.x = pk2(bflo(d[cb][0].x) * sv[0].x + sv[0].y, bfhi(d[cb][0].x) * sv[0].z + sv[0].w);
      o.y = pk2(bflo(d[cb][0].y) * sv[1].x + sv[1].y, bfhi(d[cb][0].y) * sv[1].z + sv[1].w);
      o.z = pk2(bflo(d[cb][1].x) * sv[2].x + sv[2].y, bfhi(d[cb][1].x) * sv[2].z + sv[2].w);
      o.w = pk2(bflo(d[cb][1].y) * sv[3].x + sv[3].y, bfhi(d[cb][1].y) * sv[3].z + sv[3].w);
      *(LAS v4u*)(L + LD_HT + (cb * 64 + lane) * 128 + ((wv ^ (lane & 7)) << 4)) = o;
    }
    if (!swave) TS(5);
    BAR();
    if (swave) {
      v4u o[16];
#pragma unroll
      for (int i = 0; i < 16; ++i) o[i] = *(LAS v4u*)(L + LD_HT + (w * 128 + i * 8 + (lane >> 3)) * 128 + (((lane ^ (lane >> 3)) & 7) << 4));
      SCHED();
#pragma unroll
      for (int i = 0; i < 16; ++i) __builtin_amdgcn_raw_buffer_store_b128(o[i], rsWS, lane * 16 + WS_XNT + hid * 65536 + (w * 16 + i) * 1024, 0, SC1);
      publish(ctl + WS_FXN / 4 + (hid * 4 + w) * FL, epoch, lane);
    }
    if (!swave) TS(6);
    SCHED();
  }

  if (swave) {
    __builtin_amdgcn_s_setprio(2);
    const u32 voX = (u32)((32 * w + (lane >> 3)) * 1024 + (((lane & 7) ^ (lane >> 3)) << 4));
    const u32 voW = (u32)((4 * w + q) * 3584 + ((r16 ^ ((q << 2) | w)) << 4));
    const int rot = (mr + 2 * mq) & 7;
#define G1_STAGE(st) G1_PART(st, 0, 4)
#define G1_PART(st, p0, p1)                                                                                  \
  {                                                                                                          \
    const int sa_ = ((st) + rot) & 7;                                                                        \
    _Pragma("unroll") for (int p = p0; p < p1; ++p) {                                                        \
      dma16(voX, rsX, (u32)(mt * 131072 + p * 8192 + sa_ * 128), (u32)(((st) & 3) * 32768 + w * 4096 + p * 1024)); \
      dma16(voW, rsW1, (u32)((sa_ * 64 + 16 * p) * 3584 + nt * 256), (u32)(((st) & 3) * 32768 + 16384 + p * 4096 + w * 1024)); \
    }                                                                                                        \
  }
    if (!helper) { G1_STAGE(0) G1_STAGE(1) G1_PART(2, 0, 2) }
    if (lane == 0) *(LAS u32*)(L + LD_PS + wv * 4) = 0u;
    TS(1);
    WAITV(12);
    u32 epoch;
    asm volatile("s_waitcnt lgkmcnt(0)\n\ts_add_u32 %0, %1, 1" : "=s"(epoch) : "s"(eraw) : "memory", "scc");
    TS(2);
    BAR();
    TS(3);
    TC(0);
    if (!helper) G1_PART(2, 2, 4)
    const int r32 = lane & 31, hh = lane >> 5;
    v4u pf[8], qf[8];
    {
#pragma unroll
      for (int s = 0; s < 8; ++s) {
        qf[s] = *(LAS v4u*)(L + LD_Q + w * 8192 + r32 * 256 + (((2 * s + hh) ^ (r32 & 15)) << 4));
        pf[s] = *(LAS v4u*)(PT + (2 * s + hh) * 464 + r32 * 16);
      }
    }
    SCHED();
    TC(1);
    if (!helper) {
      WAITV(8); BAR(); TC(2); G1_STAGE(3) G1_PART(4, 0, 2) TC(3);
      WAITV(12); BAR(); TC(4); G1_PART(4, 2, 4) G1_STAGE(5) TC(5);
      WAITV(16); BAR(); TC(6);
      G1_STAGE(6) TC(7);
      WAITV(16); BAR(); TC(8); G1_STAGE(7) TC(9);
      WAITV(16); BAR();
    }
    TC(10);
    GAS u32* const fxnp = ctl + WS_FXN / 4 + ((hw * 16 + r16) * 4 + (ch >> 7)) * FL;
    GAS u32* const fw2p = ctl + WS_FW2 / 4 + ((cg * 56 + 7 * (w + 4 * ((lane >> 4) & 1))) * 2 + min(r16, 13)) * FL;
    const int sto = WS_STAT + ((helper ? 0 : mt) * 128 + 32 * w + r32) * 16;
    v4u sg = __builtin_amdgcn_raw_buffer_load_b128(rsWS, sto, 0, SC1);
    const u32 fw2 = __hip_atomic_load(fw2p, RLX_AGENT);
    const u32 pDs = LD16(Dskip, ch);
    SCHED();
    f32x16 kacc;
#pragma unroll
    for (int i = 0; i < 16; ++i) kacc[i] = 0.f;
#pragma unroll
    for (int s = 0; s < 8; ++s) kacc = mfma32(pf[s], qf[s], kacc);
    SCHED();
    TC(11);
    if (!helper) { WAITV(3); BAR(); }
    TC(12);
    TS(4);
#pragma unroll
    for (int i = 0; i < 4; ++i)
      *(LAS f32x4*)(L + LD_CX + w * 4096 + i * 1024 + lane * 16) = f32x4{kacc[4 * i], kacc[4 * i + 1], kacc[4 * i + 2], kacc[4 * i + 3]};
    if (!helper) {
      if (!__all(sg.y == epoch && sg.w == epoch)) {
#pragma unroll 1
        for (u32 spins = 0; spins < SPIN_CAP; ++spins) {
          __builtin_amdgcn_s_sleep(4);
          asm volatile("" ::: "memory");
          sg = __builtin_amdgcn_raw_buffer_load_b128(rsWS, sto, 0, SC1);
          if (__all(sg.y == epoch && sg.w == epoch)) break;
        }
      }
      *(LAS v2u*)(L + LD_STAT + (32 * w + r32) * 8) = v2u{sg.x, sg.z};
    }
    poll_flags<4>(fw2p, epoch, fw2);
    set_progress(L, wv, 1u);
    TS(5);
    v4u aw[7][4];
#pragma unroll
    for (int c = 0; c < 4; ++c) AW_LOAD(c >> 2, c & 3);
    SCHED();
    wait_progress(L, wv ^ 1, 1u);
    {
      f32x4 v[4];
#pragma unroll
      for (int i = 0; i < 4; ++i) v[i] = *(LAS f32x4*)(L + LD_CX + (w ^ 1) * 4096 + i * 1024 + lane * 16);
#pragma unroll
      for (int i = 0; i < 15; ++i) {
        kacc[i] = sadd(kacc[i], v[i >> 2][i & 3]);
      }
    }
    {
      const bool odd = r32 & 1, up = hh != 0;
      const int ia[4][9] = {{0, 1, 2, 3, 4, 5, 6, 7, 8}, {9, 9, 9, 9, -1, 10, 10, 10, 10}, {11, 11, 11, 11, -1, 12, 12, 12, 12}, {13, 13, 13, 13, -1, 14, 14, 14, 14}};
      const int ib[4][9] = {{0, 0, 1, 2, 2, 2, 3, 4, 4}, {5, 5, 5, 6, 6, 6, 7, 7, 7}, {8, 8, 8, 9, 9, 9, 10, 10, 10}, {11, 11, 11, 11, -1, 12, 12, 12, 12}};
      LAS char* const KC = PT + hw * 64 + (255 - r32) * 16;
#pragma unroll
      for (int u = 0; u < 4; ++u) {
        SCHED();
        AW_LOAD(1 + (u >> 1), 2 * (u & 1));
        SCHED();
        float e[9];
#pragma unroll
        for (int i = 0; i < 9; ++i) {
          const float va = ia[u][i] >= 0 ? kacc[ia[u][i]] : (odd ? kacc[ia[u][5]] : kacc[ia[u][3]]);
          const float vb = ib[u][i] >= 0 ? kacc[ib[u][i]] : (odd ? kacc[ib[u][5]] : kacc[ib[u][3]]);
          e[i] = up ? vb : va;
        }
        SCHED();
        AW_LOAD(1 + (u >> 1), 2 * (u & 1) + 1);
        SCHED();
        v4u d;
        if (hw) { d.x = pk2(e[8], e[7]); d.y = pk2(e[6], e[5]); d.z = pk2(e[4], e[3]); d.w = pk2(e[2], e[1]); }
        else    { d.x = pk2(e[7], e[6]); d.y = pk2(e[5], e[4]); d.z = pk2(e[3], e[2]); d.w = pk2(e[1], e[0]); }
        const int sa = u == 0 ? 0 : u + 3, sb = u == 3 ? 7 : u + 1;
        *(LAS v4u*)(KC - (up ? sb : sa) * 512) = d;
      }
      if (lane < 2) *(LAS v4u*)(PT + hw * 64 + 4096 + lane * 16) = v4u{(hw && lane == 0) ? pk2(kacc[0], 0.f) : 0u, 0u, 0u, 0u};
    }
    SCHED();
    TC(13);
    u32 fxa = __hip_atomic_load(fxnp, RLX_AGENT);
#pragma unroll
    for (int c = 12; c < 22; ++c) AW_LOAD(c >> 2, c & 3);
    SCHED();
    asm volatile("" : "+v"(fxa));
    const bool xearly = __all(fxa == epoch);
    ORDER_BEHIND_POLL();
    const int xo = WS_XNT + (16 * hw) * 65536 + ch * 128;
    v4u u0 = __builtin_amdgcn_raw_buffer_load_b128(rsWS, (lane >> 3) * 65536 + (lane & 7) * 16, xearly ? xo : WS_END, SC1);
    v4u u1 = __builtin_amdgcn_raw_buffer_load_b128(rsWS, (lane >> 3) * 65536 + (lane & 7) * 16, xearly ? xo + 8 * 65536 : WS_END, SC1);
#pragma unroll
    for (int c = 22; c < 28; ++c) AW_LOAD(c >> 2, c & 3);
    SCHED();
    if (!xearly) {
      poll_flags<8>(fxnp, epoch, epoch + 1u);
      u0 = __builtin_amdgcn_raw_buffer_load_b128(rsWS, (lane >> 3) * 65536 + (lane & 7) * 16, xo, SC1);
      u1 = __builtin_amdgcn_raw_buffer_load_b128(rsWS, (lane >> 3) * 65536 + (lane & 7) * 16, xo + 8 * 65536, SC1);
      __builtin_amdgcn_s_waitcnt(0x0F70);
    }
    LAS char* const UB = L + LD_U + cw * 4608;
    if (hw == 0 && lane < 32) *(LAS v4u*)(UB + lane * 16) = v4u{0u, 0u, 0u, 0u};
    *(LAS v4u*)(UB + 512 + (128 * hw + lane) * 16) = u0;
    *(LAS v4u*)(UB + 512 + (128 * hw + 64 + lane) * 16) = u1;
    set_progress(L, wv, 2u);
    {
      const u32 po = (u32)(WS_W2T + (cg * 56 + 7 * w) * 4096 + ((tg & 3) * 56 + min(lane, 55)) * 128);
      asm volatile("s_mov_b32 m0, %0\n\ts_nop 0\n\tglobal_load_lds_dword %1, %2 sc1" ::"s"((u32)LD_DUMMY), "v"(po), "s"(ws) : "memory");
    }
    SCHED();
    TS(6);
    const float dskip = bf2f(pDs);
    const int sgm = 1 - (r16 & 1);
    LAS char* const ka = L + LD_KREG + (2 * cw + sgm) * 8192 + sgm * 64 + 2 * (2031 - r16 + 8 * q + sgm) - hw * 256 - 7 * 512 - 3 * 64;
    LAS char* const ub = UB + 2 * (256 - 16 + 16 * r16 + 8 * q) - hw * 256 - 3 * 64;
    u32 kb[4];
#pragma unroll
    for (int i = 0; i < 4; ++i) { kb[i] = (u32)(size_t)ka + i * 1024; asm volatile("" : "+v"(kb[i])); }
    f32x4 y[8];
    {
      f32x4 y0[4];
#pragma unroll
      for (int i = 0; i < 4; ++i) {
        const v2u xv = *(LAS v2u*)(UB + 512 + (256 * (4 * hw + i) + 16 * r16 + 4 * q) * 2);
        y0[i] = f32x4{smul(bflo(xv.x), dskip), smul(bfhi(xv.x), dskip), smul(bflo(xv.y), dskip), smul(bfhi(xv.y), dskip)};
      }
      const f32x4 zz = {0.f, 0.f, 0.f, 0.f};
      if (hw) {
#pragma unroll
        for (int i = 0; i < 4; ++i) { y[i] = zz; y[4 + i] = y0[i]; }
      } else {
#pragma unroll
        for (int i = 0; i < 4; ++i) { y[i] = y0[i]; y[4 + i] = zz; }
      }
#pragma unroll
      for (int i = 0; i < 8; ++i) asm volatile("" : "+v"(y[i]));
    }
    wait_progress(L, wv ^ 1, 2u);
    v4u af[4][8], bfv[4][8];
#define CV_READ(rr, g)                                                                                   \
  {                                                                                                      \
    LAS u32* pa = (LAS u32*)(size_t)(kb[(7 - (g)) >> 1] + ((7 - (g)) & 1) * 512 + (3 - (rr)) * 64);      \
    af[rr][g] = v4u{pa[0], pa[1], pa[2], pa[3]};                                                         \
    bfv[rr][g] = *(LAS v4u*)(ub + (g) * 512 + (3 - (rr)) * 64);                                          \
  }
#define CV_LEVEL(rr, k, T0, T1)                                                              \
  _Pragma("unroll") for (int o = 0; o <= (k); ++o)                                           \
    if ((k) + o >= (T0) && (k) + o < (T1)) {                                                 \
      y[(k) + o] = mfma16(af[rr][k], bfv[rr][o], y[(k) + o]);                                \
      if (o < (k)) y[(k) + o] = mfma16(af[rr][o], bfv[rr][k], y[(k) + o]);                   \
    }
#define CV_MIX(n)                                                                            \
  _Pragma("unroll") for (int t = 0; t < (n); ++t) {                                          \
    __builtin_amdgcn_sched_group_barrier(0x008, 1, 0);                                       \
    __builtin_amdgcn_sched_group_barrier(0x100, 1, 0);                                       \
  }                                                                                          \
  SCHED();
#pragma unroll
    for (int g = 0; g < 4; ++g) CV_READ(0, g)
    SCHED();
    wait_progress(L, w, 4u);
    __builtin_amdgcn_s_setprio(2);
    TS(7);
    TC(14);
#pragma unroll
    for (int rr = 0; rr < 3; ++rr) {
      CV_READ(rr, 4) CV_READ(rr, 5) CV_READ(rr, 6) CV_READ(rr, 7)
      CV_LEVEL(rr, 0, 0, 8) CV_LEVEL(rr, 1, 0, 8) CV_LEVEL(rr, 2, 0, 8) CV_LEVEL(rr, 3, 0, 8)
      CV_MIX(12)
      CV_READ(rr + 1, 0) CV_READ(rr + 1, 1) CV_READ(rr + 1, 2) CV_READ(rr + 1, 3)
      CV_LEVEL(rr, 4, 0, 8) CV_LEVEL(rr, 5, 0, 8) CV_LEVEL(rr, 6, 0, 8) CV_LEVEL(rr, 7, 0, 8)
      CV_MIX(12)
    }
    f32x4 mine[4];
    CV_READ(3, 4) CV_READ(3, 5) CV_READ(3, 6) CV_READ(3, 7)
    SCHED();
    if (hw) {
      CV_LEVEL(3, 0, 0, 4) CV_LEVEL(3, 1, 0, 4) CV_LEVEL(3, 2, 0, 4) CV_LEVEL(3, 3, 0, 4)
      CV_LEVEL(3, 2, 4, 8) CV_LEVEL(3, 3, 4, 8)
      SCHED();
#pragma unroll
      for (int i = 0; i < 4; ++i) *(LAS f32x4*)(L + LD_CX + w * 4096 + i * 1024 + lane * 16) = y[i];
      *(volatile LAS u32*)(L + LD_PS + wv * 4) = 3u;
      SCHED();
      CV_LEVEL(3, 4, 4, 8) CV_LEVEL(3, 5, 4, 8) CV_LEVEL(3, 6, 4, 8) CV_LEVEL(3, 7, 4, 8)
#pragma unroll
      for (int i = 0; i < 4; ++i) mine[i] = y[4 + i];
    } else {
      CV_LEVEL(3, 2, 4, 8) CV_LEVEL(3, 3, 4, 8) CV_LEVEL(3, 4, 4, 8) CV_LEVEL(3, 5, 4, 8) CV_LEVEL(3, 6, 4, 8) CV_LEVEL(3, 7, 4, 8)
      CV_LEVEL(3, 0, 0, 4) CV_LEVEL(3, 1, 0, 4)
      SCHED();
#pragma unroll
      for (int i = 0; i < 4; ++i) *(LAS f32x4*)(L + LD_CX + w * 4096 + i * 1024 + lane * 16) = y[4 + i];
      *(volatile LAS u32*)(L + LD_PS + wv * 4) = 3u;
      SCHED();
      CV_LEVEL(3, 2, 0, 4) CV_LEVEL(3, 3, 0, 4)
#pragma unroll
      for (int i = 0; i < 4; ++i) mine[i] = y[i];
    }
    SCHED();
    TC(15);
    const u32 fh0 = __hip_atomic_load(ctl + WS_FH / 4 + (tg * 32 + ((7 * wv) >> 1) + (lane & 3)) * FL, RLX_AGENT);
    SCHED();
    wait_progress(L, wv ^ 1, 3u);
    f32x4 oy[4];
#pragma unroll
    for (int i = 0; i < 4; ++i) oy[i] = *(LAS f32x4*)(L + LD_CX + (w ^ 1) * 4096 + i * 1024 + lane * 16);
    set_progress(L, wv, 4u);
#pragma unroll
    for (int i = 0; i < 4; ++i) {
      v2u o;
      o.x = pk2(sadd(mine[i].x, oy[i].x), sadd(mine[i].y, oy[i].y));
      o.y = pk2(sadd(mine[i].z, oy[i].z), sadd(mine[i].w, oy[i].w));
      __builtin_amdgcn_raw_buffer_store_b64(o, rsWS, (16 * r16 + 4 * q) * 2 + WS_ST + ch * 4096 + (4 * hw + i) * 512, 0, SC1);
    }
    TS(8);
    gemm2_tail(aw, epoch, ctl + WS_FS / 4 + (hw * 512 + ch) * FL, fh0);
  } else {
    if (!helper) __builtin_amdgcn_s_setprio(3);
    const int po = ch * 128 + hw * 64 + lane;
    const u32 pLd = LD16(log_dt, ch), pAr = LD16(A_re, po), pAi = LD16(A_im, po);
    const u32 pBr = LD16(B_re, po), pBi = LD16(B_im, po), pCr = LD16(C_re, po), pCi = LD16(C_im, po);
    const u32 pB1 = LD16(b1, (helper ? 0 : nt * 128) + (tid & 127));
    const int uks = 2 * (helper ? 0 : idx) + (w >> 1), upp = w & 1;
    v4u w2a, w2b;
    {
      const GAS v4u* src = (const GAS v4u*)(w2 + (uks * 32 + (lane >> 2)) * 512 + xcd * 64 + upp * 32 + (lane & 3) * 8);
      asm volatile("" : "+v"(src));
      w2a = src[0];
      w2b = src[16 * 64];
    }
    if (lane == 0) *(LAS u32*)(L + LD_PS + wv * 4) = 0u;
    SCHED();
    TS(1);
    const int mi = w >> 1, ni = w & 1;
    f32x4 acc[4][4], csum[4];
#pragma unroll
    for (int i = 0; i < 4; ++i) {
#pragma unroll
      for (int j = 0; j < 4; ++j) { acc[i][j] = f32x4{0.f, 0.f, 0.f, 0.f}; asm volatile("" : "+v"(acc[i][j])); }
      csum[i] = f32x4{0.f, 0.f, 0.f, 0.f};
      asm volatile("" : "+v"(csum[i]));
    }
    u32 xb[2][2], wb[2][4][2];
#pragma unroll
    for (int h = 0; h < 2; ++h) {
#pragma unroll
      for (int k = 0; k < 2; ++k) {
        xb[h][k] = (u32)(((64 * mi + r16) * 128 + ((q ^ (r16 & 7)) << 4)) ^ (k * 64)) + h * 65536;
        asm volatile("" : "+v"(xb[h][k]));
      }
#pragma unroll
      for (int j = 0; j < 4; ++j)
#pragma unroll
        for (int t = 0; t < 2; ++t) {
          wb[h][j][t] = (u32)(16384 + (8 * q + 4 * t + qq) * 256 + (((2 * (4 * ni + j) + (p4 >> 1)) ^ ((qq << 2) | ((2 * q + t) & 3))) << 4) + 8 * (p4 & 1)) + h * 65536;
          asm volatile("" : "+v"(wb[h][j][t]));
        }
    }
    v4u onev = ones;
    asm volatile("" : "+v"(onev));
    u32 qad[16];
#pragma unroll
    for (int r = 0; r < 16; ++r) { qad[r] = (u32)(LD_Q + w * 8192) + (wo ^ (u32)(r << 4)); asm volatile("" : "+v"(qad[r])); }
    SCHED();
    TC(1);
    u32 epoch;
    asm volatile("s_waitcnt lgkmcnt(0)\n\ts_add_u32 %0, %1, 1" : "=s"(epoch) : "s"(eraw) : "memory", "scc");
    SCHED();
    {
      const float dt = bfr(__builtin_amdgcn_exp2f(bf2f(pLd) * 1.4426950408889634f));
      const float la = dt * bf2f(pAr) * 1.4426950408889634f;
      const float th = dt * bf2f(pAi) * 0.15915494309189535f;
      const f32x2 dBc = {dt * bf2f(pBr), -dt * bf2f(pBi)};
      const f32x2 Ccc = {bf2f(pCr), -bf2f(pCi)};
      const f32x2 z8 = zpow(la, th, 8.f), z16 = zpow(la, th, 16.f);
      const f32x2 y1 = zpow(la, -th, 1.f), y256 = zpow(la, -th, 256.f), y8 = {z8.x, -z8.y};
      const f32x2 y2 = cmul(y1, y1), y4 = cmul(y2, y2);
#define PROW(h, g) (((g) & 3) + 8 * ((g) >> 2) + 4 * (h))
#define PW(h, g, v) *(LAS u32*)(PT + (lane >> 2) * 464 + (lane & 3) * 4 + PROW(h, g) * 16) = pk2v(v)
      f32x2 W[8];
      W[0] = cmul(Ccc, dBc);
#pragma unroll
      for (int c = 1; c < 8; ++c) W[c] = cmul(W[c - 1], y256);
      {
        f32x2 te = W[0], to = cmul(W[0], y1), t1 = W[1];
        PW(0, 0, te); PW(0, 1, to); PW(1, 0, t1);
#pragma unroll
        for (int a = 1; a <= 4; ++a) {
          te = cmul(te, y2); PW(0, 2 * a, te);
          if (a < 4) { to = cmul(to, y2); PW(0, 2 * a + 1, to); }
          t1 = cmul(t1, y2); PW(1, a, t1);
        }
      }
      PW(1, 5, W[2]); PW(1, 6, cmul(W[2], y4)); PW(1, 7, cmul(W[2], y8));
      PW(1, 8, W[3]); PW(1, 9, cmul(W[3], y4)); PW(1, 10, cmul(W[3], y8));
      PW(0, 9, W[4]); PW(0, 10, cmul(W[4], y8));
      PW(0, 11, W[5]); PW(0, 12, cmul(W[5], y8));
      PW(0, 13, W[6]); PW(0, 14, cmul(W[6], y8));
      PW(1, 11, W[7]); PW(1, 12, cmul(W[7], y8));
      f32x2 qa = {1.f, 0.f}, qb = z8;
      u32 qv[32];
#pragma unroll
      for (int i = 0; i < 16; ++i) {
        qv[2 * i] = pk2v(qa);
        qv[2 * i + 1] = pk2v(qb);
        if (i < 15) { qa = (i == 0) ? z16 : cmul(qa, z16); qb = cmul(qb, z16); }
      }
#pragma unroll
      for (int r = 0; r < 16; ++r) {
        *(LAS u32*)(size_t)(qad[r] + r * 256) = qv[r];
        *(LAS u32*)(size_t)(qad[r] + (r + 16) * 256) = qv[r + 16];
      }
    }
    SCHED();
    TC(2);
    {
      LAS char* const WS2 = L + LD_W2S + w * 2048;
      *(LAS v4u*)(WS2 + lane * 16) = w2a;
      *(LAS v4u*)(WS2 + 1024 + lane * 16) = w2b;
#pragma unroll
      for (int e = 0; e < 2; ++e) {
        const v2u lo = trread(WS2 + (4 * q + qq) * 64 + 16 * p4 + 8 * e), hi = trread(WS2 + (16 + 4 * q + qq) * 64 + 16 * p4 + 8 * e);
        __builtin_amdgcn_raw_buffer_store_b128(v4u{lo.x, lo.y, hi.x, hi.y}, rsWS,
                                               lane * 16 + (helper ? WS_END : WS_W2T) + ((xcd * 56 + uks) * 4 + 2 * upp + e) * 1024, 0, SC1);
      }
    }
    *(LAS float*)(L + LD_B1 + (tid & 127) * 4) = bf2f(pB1);
    TC(3);
    TS(2);
    v4u aw[7][4];
    u32 fh0 = epoch + 1u;
    if (!helper) {
      v4u xf[2][4], wf[2][4];
#define G1_READ(n)                                                                                            \
  _Pragma("unroll") for (int i = 0; i < 4; ++i) {                                                             \
    const v2u lo = trread(L + (((n) >> 1) & 1) * 32768 + ((n) & 1) * 8192 + wb[((n) >> 2) & 1][i][0]);        \
    const v2u hi = trread(L + (((n) >> 1) & 1) * 32768 + ((n) & 1) * 8192 + wb[((n) >> 2) & 1][i][1]);        \
    wf[(n) & 1][i] = v4u{lo.x, lo.y, hi.x, hi.y};                                                             \
    xf[(n) & 1][i] = *(LAS v4u*)(L + (((n) >> 1) & 1) * 32768 + i * 2048 + xb[((n) >> 2) & 1][(n) & 1]);      \
  }
      __builtin_amdgcn_s_setprio(0);
      TC(4);
      BAR();
      TC(5);
      TS(3);
      G1_READ(0)
      SCHED();
#pragma unroll 1
      for (int it = 0; it < 2; ++it) {
#pragma unroll
        for (int n = 0; n < 8; ++n) {
          if (n & 1) {
            if (n < 4 || it == 0) BAR();
            if (n == 5 && it == 0) {
              __builtin_amdgcn_s_setprio(3);
              publish(ctl + WS_FW2 / 4 + ((xcd * 56 + uks) * 2 + upp) * FL, epoch, lane);
              __builtin_amdgcn_s_setprio(0);
            }
            SCHED();
          }
          G1_READ(n + 1)
#pragma unroll
          for (int j = 0; j < 4; ++j) {
#pragma unroll
            for (int i = 0; i < 4; ++i) acc[j][i] = mfma16(wf[n & 1][j], xf[n & 1][i], acc[j][i]);
            csum[j] = mfma16(wf[n & 1][j], onev, csum[j]);
          }
#pragma unroll
          for (int k = 0; k < 12; ++k) {
            __builtin_amdgcn_sched_group_barrier(0x008, 1, 0);
            __builtin_amdgcn_sched_group_barrier(0x100, 1, 0);
          }
          __builtin_amdgcn_sched_group_barrier(0x008, 8, 0);
          SCHED();
        }
      }
      TS(4);
      __builtin_amdgcn_s_setprio(3);
      wait_progress(L, 4 + (lane & 3), 1u);
      TS(5);
      float rs[4], rm[4];
      f32x4 bb[4];
#pragma unroll
      for (int i = 0; i < 4; ++i) {
        const f32x2 st = *(LAS f32x2*)(L + LD_STAT + (64 * mi + 16 * i + r16) * 8);
        rs[i] = st.x; rm[i] = -st.y;
        asm volatile("" : "+v"(rm[i]));
        bb[i] = *(LAS f32x4*)(L + LD_B1 + (64 * ni + 16 * i + 4 * q) * 4);
      }
      SCHED();
#pragma unroll
      for (int jp = 0; jp < 2; ++jp) {
#pragma unroll
        for (int i = 0; i < 4; ++i) {
          TC(8 + jp * 4 + i);
          f32x2 pre[4], ee[4];
#pragma unroll
          for (int k = 0; k < 4; ++k) {
            const f32x4 a4 = acc[2 * jp + (k >> 1)][i], c4 = csum[2 * jp + (k >> 1)], b4 = bb[2 * jp + (k >> 1)];
            const f32x2 a2 = (k & 1) ? f32x2{a4.z, a4.w} : f32x2{a4.x, a4.y};
            const f32x2 c2 = (k & 1) ? f32x2{c4.z, c4.w} : f32x2{c4.x, c4.y};
            const f32x2 b2v = (k & 1) ? f32x2{b4.z, b4.w} : f32x2{b4.x, b4.y};
            pre[k] = a2 * rs[i] + (c2 * rm[i] + b2v);
            f32x2 arg = pre[k] * (pre[k] * pre[k] * -0.1029432397f + -2.302208198f);
            asm("" : "+v"(arg));
            ee[k] = f32x2{__builtin_amdgcn_exp2f(arg.x), __builtin_amdgcn_exp2f(arg.y)};
          }
          u32 o[4];
#pragma unroll
          for (int k = 0; k < 4; ++k) {
            f32x2 dn = ee[k] + 1.0f;
            asm("" : "+v"(dn));
            o[k] = pk2v(pre[k] * f32x2{__builtin_amdgcn_rcpf(dn.x), __builtin_amdgcn_rcpf(dn.y)});
          }
          __builtin_amdgcn_raw_buffer_store_b128(v4u{o[0], o[1], o[2], o[3]}, rsWS,
                                                 lane * 16 + ABL_HT + (((2 * mt + mi) * 56 + nt * 4 + 2 * ni + jp) * 4 + i) * 1024, 0, SC1);
          SCHED();
        }
      }
      set_progress(L, wv, 4u);
      __builtin_amdgcn_s_setprio(0);
      TS(6);
#pragma unroll
      for (int c = 0; c < 8; ++c) AW_LOAD(c >> 2, c & 3);
      SCHED();
      __builtin_amdgcn_s_setprio(3);
      WAITV(8);
      if (lane == 0) __hip_atomic_store(ctl + WS_FH / 4 + ((2 * mt + mi) * 32 + 2 * nt + ni) * FL, epoch, RLX_AGENT);
      __builtin_amdgcn_s_setprio(0);
      SCHED();
      TS(7);
#pragma unroll
      for (int c = 8; c < 20; ++c) AW_LOAD(c >> 2, c & 3);
      fh0 = __hip_atomic_load(ctl + WS_FH / 4 + (tg * 32 + ((7 * wv) >> 1) + (lane & 3)) * FL, RLX_AGENT);
#pragma unroll
      for (int c = 20; c < 28; ++c) AW_LOAD(c >> 2, c & 3);
      SCHED();
    } else {
      __builtin_amdgcn_s_setprio(0);
      BAR();
      set_progress(L, wv, 4u);
      wait_progress(L, 4 + (lane & 3), 2u);
#pragma unroll
      for (int s = 0; s < 7; ++s)
#pragma unroll
        for (int c = 0; c < 4; ++c) AW_LOAD(s, c);
      SCHED();
    }
    TS(8);
    gemm2_tail(aw, epoch, nullptr, fh0);
  }
}

extern "C" void kernel_launch(void* const* d_in, const int* in_sizes, int n_in, void* d_out, int out_size, void* d_ws,
                              size_t ws_size, hipStream_t stream) {
  (void)in_sizes; (void)n_in; (void)out_size; (void)ws_size;
  (void)hipFuncSetAttribute((const void*)s4_mixer_kernel, hipFuncAttributeMaxDynamicSharedMemorySize, LDS_BYTES);
  s4_mixer_kernel<<<256, 512, LDS_BYTES, stream>>>(
      (const u16*)d_in[0], 2097152u, 0x00020000u, (const u16*)d_in[10], 1835008u, 0x00020000u, (const u16*)d_in[12], (char*)d_ws, (const u16*)d_in[1], (const u16*)d_in[2],
      (const u16*)d_in[3], (const u16*)d_in[4], (const u16*)d_in[5], (const u16*)d_in[6], (const u16*)d_in[7],
      (const u16*)d_in[8], (const u16*)d_in[11], (const u16*)d_in[13], (u16*)d_out);
}
```

```cpp
#define STAMP
#include <hip/hip_runtime.h>
#include <hip/hip_bf16.h>

typedef unsigned int u32;
typedef unsigned short u16;
typedef u32 v4u __attribute__((ext_vector_type(4)));
typedef u32 v2u __attribute__((ext_vector_type(2)));
typedef float f32x2 __attribute__((ext_vector_type(2)));
typedef float f32x4 __attribute__((ext_vector_type(4)));
typedef float f32x16 __attribute__((ext_vector_type(16)));
typedef short s16x4 __attribute__((ext_vector_type(4)));
typedef short s16x8 __attribute__((ext_vector_type(8)));
typedef __bf16 bf16x2 __attribute__((ext_vector_type(2)));
#define GAS __attribute__((address_space(1)))
#define LAS __attribute__((address_space(3)))
#define DEV __device__ __forceinline__
#define RLX_AGENT __ATOMIC_RELAXED, __HIP_MEMORY_SCOPE_AGENT
#define SC1 16
#define TS(i) do { SCHED(); ts[i] = (u32)__builtin_amdgcn_s_memrealtime(); SCHED(); } while (0)
#define TC(i) do { SCHED(); unsigned long long tt_; asm volatile("s_memtime %0\n\ts_waitcnt lgkmcnt(0)" : "=s"(tt_)::"memory"); tc[i] = (u32)tt_; SCHED(); } while (0)

constexpr int WS_E = 0;
constexpr int FL = 32;
constexpr int WS_STAT = 4096;
constexpr int WS_FXN = 65536;
constexpr int WS_FW2 = WS_FXN + 256 * 128;
constexpr int WS_FH = WS_FW2 + 1024 * 128;
constexpr int WS_FS = WS_FH + 1024 * 128;
constexpr int WS_XNT = 524288;
constexpr int WS_W2T = WS_XNT + 2097152;
constexpr int WS_HT = WS_W2T + 1835008;
constexpr int WS_ST = WS_HT + 7340032;
constexpr int WS_END = WS_ST + 2097152;
constexpr int LD_Q = 98304;
constexpr int LD_KREG = 131072;
constexpr int LD_PS = LD_KREG + 7424;
constexpr int LD_DUMMY = LD_KREG + 7680;
constexpr int LD_B1 = LD_KREG + 8192 + 7424;
constexpr int LD_HST = LD_KREG + 3 * 8192 + 7424;
constexpr int LD_STAT = 12288;
constexpr int LD_W2S = 90112;
constexpr int LD_U = 0;
constexpr int LD_CX = 16384;
constexpr int LD_HT = 0;
constexpr int LD_HX = 65536;
constexpr int LD_RED = 0;
constexpr int LD_ST = 114688;
constexpr int ABL_HT = WS_HT;
constexpr int LDS_BYTES = 163840;
constexpr u32 SPIN_CAP = 1u << 16;
constexpr u32 CODE_BYTES = $$$$$$$$$$$$$$$$$$$$$$$$$$$$$$$$$$$$$$$$$$$$$$$$$$$$$$$$$$$$$$$$$$$$$$$$$$$$$$;

DEV float bf2f(u32 h) { return __builtin_bit_cast(float, h << 16); }
DEV float bflo(u32 d) { return __builtin_bit_cast(float, d << 16); }
DEV float bfhi(u32 d) { return __builtin_bit_cast(float, d & 0xffff0000u); }
DEV u32 pk2(float lo, float hi) {
  f32x2 v = {lo, hi};
  return __builtin_bit_cast(u32, __builtin_convertvector(v, bf16x2));
}
DEV u32 pk2v(f32x2 v) { return __builtin_bit_cast(u32, __builtin_convertvector(v, bf16x2)); }
DEV float bfr(float v) { return bflo(pk2(v, v)); }
DEV f32x4 mfma16(v4u a, v4u b, f32x4 c) {
  return __builtin_amdgcn_mfma_f32_16x16x32_bf16(__builtin_bit_cast(s16x8, a), __builtin_bit_cast(s16x8, b), c, 0, 0, 0);
}
DEV f32x16 mfma32(v4u a, v4u b, f32x16 c) {
  return __builtin_amdgcn_mfma_f32_32x32x16_bf16(__builtin_bit_cast(s16x8, a), __builtin_bit_cast(s16x8, b), c, 0, 0, 0);
}
DEV v2u trread(LAS char* p) { return __builtin_bit_cast(v2u, __builtin_amdgcn_ds_read_tr16_b64_v4i16((LAS s16x4*)p)); }
DEV v4u mk_rsrc(const void* p, u32 bytes, u32 fmt) {
  const unsigned long long a = (unsigned long long)p;
  v4u r = {(u32)a, (u32)(a >> 32), bytes, fmt};
  return r;
}
DEV void dma16(u32 voff, v4u rs, u32 soff, u32 lds) {
  asm volatile("s_mov_b32 m0, %0\n\ts_nop 0\n\tbuffer_load_dwordx4 %1, %2, %3 offen lds" ::"s"(lds), "v"(voff), "s"(rs), "s"(soff) : "memory");
}
#define WAITV(n) asm volatile("s_waitcnt vmcnt(" #n ")" ::: "memory")
#define WAITL() asm volatile("s_waitcnt lgkmcnt(0)" ::: "memory")
#define SCHED() __builtin_amdgcn_sched_barrier(0)
#define BAR() do { asm volatile("s_waitcnt lgkmcnt(0)" ::: "memory"); __builtin_amdgcn_s_barrier(); asm volatile("" ::: "memory"); SCHED(); } while (0)
#define ORDER_BEHIND_POLL() __builtin_amdgcn_fence(__ATOMIC_ACQUIRE, "wavefront")

DEV f32x2 cmul(f32x2 a, f32x2 z) {
  const f32x2 zr = {-z.y, z.x};
  f32x2 r;
  asm("v_pk_mul_f32 %0, %1, %2 op_sel:[1,0] op_sel_hi:[1,1]\n\tv_pk_fma_f32 %0, %1, %3, %0 op_sel_hi:[0,1,1]" : "=&v"(r) : "v"(a), "v"(zr), "v"(z));
  return r;
}
DEV float sadd(float a, float b) { float r; asm("v_add_f32 %0, %1, %2" : "=v"(r) : "v"(a), "v"(b)); return r; }
DEV float smul(float a, float b) { float r; asm("v_mul_f32 %0, %1, %2" : "=v"(r) : "v"(a), "v"(b)); return r; }
DEV f32x2 zpow(float la, float th, float k) {
  const float mag = __builtin_amdgcn_exp2f(la * k);
  const float fr = __builtin_amdgcn_fractf(th * k);
  f32x2 r = {mag * __builtin_amdgcn_cosf(fr), mag * __builtin_amdgcn_sinf(fr)};
  return r;
}
template <int SLEEP>
DEV void poll_flags(GAS u32* p, u32 epoch, u32 first) {
  if (!__all(first == epoch)) {
#pragma unroll 1
    for (u32 spins = 0; spins < SPIN_CAP; ++spins) {
      const u32 v = __hip_atomic_load(p, RLX_AGENT);
      if (__all(v == epoch)) break;
      __builtin_amdgcn_s_sleep(SLEEP);
    }
  }
  ORDER_BEHIND_POLL();
}
DEV void publish(GAS u32* p, u32 epoch, int lane) {
  WAITV(0);
  if (lane == 0) __hip_atomic_store(p, epoch, RLX_AGENT);
}
DEV void set_progress(LAS char* L, int wv, u32 k) {
  WAITL();
  *(volatile LAS u32*)(L + LD_PS + wv * 4) = k;
}
DEV void wait_progress(LAS char* L, int word, u32 k) {
#pragma unroll 1
  for (u32 spins = 0; spins < (1u << 22); ++spins) {
    const u32 pv = *(volatile LAS u32*)(L + LD_PS + word * 4);
    if (__all(pv >= k)) break;
    __builtin_amdgcn_s_sleep(1);
  }
  asm volatile("" ::: "memory");
}
DEV void ln_stats(float s1, float s2, float& mean, float& var, float& den1) {
  mean = s1 * (1.0f / 512.0f);
  var = fmaxf(s2 * (1.0f / 512.0f) - mean * mean, 0.0f);
  den1 = bfr(__builtin_amdgcn_sqrtf(bfr(bfr(var) + 1e-5f)));
}
DEV float pick4(f32x4 g, int k) { return k == 0 ? g.x : k == 1 ? g.y : k == 2 ? g.z : g.w; }
#define LD16(p, i) ((u32)__hip_atomic_load((const GAS u16*)(p) + (i), __ATOMIC_RELAXED, __HIP_MEMORY_SCOPE_WAVEFRONT))

__global__ void __launch_bounds__(512, 2)
s4_mixer_kernel(const u16* __restrict__ x, u32 x_bytes, u32 x_fmt, const u16* __restrict__ w1, u32 w1_bytes, u32 w1_fmt,
                const u16* __restrict__ w2, char* __restrict__ ws,
                const u16* __restrict__ A_re, const u16* __restrict__ A_im, const u16* __restrict__ B_re,
                const u16* __restrict__ B_im, const u16* __restrict__ C_re, const u16* __restrict__ C_im,
                const u16* __restrict__ log_dt, const u16* __restrict__ Dskip, const u16* __restrict__ b1,
                const u16* __restrict__ b2, u16* __restrict__ out) {
  extern __shared__ __attribute__((aligned(16))) char smem[];
  LAS char* const L = (LAS char*)smem;
  unsigned long long pc;
  asm volatile("s_getpc_b64 %0" : "=s"(pc));
  {
    u32 coff = ((((threadIdx.x & 63) << 3) | (threadIdx.x >> 6)) * 32u + (blockIdx.x >> 3)) * 64u;
    coff = coff < CODE_BYTES ? coff : 0u;
    asm volatile("s_mov_b32 m0, %0\n\ts_nop 0\n\tglobal_load_lds_dword %1, %2" ::"s"((u32)LD_DUMMY), "v"(coff), "s"(pc) : "memory");
    SCHED();
  }
  const int tid = threadIdx.x, lane = tid & 63;
  const int wv = __builtin_amdgcn_readfirstlane(tid >> 6);
  const int w = wv & 3;
  const bool swave = wv >= 4;
  const int b = blockIdx.x, xcd = b & 7, idx = b >> 3;
  const int r16 = lane & 15, q = lane >> 4, qq = r16 >> 2, p4 = lane & 3;
  const bool helper = idx >= 28;
  u32 ts[16], tc[16];
#pragma unroll
  for (int i = 0; i < 16; ++i) { ts[i] = 0; tc[i] = 0; }
  TS(0);
  const u32 cyc0 = (u32)__builtin_amdgcn_s_memtime();
  const v4u rsX = mk_rsrc(x, x_bytes, x_fmt);
  const v4u rsW1 = mk_rsrc(w1, w1_bytes, w1_fmt);
  asm volatile("" ::"s"(rsX), "s"(rsW1), "s"(w2), "s"(ws), "s"(A_re), "s"(A_im), "s"(B_re), "s"(B_im), "s"(C_re), "s"(C_im), "s"(log_dt), "s"(Dskip), "s"(b1));
  const u32 eraw = *(const __attribute__((address_space(4))) u32*)(ws + WS_E);
  const int mq = (idx * 37) >> 8, mr = idx - 7 * mq;
  const int mt = (xcd >> 1) * 4 + mq, nt = (xcd & 1) * 7 + mr;
  const int hid = xcd * 4 + (idx - 28);
  const int tg = xcd * 4 + (idx >> 3), cg = idx & 7;
  const int cw = w >> 1, hw = w & 1;
  const int ch = 64 * xcd + 2 * idx + cw;
  const __amdgpu_buffer_rsrc_t rsWS = __builtin_amdgcn_make_buffer_rsrc((void*)ws, 0, WS_END, 0x00020000);
  GAS u32* const ctl = (GAS u32*)ws;
  const v4u ones = {0x3f803f80u, 0x3f803f80u, 0x3f803f80u, 0x3f803f80u};
  int rot2 = cg + 2 * (tg & 3);
  rot2 -= rot2 >= 7 ? 7 : 0;
#define KS2(s) (7 * wv + ((s) + rot2 >= 7 ? (s) + rot2 - 7 : (s) + rot2))
#define AW_LOAD(s, c) \
  aw[s][c] = __builtin_amdgcn_raw_buffer_load_b128(rsWS, lane * 16 + ((((c) + 2 * (wv & 1)) & 3) << 10), WS_W2T + (cg * 56 + KS2(s)) * 4096, SC1)
  const u32 wo = (u32)lane * 4u;
#define TW(rowp, row, val) *(LAS u32*)((rowp) + (wo ^ (u32)(((row) & 15) << 4))) = (val)
  LAS char* const PT = L + LD_KREG + w * 8192;

  auto gemm2_tail = [&](v4u (&aw)[7][4], const u32 epoch, GAS u32* const pend, u32 fh0) __attribute__((always_inline)) {
  {
    TS(9);
    int tz = threadIdx.x;
    asm volatile("" : "+v"(tz));
    const int lane = tz & 63, r16 = lane & 15, q = lane >> 4, qq = r16 >> 2, p4 = lane & 3;
    asm volatile("" : "+v"(fh0));
    poll_flags<2>(ctl + WS_FH / 4 + (tg * 32 + ((7 * wv) >> 1) + (lane & 3)) * FL, epoch, fh0);
    TS(10);
#define G2WIN 6
    constexpr int WIN = G2WIN;
    v4u hf[28];
#define H_LOAD(n) hf[n] = __builtin_amdgcn_raw_buffer_load_b128(rsWS, lane * 16 + (((((n) & 3) + 1 + (wv >> 1)) & 3) << 10), WS_HT + (tg * 56 + KS2((n) >> 2)) * 4096, SC1)
#pragma unroll
    for (int n = 0; n < WIN; ++n) { H_LOAD(n); SCHED(); }
    if (pend) {
      asm volatile("s_waitcnt vmcnt(%0)" ::"n"(WIN) : "memory");
      if (lane == 0) __hip_atomic_store(pend, epoch, RLX_AGENT);
      __builtin_amdgcn_s_setprio(0);
      SCHED();
    }
    GAS u32* const fsp = ctl + WS_FS / 4 + ((tg >> 4) * 512 + cg * 64 + wv * 8 + (lane >> 3)) * FL;
    const int tok = tg * 64 + 16 * (wv >> 1) + r16;
    const int c8 = cg * 64 + 32 * (wv & 1) + 8 * q;
    u32 fs, fe0 = epoch, fe1 = epoch;
    bool early;
    v4u xv, bv, sv;
    f32x4 acc[4][4];
#pragma unroll
    for (int i = 0; i < 4; ++i)
#pragma unroll
      for (int j = 0; j < 4; ++j) acc[i][j] = f32x4{0.f, 0.f, 0.f, 0.f};
#define RED_WRITE(j)                                                                                                  \
  _Pragma("unroll") for (int cp = 0; cp < 2; ++cp) if ((j) < 3 || cp) {                                               \
    const int d = ((((j) + 1 + (wv >> 1)) & 3) << 1) | ((cp + wv) & 1);                                               \
    _Pragma("unroll") for (int e = 0; e < 2; ++e)                                                                     \
      *(LAS f32x4*)(L + LD_RED + (d * 7 + ((j) < 3 ? 2 * (j) + cp : 6)) * 2048 + e * 1024 + lane * 16) = acc[2 * cp + e][j]; \
  }
#pragma unroll
    for (int n = 0; n < 28; ++n) {
#pragma unroll
      for (int c = 0; c < 4; ++c) acc[c][n & 3] = mfma16(aw[n >> 2][c], hf[n], acc[c][n & 3]);
      SCHED();
      if (n == 22) wait_progress(L, lane & 7, 4u);
      if (n >= 25) RED_WRITE(n - 25)
      if (n == 24 - 2 * WIN + 4) fs = __hip_atomic_load(fsp, RLX_AGENT);
      if (n + WIN < 28) H_LOAD(n + WIN);
      if (n == 27 - WIN) {
        xv = *(const GAS v4u*)(x + tok * 512 + c8);
        bv = *(const GAS v4u*)(b2 + c8);
      }
      if (n == 28 - WIN) {
        early = __all(fs == epoch);
        ORDER_BEHIND_POLL();
        sv = __builtin_amdgcn_raw_buffer_load_b128(rsWS, (lane >> 3) * 4096 + (lane & 7) * 16, early ? WS_ST + (cg * 64 + wv * 8) * 4096 + tg * 128 : WS_END, SC1);
        if (b == 0) {
          fe0 = __hip_atomic_load(ctl + WS_FS / 4 + tz * FL, RLX_AGENT);
          fe1 = __hip_atomic_load(ctl + WS_FS / 4 + (512 + tz) * FL, RLX_AGENT);
        }
      }
      SCHED();
    }
    TS(11);
    RED_WRITE(3)
    if (!early) {
      poll_flags<2>(fsp, epoch, epoch + 1u);
      sv = __builtin_amdgcn_raw_buffer_load_b128(rsWS, (lane >> 3) * 4096 + (lane & 7) * 16, WS_ST + (cg * 64 + wv * 8) * 4096 + tg * 128, SC1);
    }
    *(LAS v4u*)(L + LD_ST + wv * 1024 + lane * 16) = sv;
    TS(12);
    asm volatile("" : "+v"(fe0), "+v"(fe1));
    poll_flags<2>(ctl + WS_FS / 4 + tz * FL, epoch, fe0);
    poll_flags<2>(ctl + WS_FS / 4 + (512 + tz) * FL, epoch, fe1);
    BAR();
    if (b == 0 && tz == 0) __hip_atomic_store(ctl + WS_E / 4, epoch, RLX_AGENT);
    TS(13);
    f32x4 part[2][7];
    v2u sq[2];
#pragma unroll
    for (int e = 0; e < 2; ++e) {
#pragma unroll
      for (int j = 0; j < 7; ++j) part[e][j] = *(LAS f32x4*)(L + LD_RED + (wv * 7 + j) * 2048 + e * 1024 + lane * 16);
      sq[e] = trread(L + LD_ST + (32 * (wv & 1) + 8 * q + 4 * e + qq) * 128 + (16 * (wv >> 1) + 4 * p4) * 2);
    }
    SCHED();
    u32 o[4];
#pragma unroll
    for (int e = 0; e < 2; ++e) {
      const u32 ba = bv[2 * e], bb = bv[2 * e + 1], xa = xv[2 * e], xb = xv[2 * e + 1];
      f32x4 m = acc[e][3] + f32x4{bflo(ba), bfhi(ba), bflo(bb), bfhi(bb)};
#pragma unroll
      for (int j = 0; j < 7; ++j) m += part[e][j];
      f32x4 g;
#pragma unroll
      for (int k = 0; k < 4; ++k) g[k] = m[k] * __builtin_amdgcn_rcpf(1.0f + __builtin_amdgcn_exp2f(m[k] * -1.4426950408889634f));
      o[2 * e] = pk2(bflo(xa) + bflo(sq[e].x) * g.x, bfhi(xa) + bfhi(sq[e].x) * g.y);
      o[2 * e + 1] = pk2(bflo(xb) + bflo(sq[e].y) * g.z, bfhi(xb) + bfhi(sq[e].y) * g.w);
    }
    *(LAS v4u*)(L + LD_KREG + (16 * (wv >> 1) + r16) * 144 + (wv & 1) * 64 + q * 16) = v4u{o[0], o[1], o[2], o[3]};
    BAR();
    const v4u ov = *(LAS v4u*)(L + LD_KREG + (8 * wv + (lane >> 3)) * 144 + (lane & 7) * 16);
    __builtin_amdgcn_raw_buffer_store_b128(ov, __builtin_amdgcn_make_buffer_rsrc((void*)out, 0, 2097152, 0x00020000), ((tg * 64 + 8 * wv + (lane >> 3)) * 512 + cg * 64 + (lane & 7) * 8) * 2, 0, 16);
  }
  TS(14);
  WAITV(0);
  TS(15);
  const u32 cyc1 = (u32)__builtin_amdgcn_s_memtime();
  if (b == 41 && wv == 0) { const u32 X = 0u; const u32 te = ts[15] + 200u + 8u * X; while ((int)((u32)__builtin_amdgcn_s_memrealtime() - te) < 0) __builtin_amdgcn_s_sleep(2); }
  if (lane == 0) *(GAS u32*)(ws + (33 << 20) + (b * 8 + wv) * 4) = cyc1 - cyc0;
  if (lane == 0) {
#pragma unroll
    for (int i = 0; i < 16; ++i) *(GAS u32*)(ws + (32 << 20) + ((b * 8 + wv) * 16 + i) * 4) = ts[i];
#pragma unroll
    for (int i = 0; i < 16; ++i) *(GAS u32*)(ws + (34 << 20) + ((b * 8 + wv) * 16 + i) * 4) = tc[i];
  }
  __builtin_amdgcn_endpgm();
  };

  if (helper) {
    LAS char* const HX = L + LD_HX + wv * 8192;
#pragma unroll
    for (int p = 0; p < 8; ++p)
      dma16((u32)(wv * 8192 + ((lane ^ (((p & 3) << 2) | (p >> 2))) << 4)), rsX, (u32)((hid * 64 + p) * 1024), (u32)(LD_HX + (wv * 8 + p) * 1024));
    u32 epoch;
    asm volatile("s_waitcnt vmcnt(0) lgkmcnt(0)\n\ts_add_u32 %0, %1, 1" : "=s"(epoch) : "s"(eraw) : "memory", "scc");
    if (!swave) TS(3);
    f32x4 gram[2], sumx[2];
#pragma unroll
    for (int i = 0; i < 2; ++i) { gram[i] = f32x4{0.f, 0.f, 0.f, 0.f}; sumx[i] = gram[i]; }
    const int r8 = lane & 7, fr = ((r8 & 3) << 2) | (r8 >> 2);
    v4u xr[8];
#pragma unroll
    for (int s = 0; s < 8; ++s) xr[s] = *(LAS v4u*)(HX + r8 * 1024 + (lane & 8) * 64 + (((4 * s + q) ^ fr) << 4));
    v2u d[8][2];
#pragma unroll
    for (int s = 0; s < 8; ++s)
#pragma unroll
      for (int t = 0; t < 2; ++t)
        d[s][t] = trread(HX + (4 * t + qq) * 1024 + (((8 * s + 2 * q + (p4 >> 1)) ^ ((qq << 2) | t)) << 4) + 8 * (p4 & 1));
    SCHED();
#pragma unroll
    for (int s = 0; s < 8; ++s) {
      gram[s & 1] = mfma16(xr[s], xr[s], gram[s & 1]);
      sumx[s & 1] = mfma16(ones, xr[s], sumx[s & 1]);
    }
    float s1 = sumx[0].x + sumx[1].x, s2 = pick4(gram[0] + gram[1], r16 & 3);
    s1 += __shfl(s1, (lane + 40) & 63);
    s2 += __shfl(s2, (lane + 40) & 63);
    float mean, var, den1;
    ln_stats(s1, s2, mean, var, den1);
    const float rd = __builtin_amdgcn_rcpf(den1);
    const float den2 = bfr(__builtin_amdgcn_sqrtf(bfr(bfr(var * rd * rd) + 1e-5f)));
    const float R = __builtin_amdgcn_rcpf(den1 * den2);
    if (q == qq && r16 < 8) {
      *(LAS f32x2*)(L + LD_HST + (8 * wv + r16) * 8) = f32x2{rd, -bfr(mean) * rd};
      __builtin_amdgcn_raw_buffer_store_b128(v4u{__builtin_bit_cast(u32, R), epoch, __builtin_bit_cast(u32, R * mean), epoch}, rsWS,
                                             WS_STAT + (hid * 64 + 8 * wv + r16) * 16, 0, SC1);
    }
    WAITL();
    if (!swave) TS(4);
    f32x4 sv[4];
#pragma unroll
    for (int i = 0; i < 4; ++i) sv[i] = *(LAS f32x4*)(L + LD_HST + wv * 64 + i * 16);
#pragma unroll
    for (int cb = 0; cb < 8; ++cb) {
      v4u o;
      o.x = pk2(bflo(d[cb][0].x) * sv[0].x + sv[0].y, bfhi(d[cb][0].x) * sv[0].z + sv[0].w);
      o.y = pk2(bflo(d[cb][0].y) * sv[1].x + sv[1].y, bfhi(d[cb][0].y) * sv[1].z + sv[1].w);
      o.z = pk2(bflo(d[cb][1].x) * sv[2].x + sv[2].y, bfhi(d[cb][1].x) * sv[2].z + sv[2].w);
      o.w = pk2(bflo(d[cb][1].y) * sv[3].x + sv[3].y, bfhi(d[cb][1].y) * sv[3].z + sv[3].w);
      *(LAS v4u*)(L + LD_HT + (cb * 64 + lane) * 128 + ((wv ^ (lane & 7)) << 4)) = o;
    }
    if (!swave) TS(5);
    BAR();
    if (swave) {
      v4u o[16];
#pragma unroll
      for (int i = 0; i < 16; ++i) o[i] = *(LAS v4u*)(L + LD_HT + (w * 128 + i * 8 + (lane >> 3)) * 128 + (((lane ^ (lane >> 3)) & 7) << 4));
      SCHED();
#pragma unroll
      for (int i = 0; i < 16; ++i) __builtin_amdgcn_raw_buffer_store_b128(o[i], rsWS, lane * 16 + WS_XNT + hid * 65536 + (w * 16 + i) * 1024, 0, SC1);
      publish(ctl + WS_FXN / 4 + (hid * 4 + w) * FL, epoch, lane);
    }
    if (!swave) TS(6);
    SCHED();
  }

  if (swave) {
    __builtin_amdgcn_s_setprio(2);
    const u32 voX = (u32)((32 * w + (lane >> 3)) * 1024 + (((lane & 7) ^ (lane >> 3)) << 4));
    const u32 voW = (u32)((4 * w + q) * 3584 + ((r16 ^ ((q << 2) | w)) << 4));
    const int rot = (mr + 2 * mq) & 7;
#define G1_STAGE(st) G1_PART(st, 0, 4)
#define G1_PART(st, p0, p1)                                                                                  \
  {                                                                                                          \
    const int sa_ = ((st) + rot) & 7;                                                                        \
    _Pragma("unroll") for (int p = p0; p < p1; ++p) {                                                        \
      dma16(voX, rsX, (u32)(mt * 131072 + p * 8192 + sa_ * 128), (u32)(((st) & 3) * 32768 + w * 4096 + p * 1024)); \
      dma16(voW, rsW1, (u32)((sa_ * 64 + 16 * p) * 3584 + nt * 256), (u32)(((st) & 3) * 32768 + 16384 + p * 4096 + w * 1024)); \
    }                                                                                                        \
  }
    if (!helper) { G1_STAGE(0) G1_STAGE(1) }
    if (lane == 0) *(LAS u32*)(L + LD_PS + wv * 4) = 0u;
    TS(1);
    WAITV(8);
    u32 epoch;
    asm volatile("s_waitcnt lgkmcnt(0)\n\ts_add_u32 %0, %1, 1" : "=s"(epoch) : "s"(eraw) : "memory", "scc");
    TS(2);
    BAR();
    TS(3);
    TC(0);
    if (!helper) G1_STAGE(2)
    const int r32 = lane & 31, hh = lane >> 5;
    v4u pf[8], qf[8];
    {
#pragma unroll
      for (int s = 0; s < 8; ++s) {
        qf[s] = *(LAS v4u*)(L + LD_Q + w * 8192 + r32 * 256 + (((2 * s + hh) ^ (r32 & 15)) << 4));
        pf[s] = *(LAS v4u*)(PT + (2 * s + hh) * 464 + r32 * 16);
      }
    }
    SCHED();
    TC(1);
    if (!helper) {
      WAITV(8); BAR(); TC(2); G1_STAGE(3) G1_PART(4, 0, 2) TC(3);
      WAITV(12); BAR(); TC(4); G1_PART(4, 2, 4) G1_STAGE(5) TC(5);
      WAITV(16); BAR(); TC(6);
      G1_STAGE(6) TC(7);
      WAITV(16); BAR(); TC(8); G1_STAGE(7) TC(9);
      WAITV(16); BAR();
    }
    TC(10);
    GAS u32* const fxnp = ctl + WS_FXN / 4 + ((hw * 16 + r16) * 4 + (ch >> 7)) * FL;
    GAS u32* const fw2p = ctl + WS_FW2 / 4 + ((cg * 56 + 7 * (w + 4 * ((lane >> 4) & 1))) * 2 + min(r16, 13)) * FL;
    const int sto = WS_STAT + ((helper ? 0 : mt) * 128 + 32 * w + r32) * 16;
    v4u sg = __builtin_amdgcn_raw_buffer_load_b128(rsWS, sto, 0, SC1);
    const u32 fw2 = __hip_atomic_load(fw2p, RLX_AGENT);
    const u32 pDs = LD16(Dskip, ch);
    SCHED();
    f32x16 kacc;
#pragma unroll
    for (int i = 0; i < 16; ++i) kacc[i] = 0.f;
#pragma unroll
    for (int s = 0; s < 8; ++s) kacc = mfma32(pf[s], qf[s], kacc);
    SCHED();
    TC(11);
    if (!helper) { WAITV(3); BAR(); }
    TC(12);
    TS(4);
#pragma unroll
    for (int i = 0; i < 4; ++i)
      *(LAS f32x4*)(L + LD_CX + w * 4096 + i * 1024 + lane * 16) = f32x4{kacc[4 * i], kacc[4 * i + 1], kacc[4 * i + 2], kacc[4 * i + 3]};
    if (!helper) {
      if (!__all(sg.y == epoch && sg.w == epoch)) {
#pragma unroll 1
        for (u32 spins = 0; spins < SPIN_CAP; ++spins) {
          __builtin_amdgcn_s_sleep(4);
          asm volatile("" ::: "memory");
          sg = __builtin_amdgcn_raw_buffer_load_b128(rsWS, sto, 0, SC1);
          if (__all(sg.y == epoch && sg.w == epoch)) break;
        }
      }
      *(LAS v2u*)(L + LD_STAT + (32 * w + r32) * 8) = v2u{sg.x, sg.z};
    }
    poll_flags<4>(fw2p, epoch, fw2);
    set_progress(L, wv, 1u);
    TS(5);
    v4u aw[7][4];
#pragma unroll
    for (int c = 0; c < 4; ++c) AW_LOAD(c >> 2, c & 3);
    SCHED();
    wait_progress(L, wv ^ 1, 1u);
    {
      f32x4 v[4];
#pragma unroll
      for (int i = 0; i < 4; ++i) v[i] = *(LAS f32x4*)(L + LD_CX + (w ^ 1) * 4096 + i * 1024 + lane * 16);
#pragma unroll
      for (int i = 0; i < 15; ++i) {
        kacc[i] = sadd(kacc[i], v[i >> 2][i & 3]);
      }
    }
    {
      const bool odd = r32 & 1, up = hh != 0;
      const int ia[4][9] = {{0, 1, 2, 3, 4, 5, 6, 7, 8}, {9, 9, 9, 9, -1, 10, 10, 10, 10}, {11, 11, 11, 11, -1, 12, 12, 12, 12}, {13, 13, 13, 13, -1, 14, 14, 14, 14}};
      const int ib[4][9] = {{0, 0, 1, 2, 2, 2, 3, 4, 4}, {5, 5, 5, 6, 6, 6, 7, 7, 7}, {8, 8, 8, 9, 9, 9, 10, 10, 10}, {11, 11, 11, 11, -1, 12, 12, 12, 12}};
      LAS char* const KC = PT + hw * 64 + (255 - r32) * 16;
#pragma unroll
      for (int u = 0; u < 4; ++u) {
        SCHED();
        AW_LOAD(1 + (u >> 1), 2 * (u & 1));
        SCHED();
        float e[9];
#pragma unroll
        for (int i = 0; i < 9; ++i) {
          const float va = ia[u][i] >= 0 ? kacc[ia[u][i]] : (odd ? kacc[ia[u][5]] : kacc[ia[u][3]]);
          const float vb = ib[u][i] >= 0 ? kacc[ib[u][i]] : (odd ? kacc[ib[u][5]] : kacc[ib[u][3]]);
          e[i] = up ? vb : va;
        }
        SCHED();
        AW_LOAD(1 + (u >> 1), 2 * (u & 1) + 1);
        SCHED();
        v4u d;
        if (hw) { d.x = pk2(e[8], e[7]); d.y = pk2(e[6], e[5]); d.z = pk2(e[4], e[3]); d.w = pk2(e[2], e[1]); }
        else    { d.x = pk2(e[7], e[6]); d.y = pk2(e[5], e[4]); d.z = pk2(e[3], e[2]); d.w = pk2(e[1], e[0]); }
        const int sa = u == 0 ? 0 : u + 3, sb = u == 3 ? 7 : u + 1;
        *(LAS v4u*)(KC - (up ? sb : sa) * 512) = d;
      }
      if (lane < 2) *(LAS v4u*)(PT + hw * 64 + 4096 + lane * 16) = v4u{(hw && lane == 0) ? pk2(kacc[0], 0.f) : 0u, 0u, 0u, 0u};
    }
    SCHED();
    TC(13);
    u32 fxa = __hip_atomic_load(fxnp, RLX_AGENT);
#pragma unroll
    for (int c = 12; c < 22; ++c) AW_LOAD(c >> 2, c & 3);
    SCHED();
    asm volatile("" : "+v"(fxa));
    const bool xearly = __all(fxa == epoch);
    ORDER_BEHIND_POLL();
    const int xo = WS_XNT + (16 * hw) * 65536 + ch * 128;
    v4u u0 = __builtin_amdgcn_raw_buffer_load_b128(rsWS, (lane >> 3) * 65536 + (lane & 7) * 16, xearly ? xo : WS_END, SC1);
    v4u u1 = __builtin_amdgcn_raw_buffer_load_b128(rsWS, (lane >> 3) * 65536 + (lane & 7) * 16, xearly ? xo + 8 * 65536 : WS_END, SC1);
#pragma unroll
    for (int c = 22; c < 28; ++c) AW_LOAD(c >> 2, c & 3);
    SCHED();
    if (!xearly) {
      poll_flags<8>(fxnp, epoch, epoch + 1u);
      u0 = __builtin_amdgcn_raw_buffer_load_b128(rsWS, (lane >> 3) * 65536 + (lane & 7) * 16, xo, SC1);
      u1 = __builtin_amdgcn_raw_buffer_load_b128(rsWS, (lane >> 3) * 65536 + (lane & 7) * 16, xo + 8 * 65536, SC1);
      __builtin_amdgcn_s_waitcnt(0x0F70);
    }
    LAS char* const UB = L + LD_U + cw * 4608;
    if (hw == 0 && lane < 32) *(LAS v4u*)(UB + lane * 16) = v4u{0u, 0u, 0u, 0u};
    *(LAS v4u*)(UB + 512 + (128 * hw + lane) * 16) = u0;
    *(LAS v4u*)(UB + 512 + (128 * hw + 64 + lane) * 16) = u1;
    set_progress(L, wv, 2u);
    {
      const u32 po = (u32)(WS_W2T + (cg * 56 + 7 * w) * 4096 + ((tg & 3) * 56 + min(lane, 55)) * 128);
      asm volatile("s_mov_b32 m0, %0\n\ts_nop 0\n\tglobal_load_lds_dword %1, %2 sc1" ::"s"((u32)LD_DUMMY), "v"(po), "s"(ws) : "memory");
    }
    SCHED();
    TS(6);
    const float dskip = bf2f(pDs);
    const int sgm = 1 - (r16 & 1);
    LAS char* const ka = L + LD_KREG + (2 * cw + sgm) * 8192 + sgm * 64 + 2 * (2031 - r16 + 8 * q + sgm) - hw * 256 - 7 * 512 - 3 * 64;
    LAS char* const ub = UB + 2 * (256 - 16 + 16 * r16 + 8 * q) - hw * 256 - 3 * 64;
    u32 kb[4];
#pragma unroll
    for (int i = 0; i < 4; ++i) { kb[i] = (u32)(size_t)ka + i * 1024; asm volatile("" : "+v"(kb[i])); }
    f32x4 y[8];
    {
      f32x4 y0[4];
#pragma unroll
      for (int i = 0; i < 4; ++i) {
        const v2u xv = *(LAS v2u*)(UB + 512 + (256 * (4 * hw + i) + 16 * r16 + 4 * q) * 2);
        y0[i] = f32x4{smul(bflo(xv.x), dskip), smul(bfhi(xv.x), dskip), smul(bflo(xv.y), dskip), smul(bfhi(xv.y), dskip)};
      }
      const f32x4 zz = {0.f, 0.f, 0.f, 0.f};
      if (hw) {
#pragma unroll
        for (int i = 0; i < 4; ++i) { y[i] = zz; y[4 + i] = y0[i]; }
      } else {
#pragma unroll
        for (int i = 0; i < 4; ++i) { y[i] = y0[i]; y[4 + i] = zz; }
      }
#pragma unroll
      for (int i = 0; i < 8; ++i) asm volatile("" : "+v"(y[i]));
    }
    wait_progress(L, wv ^ 1, 2u);
    v4u af[4][8], bfv[4][8];
#define CV_READ(rr, g)                                                                                   \
  {                                                                                                      \
    LAS u32* pa = (LAS u32*)(size_t)(kb[(7 - (g)) >> 1] + ((7 - (g)) & 1) * 512 + (3 - (rr)) * 64);      \
    af[rr][g] = v4u{pa[0], pa[1], pa[2], pa[3]};                                                         \
    bfv[rr][g] = *(LAS v4u*)(ub + (g) * 512 + (3 - (rr)) * 64);                                          \
  }
#define CV_LEVEL(rr, k, T0, T1)                                                              \
  _Pragma("unroll") for (int o = 0; o <= (k); ++o)                                           \
    if ((k) + o >= (T0) && (k) + o < (T1)) {                                                 \
      y[(k) + o] = mfma16(af[rr][k], bfv[rr][o], y[(k) + o]);                                \
      if (o < (k)) y[(k) + o] = mfma16(af[rr][o], bfv[rr][k], y[(k) + o]);                   \
    }
#define CV_MIX(n)                                                                            \
  _Pragma("unroll") for (int t = 0; t < (n); ++t) {                                          \
    __builtin_amdgcn_sched_group_barrier(0x008, 1, 0);                                       \
    __builtin_amdgcn_sched_group_barrier(0x100, 1, 0);                                       \
  }                                                                                          \
  SCHED();
#pragma unroll
    for (int g = 0; g < 4; ++g) CV_READ(0, g)
    SCHED();
    wait_progress(L, w, 4u);
    __builtin_amdgcn_s_setprio(2);
    TS(7);
    TC(14);
#pragma unroll
    for (int rr = 0; rr < 3; ++rr) {
      CV_READ(rr, 4) CV_READ(rr, 5) CV_READ(rr, 6) CV_READ(rr, 7)
      CV_LEVEL(rr, 0, 0, 8) CV_LEVEL(rr, 1, 0, 8) CV_LEVEL(rr, 2, 0, 8) CV_LEVEL(rr, 3, 0, 8)
      CV_MIX(12)
      CV_READ(rr + 1, 0) CV_READ(rr + 1, 1) CV_READ(rr + 1, 2) CV_READ(rr + 1, 3)
      CV_LEVEL(rr, 4, 0, 8) CV_LEVEL(rr, 5, 0, 8) CV_LEVEL(rr, 6, 0, 8) CV_LEVEL(rr, 7, 0, 8)
      CV_MIX(12)
    }
    f32x4 mine[4];
    CV_READ(3, 4) CV_READ(3, 5) CV_READ(3, 6) CV_READ(3, 7)
    SCHED();
    if (hw) {
      CV_LEVEL(3, 0, 0, 4) CV_LEVEL(3, 1, 0, 4) CV_LEVEL(3, 2, 0, 4) CV_LEVEL(3, 3, 0, 4)
      CV_LEVEL(3, 2, 4, 8) CV_LEVEL(3, 3, 4, 8)
      SCHED();
#pragma unroll
      for (int i = 0; i < 4; ++i) *(LAS f32x4*)(L + LD_CX + w * 4096 + i * 1024 + lane * 16) = y[i];
      *(volatile LAS u32*)(L + LD_PS + wv * 4) = 3u;
      SCHED();
      CV_LEVEL(3, 4, 4, 8) CV_LEVEL(3, 5, 4, 8) CV_LEVEL(3, 6, 4, 8) CV_LEVEL(3, 7, 4, 8)
#pragma unroll
      for (int i = 0; i < 4; ++i) mine[i] = y[4 + i];
    } else {
      CV_LEVEL(3, 2, 4, 8) CV_LEVEL(3, 3, 4, 8) CV_LEVEL(3, 4, 4, 8) CV_LEVEL(3, 5, 4, 8) CV_LEVEL(3, 6, 4, 8) CV_LEVEL(3, 7, 4, 8)
      CV_LEVEL(3, 0, 0, 4) CV_LEVEL(3, 1, 0, 4)
      SCHED();
#pragma unroll
      for (int i = 0; i < 4; ++i) *(LAS f32x4*)(L + LD_CX + w * 4096 + i * 1024 + lane * 16) = y[4 + i];
      *(volatile LAS u32*)(L + LD_PS + wv * 4) = 3u;
      SCHED();
      CV_LEVEL(3, 2, 0, 4) CV_LEVEL(3, 3, 0, 4)
#pragma unroll
      for (int i = 0; i < 4; ++i) mine[i] = y[i];
    }
    SCHED();
    TC(15);
    const u32 fh0 = __hip_atomic_load(ctl + WS_FH / 4 + (tg * 32 + ((7 * wv) >> 1) + (lane & 3)) * FL, RLX_AGENT);
    SCHED();
    wait_progress(L, wv ^ 1, 3u);
    f32x4 oy[4];
#pragma unroll
    for (int i = 0; i < 4; ++i) oy[i] = *(LAS f32x4*)(L + LD_CX + (w ^ 1) * 4096 + i * 1024 + lane * 16);
    set_progress(L, wv, 4u);
#pragma unroll
    for (int i = 0; i < 4; ++i) {
      v2u o;
      o.x = pk2(sadd(mine[i].x, oy[i].x), sadd(mine[i].y, oy[i].y));
      o.y = pk2(sadd(mine[i].z, oy[i].z), sadd(mine[i].w, oy[i].w));
      __builtin_amdgcn_raw_buffer_store_b64(o, rsWS, (16 * r16 + 4 * q) * 2 + WS_ST + ch * 4096 + (4 * hw + i) * 512, 0, SC1);
    }
    TS(8);
    gemm2_tail(aw, epoch, ctl + WS_FS / 4 + (hw * 512 + ch) * FL, fh0);
  } else {
    if (!helper) __builtin_amdgcn_s_setprio(3);
    const int po = ch * 128 + hw * 64 + lane;
    const u32 pLd = LD16(log_dt, ch), pAr = LD16(A_re, po), pAi = LD16(A_im, po);
    const u32 pBr = LD16(B_re, po), pBi = LD16(B_im, po), pCr = LD16(C_re, po), pCi = LD16(C_im, po);
    const u32 pB1 = LD16(b1, (helper ? 0 : nt * 128) + (tid & 127));
    const int uks = 2 * (helper ? 0 : idx) + (w >> 1), upp = w & 1;
    v4u w2a, w2b;
    {
      const GAS v4u* src = (const GAS v4u*)(w2 + (uks * 32 + (lane >> 2)) * 512 + xcd * 64 + upp * 32 + (lane & 3) * 8);
      asm volatile("" : "+v"(src));
      w2a = src[0];
      w2b = src[16 * 64];
    }
    if (lane == 0) *(LAS u32*)(L + LD_PS + wv * 4) = 0u;
    SCHED();
    TS(1);
    const int mi = w >> 1, ni = w & 1;
    f32x4 acc[4][4], csum[4];
#pragma unroll
    for (int i = 0; i < 4; ++i) {
#pragma unroll
      for (int j = 0; j < 4; ++j) { acc[i][j] = f32x4{0.f, 0.f, 0.f, 0.f}; asm volatile("" : "+v"(acc[i][j])); }
      csum[i] = f32x4{0.f, 0.f, 0.f, 0.f};
      asm volatile("" : "+v"(csum[i]));
    }
    u32 xb[2][2], wb[2][4][2];
#pragma unroll
    for (int h = 0; h < 2; ++h) {
#pragma unroll
      for (int k = 0; k < 2; ++k) {
        xb[h][k] = (u32)(((64 * mi + r16) * 128 + ((q ^ (r16 & 7)) << 4)) ^ (k * 64)) + h * 65536;
        asm volatile("" : "+v"(xb[h][k]));
      }
#pragma unroll
      for (int j = 0; j < 4; ++j)
#pragma unroll
        for (int t = 0; t < 2; ++t) {
          wb[h][j][t] = (u32)(16384 + (8 * q + 4 * t + qq) * 256 + (((2 * (4 * ni + j) + (p4 >> 1)) ^ ((qq << 2) | ((2 * q + t) & 3))) << 4) + 8 * (p4 & 1)) + h * 65536;
          asm volatile("" : "+v"(wb[h][j][t]));
        }
    }
    v4u onev = ones;
    asm volatile("" : "+v"(onev));
    u32 qad[16];
#pragma unroll
    for (int r = 0; r < 16; ++r) { qad[r] = (u32)(LD_Q + w * 8192) + (wo ^ (u32)(r << 4)); asm volatile("" : "+v"(qad[r])); }
    SCHED();
    TC(1);
    u32 epoch;
    asm volatile("s_waitcnt lgkmcnt(0)\n\ts_add_u32 %0, %1, 1" : "=s"(epoch) : "s"(eraw) : "memory", "scc");
    SCHED();
    {
      const float dt = bfr(__builtin_amdgcn_exp2f(bf2f(pLd) * 1.4426950408889634f));
      const float la = dt * bf2f(pAr) * 1.4426950408889634f;
      const float th = dt * bf2f(pAi) * 0.15915494309189535f;
      const f32x2 dBc = {dt * bf2f(pBr), -dt * bf2f(pBi)};
      const f32x2 Ccc = {bf2f(pCr), -bf2f(pCi)};
      const f32x2 z8 = zpow(la, th, 8.f), z16 = zpow(la, th, 16.f);
      const f32x2 y1 = zpow(la, -th, 1.f), y256 = zpow(la, -th, 256.f), y8 = {z8.x, -z8.y};
      const f32x2 y2 = cmul(y1, y1), y4 = cmul(y2, y2);
#define PROW(h, g) (((g) & 3) + 8 * ((g) >> 2) + 4 * (h))
#define PW(h, g, v) *(LAS u32*)(PT + (lane >> 2) * 464 + (lane & 3) * 4 + PROW(h, g) * 16) = pk2v(v)
      f32x2 W[8];
      W[0] = cmul(Ccc, dBc);
#pragma unroll
      for (int c = 1; c < 8; ++c) W[c] = cmul(W[c - 1], y256);
      {
        f32x2 te = W[0], to = cmul(W[0], y1), t1 = W[1];
        PW(0, 0, te); PW(0, 1, to); PW(1, 0, t1);
#pragma unroll
        for (int a = 1; a <= 4; ++a) {
          te = cmul(te, y2); PW(0, 2 * a, te);
          if (a < 4) { to = cmul(to, y2); PW(0, 2 * a + 1, to); }
          t1 = cmul(t1, y2); PW(1, a, t1);
        }
      }
      PW(1, 5, W[2]); PW(1, 6, cmul(W[2], y4)); PW(1, 7, cmul(W[2], y8));
      PW(1, 8, W[3]); PW(1, 9, cmul(W[3], y4)); PW(1, 10, cmul(W[3], y8));
      PW(0, 9, W[4]); PW(0, 10, cmul(W[4], y8));
      PW(0, 11, W[5]); PW(0, 12, cmul(W[5], y8));
      PW(0, 13, W[6]); PW(0, 14, cmul(W[6], y8));
      PW(1, 11, W[7]); PW(1, 12, cmul(W[7], y8));
      f32x2 qa = {1.f, 0.f}, qb = z8;
      u32 qv[32];
#pragma unroll
      for (int i = 0; i < 16; ++i) {
        qv[2 * i] = pk2v(qa);
        qv[2 * i + 1] = pk2v(qb);
        if (i < 15) { qa = (i == 0) ? z16 : cmul(qa, z16); qb = cmul(qb, z16); }
      }
#pragma unroll
      for (int r = 0; r < 16; ++r) {
        *(LAS u32*)(size_t)(qad[r] + r * 256) = qv[r];
        *(LAS u32*)(size_t)(qad[r] + (r + 16) * 256) = qv[r + 16];
      }
    }
    SCHED();
    TC(2);
    {
      LAS char* const WS2 = L + LD_W2S + w * 2048;
      *(LAS v4u*)(WS2 + lane * 16) = w2a;
      *(LAS v4u*)(WS2 + 1024 + lane * 16) = w2b;
#pragma unroll
      for (int e = 0; e < 2; ++e) {
        const v2u lo = trread(WS2 + (4 * q + qq) * 64 + 16 * p4 + 8 * e), hi = trread(WS2 + (16 + 4 * q + qq) * 64 + 16 * p4 + 8 * e);
        __builtin_amdgcn_raw_buffer_store_b128(v4u{lo.x, lo.y, hi.x, hi.y}, rsWS,
                                               lane * 16 + (helper ? WS_END : WS_W2T) + ((xcd * 56 + uks) * 4 + 2 * upp + e) * 1024, 0, SC1);
      }
    }
    *(LAS float*)(L + LD_B1 + (tid & 127) * 4) = bf2f(pB1);
    TC(3);
    TS(2);
    v4u aw[7][4];
    u32 fh0 = epoch + 1u;
    if (!helper) {
      v4u xf[2][4], wf[2][4];
#define G1_READ(n)                                                                                            \
  _Pragma("unroll") for (int i = 0; i < 4; ++i) {                                                             \
    const v2u lo = trread(L + (((n) >> 1) & 1) * 32768 + ((n) & 1) * 8192 + wb[((n) >> 2) & 1][i][0]);        \
    const v2u hi = trread(L + (((n) >> 1) & 1) * 32768 + ((n) & 1) * 8192 + wb[((n) >> 2) & 1][i][1]);        \
    wf[(n) & 1][i] = v4u{lo.x, lo.y, hi.x, hi.y};                                                             \
    xf[(n) & 1][i] = *(LAS v4u*)(L + (((n) >> 1) & 1) * 32768 + i * 2048 + xb[((n) >> 2) & 1][(n) & 1]);      \
  }
      __builtin_amdgcn_s_setprio(0);
      TC(4);
      BAR();
      TC(5);
      TS(3);
      G1_READ(0)
      SCHED();
#pragma unroll 1
      for (int it = 0; it < 2; ++it) {
#pragma unroll
        for (int n = 0; n < 8; ++n) {
          if (n & 1) {
            if (n < 4 || it == 0) BAR();
            if (n == 5 && it == 0) {
              __builtin_amdgcn_s_setprio(3);
              publish(ctl + WS_FW2 / 4 + ((xcd * 56 + uks) * 2 + upp) * FL, epoch, lane);
              __builtin_amdgcn_s_setprio(0);
            }
            SCHED();
          }
          G1_READ(n + 1)
#pragma unroll
          for (int j = 0; j < 4; ++j) {
#pragma unroll
            for (int i = 0; i < 4; ++i) acc[j][i] = mfma16(wf[n & 1][j], xf[n & 1][i], acc[j][i]);
            csum[j] = mfma16(wf[n & 1][j], onev, csum[j]);
          }
#pragma unroll
          for (int k = 0; k < 12; ++k) {
            __builtin_amdgcn_sched_group_barrier(0x008, 1, 0);
            __builtin_amdgcn_sched_group_barrier(0x100, 1, 0);
          }
          __builtin_amdgcn_sched_group_barrier(0x008, 8, 0);
          SCHED();
        }
      }
      TS(4);
      __builtin_amdgcn_s_setprio(3);
      wait_progress(L, 4 + (lane & 3), 1u);
      TS(5);
      float rs[4], rm[4];
      f32x4 bb[4];
#pragma unroll
      for (int i = 0; i < 4; ++i) {
        const f32x2 st = *(LAS f32x2*)(L + LD_STAT + (64 * mi + 16 * i + r16) * 8);
        rs[i] = st.x; rm[i] = -st.y;
        asm volatile("" : "+v"(rm[i]));
        bb[i] = *(LAS f32x4*)(L + LD_B1 + (64 * ni + 16 * i + 4 * q) * 4);
      }
      SCHED();
#pragma unroll
      for (int jp = 0; jp < 2; ++jp) {
#pragma unroll
        for (int i = 0; i < 4; ++i) {
          TC(8 + jp * 4 + i);
          f32x2 pre[4], ee[4];
#pragma unroll
          for (int k = 0; k < 4; ++k) {
            const f32x4 a4 = acc[2 * jp + (k >> 1)][i], c4 = csum[2 * jp + (k >> 1)], b4 = bb[2 * jp + (k >> 1)];
            const f32x2 a2 = (k & 1) ? f32x2{a4.z, a4.w} : f32x2{a4.x, a4.y};
            const f32x2 c2 = (k & 1) ? f32x2{c4.z, c4.w} : f32x2{c4.x, c4.y};
            const f32x2 b2v = (k & 1) ? f32x2{b4.z, b4.w} : f32x2{b4.x, b4.y};
            pre[k] = a2 * rs[i] + (c2 * rm[i] + b2v);
            f32x2 arg = pre[k] * (pre[k] * pre[k] * -0.1029432397f + -2.302208198f);
            asm("" : "+v"(arg));
            ee[k] = f32x2{__builtin_amdgcn_exp2f(arg.x), __builtin_amdgcn_exp2f(arg.y)};
          }
          u32 o[4];
#pragma unroll
          for (int k = 0; k < 4; ++k) {
            f32x2 dn = ee[k] + 1.0f;
            asm("" : "+v"(dn));
            o[k] = pk2v(pre[k] * f32x2{__builtin_amdgcn_rcpf(dn.x), __builtin_amdgcn_rcpf(dn.y)});
          }
          __builtin_amdgcn_raw_buffer_store_b128(v4u{o[0], o[1], o[2], o[3]}, rsWS,
                                                 lane * 16 + ABL_HT + (((2 * mt + mi) * 56 + nt * 4 + 2 * ni + jp) * 4 + i) * 1024, 0, SC1);
          SCHED();
        }
      }
      set_progress(L, wv, 4u);
      __builtin_amdgcn_s_setprio(0);
      TS(6);
#pragma unroll
      for (int c = 0; c < 8; ++c) AW_LOAD(c >> 2, c & 3);
      SCHED();
      __builtin_amdgcn_s_setprio(3);
      WAITV(8);
      if (lane == 0) __hip_atomic_store(ctl + WS_FH / 4 + ((2 * mt + mi) * 32 + 2 * nt + ni) * FL, epoch, RLX_AGENT);
      __builtin_amdgcn_s_setprio(0);
      SCHED();
      TS(7);
#pragma unroll
      for (int c = 8; c < 20; ++c) AW_LOAD(c >> 2, c & 3);
      fh0 = __hip_atomic_load(ctl + WS_FH / 4 + (tg * 32 + ((7 * wv) >> 1) + (lane & 3)) * FL, RLX_AGENT);
#pragma unroll
      for (int c = 20; c < 28; ++c) AW_LOAD(c >> 2, c & 3);
      SCHED();
    } else {
      __builtin_amdgcn_s_setprio(0);
      BAR();
      set_progress(L, wv, 4u);
      wait_progress(L, 4 + (lane & 3), 2u);
#pragma unroll
      for (int s = 0; s < 7; ++s)
#pragma unroll
        for (int c = 0; c < 4; ++c) AW_LOAD(s, c);
      SCHED();
    }
    TS(8);
    gemm2_tail(aw, epoch, nullptr, fh0);
  }
}

extern "C" void kernel_launch(void* const* d_in, const int* in_sizes, int n_in, void* d_out, int out_size, void* d_ws,
                              size_t ws_size, hipStream_t stream) {
  (void)in_sizes; (void)n_in; (void)out_size; (void)ws_size;
  (void)hipFuncSetAttribute((const void*)s4_mixer_kernel, hipFuncAttributeMaxDynamicSharedMemorySize, LDS_BYTES);
  s4_mixer_kernel<<<256, 512, LDS_BYTES, stream>>>(
      (const u16*)d_in[0], 2097152u, 0x00020000u, (const u16*)d_in[10], 1835008u, 0x00020000u, (const u16*)d_in[12], (char*)d_ws, (const u16*)d_in[1], (const u16*)d_in[2],
      (const u16*)d_in[3], (const u16*)d_in[4], (const u16*)d_in[5], (const u16*)d_in[6], (const u16*)d_in[7],
      (const u16*)d_in[8], (const u16*)d_in[11], (const u16*)d_in[13], (u16*)d_out);
}
```

```cpp
#include <hip/hip_runtime.h>
#include <hip/hip_bf16.h>

typedef unsigned int u32;
typedef unsigned short u16;
typedef u32 v4u __attribute__((ext_vector_type(4)));
typedef u32 v2u __attribute__((ext_vector_type(2)));
typedef float f32x2 __attribute__((ext_vector_type(2)));
typedef float f32x4 __attribute__((ext_vector_type(4)));
typedef float f32x16 __attribute__((ext_vector_type(16)));
typedef short s16x4 __attribute__((ext_vector_type(4)));
typedef short s16x8 __attribute__((ext_vector_type(8)));
typedef __bf16 bf16x2 __attribute__((ext_vector_type(2)));
#define GAS __attribute__((address_space(1)))
#define LAS __attribute__((address_space(3)))
#define DEV __device__ __forceinline__
#define RLX_AGENT __ATOMIC_RELAXED, __HIP_MEMORY_SCOPE_AGENT
#define SC1 16
#define TS(i) do { } while (0)
#define TC(i) do { } while (0)

constexpr int WS_E = 0;
constexpr int FL = 32;
constexpr int WS_STAT = 4096;
constexpr int WS_FXN = 65536;
constexpr int WS_FW2 = WS_FXN + 256 * 128;
constexpr int WS_FH = WS_FW2 + 1024 * 128;
constexpr int WS_FS = WS_FH + 1024 * 128;
constexpr int WS_XNT = 524288;
constexpr int WS_W2T = WS_XNT + 2097152;
constexpr int WS_HT = WS_W2T + 1835008;
constexpr int WS_ST = WS_HT + 7340032;
constexpr int WS_END = WS_ST + 2097152;
constexpr int LD_Q = 98304;
constexpr int LD_KREG = 131072;
constexpr int LD_PS = LD_KREG + 7424;
constexpr int LD_DUMMY = LD_KREG + 7680;
constexpr int LD_B1 = LD_KREG + 8192 + 7424;
constexpr int LD_HST = LD_KREG + 3 * 8192 + 7424;
constexpr int LD_STAT = 12288;
constexpr int LD_W2S = 90112;
constexpr int LD_U = 0;
constexpr int LD_CX = 16384;
constexpr int LD_HT = 0;
constexpr int LD_HX = 65536;
constexpr int LD_RED = 0;
constexpr int LD_ST = 114688;
constexpr int ABL_HT = WS_HT;
constexpr int LDS_BYTES = 163840;
constexpr u32 SPIN_CAP = 1u << 16;
constexpr u32 CODE_BYTES = $$$$$$$$$$$$$$$$$$$$$$$$$$$$$$$$$$$$$$$$$$$$$$$$$$$$$$$$$$$$$$$$$$$$$$$$$$$$;

DEV float bf2f(u32 h) { return __builtin_bit_cast(float, h << 16); }
DEV float bflo(u32 d) { return __builtin_bit_cast(float, d << 16); }
DEV float bfhi(u32 d) { return __builtin_bit_cast(float, d & 0xffff0000u); }
DEV u32 pk2(float lo, float hi) {
  f32x2 v = {lo, hi};
  return __builtin_bit_cast(u32, __builtin_convertvector(v, bf16x2));
}
DEV u32 pk2v(f32x2 v) { return __builtin_bit_cast(u32, __builtin_convertvector(v, bf16x2)); }
DEV float bfr(float v) { return bflo(pk2(v, v)); }
DEV f32x4 mfma16(v4u a, v4u b, f32x4 c) {
  return __builtin_amdgcn_mfma_f32_16x16x32_bf16(__builtin_bit_cast(s16x8, a), __builtin_bit_cast(s16x8, b), c, 0, 0, 0);
}
DEV f32x16 mfma32(v4u a, v4u b, f32x16 c) {
  return __builtin_amdgcn_mfma_f32_32x32x16_bf16(__builtin_bit_cast(s16x8, a), __builtin_bit_cast(s16x8, b), c, 0, 0, 0);
}
DEV v2u trread(LAS char* p) { return __builtin_bit_cast(v2u, __builtin_amdgcn_ds_read_tr16_b64_v4i16((LAS s16x4*)p)); }
DEV v4u mk_rsrc(const void* p, u32 bytes, u32 fmt) {
  const unsigned long long a = (unsigned long long)p;
  v4u r = {(u32)a, (u32)(a >> 32), bytes, fmt};
  return r;
}
DEV void dma16(u32 voff, v4u rs, u32 soff, u32 lds) {
  asm volatile("s_mov_b32 m0, %0\n\ts_nop 0\n\tbuffer_load_dwordx4 %1, %2, %3 offen lds" ::"s"(lds), "v"(voff), "s"(rs), "s"(soff) : "memory");
}
#define WAITV(n) asm volatile("s_waitcnt vmcnt(" #n ")" ::: "memory")
#define WAITL() asm volatile("s_waitcnt lgkmcnt(0)" ::: "memory")
#define SCHED() __builtin_amdgcn_sched_barrier(0)
#define BAR() do { asm volatile("s_waitcnt lgkmcnt(0)" ::: "memory"); __builtin_amdgcn_s_barrier(); asm volatile("" ::: "memory"); SCHED(); } while (0)
#define ORDER_BEHIND_POLL() __builtin_amdgcn_fence(__ATOMIC_ACQUIRE, "wavefront")

DEV f32x2 cmul(f32x2 a, f32x2 z) {
  const f32x2 zr = {-z.y, z.x};
  f32x2 r;
  asm("v_pk_mul_f32 %0, %1, %2 op_sel:[1,0] op_sel_hi:[1,1]\n\tv_pk_fma_f32 %0, %1, %3, %0 op_sel_hi:[0,1,1]" : "=&v"(r) : "v"(a), "v"(zr), "v"(z));
  return r;
}
DEV float sadd(float a, float b) { float r; asm("v_add_f32 %0, %1, %2" : "=v"(r) : "v"(a), "v"(b)); return r; }
DEV float smul(float a, float b) { float r; asm("v_mul_f32 %0, %1, %2" : "=v"(r) : "v"(a), "v"(b)); return r; }
DEV f32x2 zpow(float la, float th, float k) {
  const float mag = __builtin_amdgcn_exp2f(la * k);
  const float fr = __builtin_amdgcn_fractf(th * k);
  f32x2 r = {mag * __builtin_amdgcn_cosf(fr), mag * __builtin_amdgcn_sinf(fr)};
  return r;
}
template <int SLEEP>
DEV void poll_flags(GAS u32* p, u32 epoch, u32 first) {
  if (!__all(first == epoch)) {
#pragma unroll 1
    for (u32 spins = 0; spins < SPIN_CAP; ++spins) {
      const u32 v = __hip_atomic_load(p, RLX_AGENT);
      if (__all(v == epoch)) break;
      __builtin_amdgcn_s_sleep(SLEEP);
    }
  }
  ORDER_BEHIND_POLL();
}
DEV void publish(GAS u32* p, u32 epoch, int lane) {
  WAITV(0);
  if (lane == 0) __hip_atomic_store(p, epoch, RLX_AGENT);
}
DEV void set_progress(LAS char* L, int wv, u32 k) {
  WAITL();
  *(volatile LAS u32*)(L + LD_PS + wv * 4) = k;
}
DEV void wait_progress(LAS char* L, int word, u32 k) {
#pragma unroll 1
  for (u32 spins = 0; spins < (1u << 22); ++spins) {
    const u32 pv = *(volatile LAS u32*)(L + LD_PS + word * 4);
    if (__all(pv >= k)) break;
    __builtin_amdgcn_s_sleep(1);
  }
  asm volatile("" ::: "memory");
}
DEV void ln_stats(float s1, float s2, float& mean, float& var, float& den1) {
  mean = s1 * (1.0f / 512.0f);
  var = fmaxf(s2 * (1.0f / 512.0f) - mean * mean, 0.0f);
  den1 = bfr(__builtin_amdgcn_sqrtf(bfr(bfr(var) + 1e-5f)));
}
DEV float pick4(f32x4 g, int k) { return k == 0 ? g.x : k == 1 ? g.y : k == 2 ? g.z : g.w; }
#define LD16(p, i) ((u32)__hip_atomic_load((const GAS u16*)(p) + (i), __ATOMIC_RELAXED, __HIP_MEMORY_SCOPE_WAVEFRONT))

__global__ void __launch_bounds__(512, 2)
s4_mixer_kernel(const u16* __restrict__ x, u32 x_bytes, u32 x_fmt, const u16* __restrict__ w1, u32 w1_bytes, u32 w1_fmt,
                const u16* __restrict__ w2, char* __restrict__ ws,
                const u16* __restrict__ A_re, const u16* __restrict__ A_im, const u16* __restrict__ B_re,
                const u16* __restrict__ B_im, const u16* __restrict__ C_re, const u16* __restrict__ C_im,
                const u16* __restrict__ log_dt, const u16* __restrict__ Dskip, const u16* __restrict__ b1,
                const u16* __restrict__ b2, u16* __restrict__ out) {
  extern __shared__ __attribute__((aligned(16))) char smem[];
  LAS char* const L = (LAS char*)smem;
  unsigned long long pc;
  asm volatile("s_getpc_b64 %0" : "=s"(pc));
  {
    u32 coff = ((((threadIdx.x & 63) << 3) | (threadIdx.x >> 6)) * 32u + (blockIdx.x >> 3)) * 64u;
    coff = coff < CODE_BYTES ? coff : 0u;
    asm volatile("s_mov_b32 m0, %0\n\ts_nop 0\n\tglobal_load_lds_dword %1, %2" ::"s"((u32)LD_DUMMY), "v"(coff), "s"(pc) : "memory");
    SCHED();
  }
  const int tid = threadIdx.x, lane = tid & 63;
  const int wv = __builtin_amdgcn_readfirstlane(tid >> 6);
  const int w = wv & 3;
  const bool swave = wv >= 4;
  const int b = blockIdx.x, xcd = b & 7, idx = b >> 3;
  const int r16 = lane & 15, q = lane >> 4, qq = r16 >> 2, p4 = lane & 3;
  const bool helper = idx >= 28;
  TS(0);
  const v4u rsX = mk_rsrc(x, x_bytes, x_fmt);
  const v4u rsW1 = mk_rsrc(w1, w1_bytes, w1_fmt);
  asm volatile("" ::"s"(rsX), "s"(rsW1), "s"(w2), "s"(ws), "s"(A_re), "s"(A_im), "s"(B_re), "s"(B_im), "s"(C_re), "s"(C_im), "s"(log_dt), "s"(Dskip), "s"(b1));
  const u32 eraw = *(const __attribute__((address_space(4))) u32*)(ws + WS_E);
  const int mq = (idx * 37) >> 8, mr = idx - 7 * mq;
  const int mt = (xcd >> 1) * 4 + mq, nt = (xcd & 1) * 7 + mr;
  const int hid = xcd * 4 + (idx - 28);
  const int tg = xcd * 4 + (idx >> 3), cg = idx & 7;
  const int cw = w >> 1, hw = w & 1;
  const int ch = 64 * xcd + 2 * idx + cw;
  const __amdgpu_buffer_rsrc_t rsWS = __builtin_amdgcn_make_buffer_rsrc((void*)ws, 0, WS_END, 0x00020000);
  GAS u32* const ctl = (GAS u32*)ws;
  const v4u ones = {0x3f803f80u, 0x3f803f80u, 0x3f803f80u, 0x3f803f80u};
  int rot2 = cg + 2 * (tg & 3);
  rot2 -= rot2 >= 7 ? 7 : 0;
#define KS2(s) (7 * wv + ((s) + rot2 >= 7 ? (s) + rot2 - 7 : (s) + rot2))
#define AW_LOAD(s, c) \
  aw[s][c] = __builtin_amdgcn_raw_buffer_load_b128(rsWS, lane * 16 + ((((c) + 2 * (wv & 1)) & 3) << 10), WS_W2T + (cg * 56 + KS2(s)) * 4096, SC1)
  const u32 wo = (u32)lane * 4u;
#define TW(rowp, row, val) *(LAS u32*)((rowp) + (wo ^ (u32)(((row) & 15) << 4))) = (val)
  LAS char* const PT = L + LD_KREG + w * 8192;

  auto gemm2_tail = [&](v4u (&aw)[7][4], const u32 epoch, GAS u32* const pend, u32 fh0) __attribute__((always_inline)) {
  {
    TS(9);
    int tz = threadIdx.x;
    asm volatile("" : "+v"(tz));
    const int lane = tz & 63, r16 = lane & 15, q = lane >> 4, qq = r16 >> 2, p4 = lane & 3;
    asm volatile("" : "+v"(fh0));
    poll_flags<2>(ctl + WS_FH / 4 + (tg * 32 + ((7 * wv) >> 1) + (lane & 3)) * FL, epoch, fh0);
    TS(10);
#define G2WIN 6
    constexpr int WIN = G2WIN;
    v4u hf[28];
#define H_LOAD(n) hf[n] = __builtin_amdgcn_raw_buffer_load_b128(rsWS, lane * 16 + (((((n) & 3) + 1 + (wv >> 1)) & 3) << 10), WS_HT + (tg * 56 + KS2((n) >> 2)) * 4096, SC1)
#pragma unroll
    for (int n = 0; n < WIN; ++n) { H_LOAD(n); SCHED(); }
    if (pend) {
      asm volatile("s_waitcnt vmcnt(%0)" ::"n"(WIN) : "memory");
      if (lane == 0) __hip_atomic_store(pend, epoch, RLX_AGENT);
      __builtin_amdgcn_s_setprio(0);
      SCHED();
    }
    GAS u32* const fsp = ctl + WS_FS / 4 + ((tg >> 4) * 512 + cg * 64 + wv * 8 + (lane >> 3)) * FL;
    const int tok = tg * 64 + 16 * (wv >> 1) + r16;
    const int c8 = cg * 64 + 32 * (wv & 1) + 8 * q;
    u32 fs, fe0 = epoch, fe1 = epoch;
    bool early;
    v4u xv, bv, sv;
    f32x4 acc[4][4];
#pragma unroll
    for (int i = 0; i < 4; ++i)
#pragma unroll
      for (int j = 0; j < 4; ++j) acc[i][j] = f32x4{0.f, 0.f, 0.f, 0.f};
#define RED_WRITE(j)                                                                                                  \
  _Pragma("unroll") for (int cp = 0; cp < 2; ++cp) if ((j) < 3 || cp) {                                               \
    const int d = ((((j) + 1 + (wv >> 1)) & 3) << 1) | ((cp + wv) & 1);                                               \
    _Pragma("unroll") for (int e = 0; e < 2; ++e)                                                                     \
      *(LAS f32x4*)(L + LD_RED + (d * 7 + ((j) < 3 ? 2 * (j) + cp : 6)) * 2048 + e * 1024 + lane * 16) = acc[2 * cp + e][j]; \
  }
#pragma unroll
    for (int n = 0; n < 28; ++n) {
#pragma unroll
      for (int c = 0; c < 4; ++c) acc[c][n & 3] = mfma16(aw[n >> 2][c], hf[n], acc[c][n & 3]);
      SCHED();
      if (n == 22) wait_progress(L, lane & 7, 4u);
      if (n >= 25) RED_WRITE(n - 25)
      if (n == 24 - 2 * WIN + 4) fs = __hip_atomic_load(fsp, RLX_AGENT);
      if (n + WIN < 28) H_LOAD(n + WIN);
      if (n == 27 - WIN) {
        xv = *(const GAS v4u*)(x + tok * 512 + c8);
        bv = *(const GAS v4u*)(b2 + c8);
      }
      if (n == 28 - WIN) {
        early = __all(fs == epoch);
        ORDER_BEHIND_POLL();
        sv = __builtin_amdgcn_raw_buffer_load_b128(rsWS, (lane >> 3) * 4096 + (lane & 7) * 16, early ? WS_ST + (cg * 64 + wv * 8) * 4096 + tg * 128 : WS_END, SC1);
        if (b == 0) {
          fe0 = __hip_atomic_load(ctl + WS_FS / 4 + tz * FL, RLX_AGENT);
          fe1 = __hip_atomic_load(ctl + WS_FS / 4 + (512 + tz) * FL, RLX_AGENT);
        }
      }
      SCHED();
    }
    TS(11);
    RED_WRITE(3)
    if (!early) {
      poll_flags<2>(fsp, epoch, epoch + 1u);
      sv = __builtin_amdgcn_raw_buffer_load_b128(rsWS, (lane >> 3) * 4096 + (lane & 7) * 16, WS_ST + (cg * 64 + wv * 8) * 4096 + tg * 128, SC1);
    }
    *(LAS v4u*)(L + LD_ST + wv * 1024 + lane * 16) = sv;
    TS(12);
    asm volatile("" : "+v"(fe0), "+v"(fe1));
    poll_flags<2>(ctl + WS_FS / 4 + tz * FL, epoch, fe0);
    poll_flags<2>(ctl + WS_FS / 4 + (512 + tz) * FL, epoch, fe1);
    BAR();
    if (b == 0 && tz == 0) __hip_atomic_store(ctl + WS_E / 4, epoch, RLX_AGENT);
    TS(13);
    f32x4 part[2][7];
    v2u sq[2];
#pragma unroll
    for (int e = 0; e < 2; ++e) {
#pragma unroll
      for (int j = 0; j < 7; ++j) part[e][j] = *(LAS f32x4*)(L + LD_RED + (wv * 7 + j) * 2048 + e * 1024 + lane * 16);
      sq[e] = trread(L + LD_ST + (32 * (wv & 1) + 8 * q + 4 * e + qq) * 128 + (16 * (wv >> 1) + 4 * p4) * 2);
    }
    SCHED();
    u32 o[4];
#pragma unroll
    for (int e = 0; e < 2; ++e) {
      const u32 ba = bv[2 * e], bb = bv[2 * e + 1], xa = xv[2 * e], xb = xv[2 * e + 1];
      f32x4 m = acc[e][3] + f32x4{bflo(ba), bfhi(ba), bflo(bb), bfhi(bb)};
#pragma unroll
      for (int j = 0; j < 7; ++j) m += part[e][j];
      f32x4 g;
#pragma unroll
      for (int k = 0; k < 4; ++k) g[k] = m[k] * __builtin_amdgcn_rcpf(1.0f + __builtin_amdgcn_exp2f(m[k] * -1.4426950408889634f));
      o[2 * e] = pk2(bflo(xa) + bflo(sq[e].x) * g.x, bfhi(xa) + bfhi(sq[e].x) * g.y);
      o[2 * e + 1] = pk2(bflo(xb) + bflo(sq[e].y) * g.z, bfhi(xb) + bfhi(sq[e].y) * g.w);
    }
    *(LAS v4u*)(L + LD_KREG + (16 * (wv >> 1) + r16) * 144 + (wv & 1) * 64 + q * 16) = v4u{o[0], o[1], o[2], o[3]};
    BAR();
    const v4u ov = *(LAS v4u*)(L + LD_KREG + (8 * wv + (lane >> 3)) * 144 + (lane & 7) * 16);
    __builtin_amdgcn_raw_buffer_store_b128(ov, __builtin_amdgcn_make_buffer_rsrc((void*)out, 0, 2097152, 0x00020000), ((tg * 64 + 8 * wv + (lane >> 3)) * 512 + cg * 64 + (lane & 7) * 8) * 2, 0, 16);
  }
  TS(14);
  __builtin_amdgcn_endpgm();
  };

  if (helper) {
    LAS char* const HX = L + LD_HX + wv * 8192;
#pragma unroll
    for (int p = 0; p < 8; ++p)
      dma16((u32)(wv * 8192 + ((lane ^ (((p & 3) << 2) | (p >> 2))) << 4)), rsX, (u32)((hid * 64 + p) * 1024), (u32)(LD_HX + (wv * 8 + p) * 1024));
    u32 epoch;
    asm volatile("s_waitcnt vmcnt(0) lgkmcnt(0)\n\ts_add_u32 %0, %1, 1" : "=s"(epoch) : "s"(eraw) : "memory", "scc");
    if (!swave) TS(3);
    f32x4 gram[2], sumx[2];
#pragma unroll
    for (int i = 0; i < 2; ++i) { gram[i] = f32x4{0.f, 0.f, 0.f, 0.f}; sumx[i] = gram[i]; }
    const int r8 = lane & 7, fr = ((r8 & 3) << 2) | (r8 >> 2);
    v4u xr[8];
#pragma unroll
    for (int s = 0; s < 8; ++s) xr[s] = *(LAS v4u*)(HX + r8 * 1024 + (lane & 8) * 64 + (((4 * s + q) ^ fr) << 4));
    v2u d[8][2];
#pragma unroll
    for (int s = 0; s < 8; ++s)
#pragma unroll
      for (int t = 0; t < 2; ++t)
        d[s][t] = trread(HX + (4 * t + qq) * 1024 + (((8 * s + 2 * q + (p4 >> 1)) ^ ((qq << 2) | t)) << 4) + 8 * (p4 & 1));
    SCHED();
#pragma unroll
    for (int s = 0; s < 8; ++s) {
      gram[s & 1] = mfma16(xr[s], xr[s], gram[s & 1]);
      sumx[s & 1] = mfma16(ones, xr[s], sumx[s & 1]);
    }
    float s1 = sumx[0].x + sumx[1].x, s2 = pick4(gram[0] + gram[1], r16 & 3);
    s1 += __shfl(s1, (lane + 40) & 63);
    s2 += __shfl(s2, (lane + 40) & 63);
    float mean, var, den1;
    ln_stats(s1, s2, mean, var, den1);
    const float rd = __builtin_amdgcn_rcpf(den1);
    const float den2 = bfr(__builtin_amdgcn_sqrtf(bfr(bfr(var * rd * rd) + 1e-5f)));
    const float R = __builtin_amdgcn_rcpf(den1 * den2);
    if (q == qq && r16 < 8) {
      *(LAS f32x2*)(L + LD_HST + (8 * wv + r16) * 8) = f32x2{rd, -bfr(mean) * rd};
      __builtin_amdgcn_raw_buffer_store_b128(v4u{__builtin_bit_cast(u32, R), epoch, __builtin_bit_cast(u32, R * mean), epoch}, rsWS,
                                             WS_STAT + (hid * 64 + 8 * wv + r16) * 16, 0, SC1);
    }
    WAITL();
    if (!swave) TS(4);
    f32x4 sv[4];
#pragma unroll
    for (int i = 0; i < 4; ++i) sv[i] = *(LAS f32x4*)(L + LD_HST + wv * 64 + i * 16);
#pragma unroll
    for (int cb = 0; cb < 8; ++cb) {
      v4u o;
      o.x = pk2(bflo(d[cb][0].x) * sv[0].x + sv[0].y, bfhi(d[cb][0].x) * sv[0].z + sv[0].w);
      o.y = pk2(bflo(d[cb][0].y) * sv[1].x + sv[1].y, bfhi(d[cb][0].y) * sv[1].z + sv[1].w);
      o.z = pk2(bflo(d[cb][1].x) * sv[2].x + sv[2].y, bfhi(d[cb][1].x) * sv[2].z + sv[2].w);
      o.w = pk2(bflo(d[cb][1].y) * sv[3].x + sv[3].y, bfhi(d[cb][1].y) * sv[3].z + sv[3].w);
      *(LAS v4u*)(L + LD_HT + (cb * 64 + lane) * 128 + ((wv ^ (lane & 7)) << 4)) = o;
    }
    if (!swave) TS(5);
    BAR();
    if (swave) {
      v4u o[16];
#pragma unroll
      for (int i = 0; i < 16; ++i) o[i] = *(LAS v4u*)(L + LD_HT + (w * 128 + i * 8 + (lane >> 3)) * 128 + (((lane ^ (lane >> 3)) & 7) << 4));
      SCHED();
#pragma unroll
      for (int i = 0; i < 16; ++i) __builtin_amdgcn_raw_buffer_store_b128(o[i], rsWS, lane * 16 + WS_XNT + hid * 65536 + (w * 16 + i) * 1024, 0, SC1);
      publish(ctl + WS_FXN / 4 + (hid * 4 + w) * FL, epoch, lane);
    }
    if (!swave) TS(6);
    SCHED();
  }

  if (swave) {
    __builtin_amdgcn_s_setprio(2);
    const u32 voX = (u32)((32 * w + (lane >> 3)) * 1024 + (((lane & 7) ^ (lane >> 3)) << 4));
    const u32 voW = (u32)((4 * w + q) * 3584 + ((r16 ^ ((q << 2) | w)) << 4));
    const int rot = (mr + 2 * mq) & 7;
#define G1_STAGE(st) G1_PART(st, 0, 4)
#define G1_PART(st, p0, p1)                                                                                  \
  {                                                                                                          \
    const int sa_ = ((st) + rot) & 7;                                                                        \
    _Pragma("unroll") for (int p = p0; p < p1; ++p) {                                                        \
      dma16(voX, rsX, (u32)(mt * 131072 + p * 8192 + sa_ * 128), (u32)(((st) & 3) * 32768 + w * 4096 + p * 1024)); \
      dma16(voW, rsW1, (u32)((sa_ * 64 + 16 * p) * 3584 + nt * 256), (u32)(((st) & 3) * 32768 + 16384 + p * 4096 + w * 1024)); \
    }                                                                                                        \
  }
    if (!helper) { G1_STAGE(0) G1_STAGE(1) }
    if (lane == 0) *(LAS u32*)(L + LD_PS + wv * 4) = 0u;
    TS(1);
    WAITV(8);
    u32 epoch;
    asm volatile("s_waitcnt lgkmcnt(0)\n\ts_add_u32 %0, %1, 1" : "=s"(epoch) : "s"(eraw) : "memory", "scc");
    TS(2);
    BAR();
    TS(3);
    TC(0);
    if (!helper) G1_STAGE(2)
    const int r32 = lane & 31, hh = lane >> 5;
    v4u pf[8], qf[8];
    {
#pragma unroll
      for (int s = 0; s < 8; ++s) {
        qf[s] = *(LAS v4u*)(L + LD_Q + w * 8192 + r32 * 256 + (((2 * s + hh) ^ (r32 & 15)) << 4));
        pf[s] = *(LAS v4u*)(PT + (2 * s + hh) * 464 + r32 * 16);
      }
    }
    SCHED();
    TC(1);
    if (!helper) {
      WAITV(8); BAR(); TC(2); G1_STAGE(3) G1_PART(4, 0, 2) TC(3);
      WAITV(12); BAR(); TC(4); G1_PART(4, 2, 4) G1_STAGE(5) TC(5);
      WAITV(16); BAR(); TC(6);
      G1_STAGE(6) TC(7);
      WAITV(16); BAR(); TC(8); G1_STAGE(7) TC(9);
      WAITV(16); BAR();
    }
    TC(10);
    GAS u32* const fxnp = ctl + WS_FXN / 4 + ((hw * 16 + r16) * 4 + (ch >> 7)) * FL;
    GAS u32* const fw2p = ctl + WS_FW2 / 4 + ((cg * 56 + 7 * (w + 4 * ((lane >> 4) & 1))) * 2 + min(r16, 13)) * FL;
    const int sto = WS_STAT + ((helper ? 0 : mt) * 128 + 32 * w + r32) * 16;
    v4u sg = __builtin_amdgcn_raw_buffer_load_b128(rsWS, sto, 0, SC1);
    const u32 fw2 = __hip_atomic_load(fw2p, RLX_AGENT);
    const u32 pDs = LD16(Dskip, ch);
    SCHED();
    f32x16 kacc;
#pragma unroll
    for (int i = 0; i < 16; ++i) kacc[i] = 0.f;
#pragma unroll
    for (int s = 0; s < 8; ++s) kacc = mfma32(pf[s], qf[s], kacc);
    SCHED();
    TC(11);
    if (!helper) { WAITV(3); BAR(); }
    TC(12);
    TS(4);
#pragma unroll
    for (int i = 0; i < 4; ++i)
      *(LAS f32x4*)(L + LD_CX + w * 4096 + i * 1024 + lane * 16) = f32x4{kacc[4 * i], kacc[4 * i + 1], kacc[4 * i + 2], kacc[4 * i + 3]};
    if (!helper) {
      if (!__all(sg.y == epoch && sg.w == epoch)) {
#pragma unroll 1
        for (u32 spins = 0; spins < SPIN_CAP; ++spins) {
          __builtin_amdgcn_s_sleep(4);
          asm volatile("" ::: "memory");
          sg = __builtin_amdgcn_raw_buffer_load_b128(rsWS, sto, 0, SC1);
          if (__all(sg.y == epoch && sg.w == epoch)) break;
        }
      }
      *(LAS v2u*)(L + LD_STAT + (32 * w + r32) * 8) = v2u{sg.x, sg.z};
    }
    poll_flags<4>(fw2p, epoch, fw2);
    set_progress(L, wv, 1u);
    TS(5);
    v4u aw[7][4];
#pragma unroll
    for (int c = 0; c < 4; ++c) AW_LOAD(c >> 2, c & 3);
    SCHED();
    wait_progress(L, wv ^ 1, 1u);
    {
      f32x4 v[4];
#pragma unroll
      for (int i = 0; i < 4; ++i) v[i] = *(LAS f32x4*)(L + LD_CX + (w ^ 1) * 4096 + i * 1024 + lane * 16);
#pragma unroll
      for (int i = 0; i < 15; ++i) {
        kacc[i] = sadd(kacc[i], v[i >> 2][i & 3]);
      }
    }
    {
      const bool odd = r32 & 1, up = hh != 0;
      const int ia[4][9] = {{0, 1, 2, 3, 4, 5, 6, 7, 8}, {9, 9, 9, 9, -1, 10, 10, 10, 10}, {11, 11, 11, 11, -1, 12, 12, 12, 12}, {13, 13, 13, 13, -1, 14, 14, 14, 14}};
      const int ib[4][9] = {{0, 0, 1, 2, 2, 2, 3, 4, 4}, {5, 5, 5, 6, 6, 6, 7, 7, 7}, {8, 8, 8, 9, 9, 9, 10, 10, 10}, {11, 11, 11, 11, -1, 12, 12, 12, 12}};
      LAS char* const KC = PT + hw * 64 + (255 - r32) * 16;
#pragma unroll
      for (int u = 0; u < 4; ++u) {
        SCHED();
        AW_LOAD(1 + (u >> 1), 2 * (u & 1));
        SCHED();
        float e[9];
#pragma unroll
        for (int i = 0; i < 9; ++i) {
          const float va = ia[u][i] >= 0 ? kacc[ia[u][i]] : (odd ? kacc[ia[u][5]] : kacc[ia[u][3]]);
          const float vb = ib[u][i] >= 0 ? kacc[ib[u][i]] : (odd ? kacc[ib[u][5]] : kacc[ib[u][3]]);
          e[i] = up ? vb : va;
        }
        SCHED();
        AW_LOAD(1 + (u >> 1), 2 * (u & 1) + 1);
        SCHED();
        v4u d;
        if (hw) { d.x = pk2(e[8], e[7]); d.y = pk2(e[6], e[5]); d.z = pk2(e[4], e[3]); d.w = pk2(e[2], e[1]); }
        else    { d.x = pk2(e[7], e[6]); d.y = pk2(e[5], e[4]); d.z = pk2(e[3], e[2]); d.w = pk2(e[1], e[0]); }
        const int sa = u == 0 ? 0 : u + 3, sb = u == 3 ? 7 : u + 1;
        *(LAS v4u*)(KC - (up ? sb : sa) * 512) = d;
      }
      if (lane < 2) *(LAS v4u*)(PT + hw * 64 + 4096 + lane * 16) = v4u{(hw && lane == 0) ? pk2(kacc[0], 0.f) : 0u, 0u, 0u, 0u};
    }
    SCHED();
    TC(13);
    u32 fxa = __hip_atomic_load(fxnp, RLX_AGENT);
#pragma unroll
    for (int c = 12; c < 22; ++c) AW_LOAD(c >> 2, c & 3);
    SCHED();
    asm volatile("" : "+v"(fxa));
    const bool xearly = __all(fxa == epoch);
    ORDER_BEHIND_POLL();
    const int xo = WS_XNT + (16 * hw) * 65536 + ch * 128;
    v4u u0 = __builtin_amdgcn_raw_buffer_load_b128(rsWS, (lane >> 3) * 65536 + (lane & 7) * 16, xearly ? xo : WS_END, SC1);
    v4u u1 = __builtin_amdgcn_raw_buffer_load_b128(rsWS, (lane >> 3) * 65536 + (lane & 7) * 16, xearly ? xo + 8 * 65536 : WS_END, SC1);
#pragma unroll
    for (int c = 22; c < 28; ++c) AW_LOAD(c >> 2, c & 3);
    SCHED();
    if (!xearly) {
      poll_flags<8>(fxnp, epoch, epoch + 1u);
      u0 = __builtin_amdgcn_raw_buffer_load_b128(rsWS, (lane >> 3) * 65536 + (lane & 7) * 16, xo, SC1);
      u1 = __builtin_amdgcn_raw_buffer_load_b128(rsWS, (lane >> 3) * 65536 + (lane & 7) * 16, xo + 8 * 65536, SC1);
      __builtin_amdgcn_s_waitcnt(0x0F70);
    }
    LAS char* const UB = L + LD_U + cw * 4608;
    if (hw == 0 && lane < 32) *(LAS v4u*)(UB + lane * 16) = v4u{0u, 0u, 0u, 0u};
    *(LAS v4u*)(UB + 512 + (128 * hw + lane) * 16) = u0;
    *(LAS v4u*)(UB + 512 + (128 * hw + 64 + lane) * 16) = u1;
    set_progress(L, wv, 2u);
    {
      const u32 po = (u32)(WS_W2T + (cg * 56 + 7 * w) * 4096 + ((tg & 3) * 56 + min(lane, 55)) * 128);
      asm volatile("s_mov_b32 m0, %0\n\ts_nop 0\n\tglobal_load_lds_dword %1, %2 sc1" ::"s"((u32)LD_DUMMY), "v"(po), "s"(ws) : "memory");
    }
    SCHED();
    TS(6);
    const float dskip = bf2f(pDs);
    const int sgm = 1 - (r16 & 1);
    LAS char* const ka = L + LD_KREG + (2 * cw + sgm) * 8192 + sgm * 64 + 2 * (2031 - r16 + 8 * q + sgm) - hw * 256 - 7 * 512 - 3 * 64;
    LAS char* const ub = UB + 2 * (256 - 16 + 16 * r16 + 8 * q) - hw * 256 - 3 * 64;
    u32 kb[4];
#pragma unroll
    for (int i = 0; i < 4; ++i) { kb[i] = (u32)(size_t)ka + i * 1024; asm volatile("" : "+v"(kb[i])); }
    f32x4 y[8];
    {
      f32x4 y0[4];
#pragma unroll
      for (int i = 0; i < 4; ++i) {
        const v2u xv = *(LAS v2u*)(UB + 512 + (256 * (4 * hw + i) + 16 * r16 + 4 * q) * 2);
        y0[i] = f32x4{smul(bflo(xv.x), dskip), smul(bfhi(xv.x), dskip), smul(bflo(xv.y), dskip), smul(bfhi(xv.y), dskip)};
      }
      const f32x4 zz = {0.f, 0.f, 0.f, 0.f};
      if (hw) {
#pragma unroll
        for (int i = 0; i < 4; ++i) { y[i] = zz; y[4 + i] = y0[i]; }
      } else {
#pragma unroll
        for (int i = 0; i < 4; ++i) { y[i] = y0[i]; y[4 + i] = zz; }
      }
#pragma unroll
      for (int i = 0; i < 8; ++i) asm volatile("" : "+v"(y[i]));
    }
    wait_progress(L, wv ^ 1, 2u);
    v4u af[4][8], bfv[4][8];
#define CV_READ(rr, g)                                                                                   \
  {                                                                                                      \
    LAS u32* pa = (LAS u32*)(size_t)(kb[(7 - (g)) >> 1] + ((7 - (g)) & 1) * 512 + (3 - (rr)) * 64);      \
    af[rr][g] = v4u{pa[0], pa[1], pa[2], pa[3]};                                                         \
    bfv[rr][g] = *(LAS v4u*)(ub + (g) * 512 + (3 - (rr)) * 64);                                          \
  }
#define CV_LEVEL(rr, k, T0, T1)                                                              \
  _Pragma("unroll") for (int o = 0; o <= (k); ++o)                                           \
    if ((k) + o >= (T0) && (k) + o < (T1)) {                                                 \
      y[(k) + o] = mfma16(af[rr][k], bfv[rr][o], y[(k) + o]);                                \
      if (o < (k)) y[(k) + o] = mfma16(af[rr][o], bfv[rr][k], y[(k) + o]);                   \
    }
#define CV_MIX(n)                                                                            \
  _Pragma("unroll") for (int t = 0; t < (n); ++t) {                                          \
    __builtin_amdgcn_sched_group_barrier(0x008, 1, 0);                                       \
    __builtin_amdgcn_sched_group_barrier(0x100, 1, 0);                                       \
  }                                                                                          \
  SCHED();
#pragma unroll
    for (int g = 0; g < 4; ++g) CV_READ(0, g)
    SCHED();
    wait_progress(L, w, 4u);
    __builtin_amdgcn_s_setprio(2);
    TS(7);
    TC(14);
#pragma unroll
    for (int rr = 0; rr < 3; ++rr) {
      CV_READ(rr, 4) CV_READ(rr, 5) CV_READ(rr, 6) CV_READ(rr, 7)
      CV_LEVEL(rr, 0, 0, 8) CV_LEVEL(rr, 1, 0, 8) CV_LEVEL(rr, 2, 0, 8) CV_LEVEL(rr, 3, 0, 8)
      CV_MIX(12)
      CV_READ(rr + 1, 0) CV_READ(rr + 1, 1) CV_READ(rr + 1, 2) CV_READ(rr + 1, 3)
      CV_LEVEL(rr, 4, 0, 8) CV_LEVEL(rr, 5, 0, 8) CV_LEVEL(rr, 6, 0, 8) CV_LEVEL(rr, 7, 0, 8)
      CV_MIX(12)
    }
    f32x4 mine[4];
    CV_READ(3, 4) CV_READ(3, 5) CV_READ(3, 6) CV_READ(3, 7)
    SCHED();
    if (hw) {
      CV_LEVEL(3, 0, 0, 4) CV_LEVEL(3, 1, 0, 4) CV_LEVEL(3, 2, 0, 4) CV_LEVEL(3, 3, 0, 4)
      CV_LEVEL(3, 2, 4, 8) CV_LEVEL(3, 3, 4, 8)
      SCHED();
#pragma unroll
      for (int i = 0; i < 4; ++i) *(LAS f32x4*)(L + LD_CX + w * 4096 + i * 1024 + lane * 16) = y[i];
      *(volatile LAS u32*)(L + LD_PS + wv * 4) = 3u;
      SCHED();
      CV_LEVEL(3, 4, 4, 8) CV_LEVEL(3, 5, 4, 8) CV_LEVEL(3, 6, 4, 8) CV_LEVEL(3, 7, 4, 8)
#pragma unroll
      for (int i = 0; i < 4; ++i) mine[i] = y[4 + i];
    } else {
      CV_LEVEL(3, 2, 4, 8) CV_LEVEL(3, 3, 4, 8) CV_LEVEL(3, 4, 4, 8) CV_LEVEL(3, 5, 4, 8) CV_LEVEL(3, 6, 4, 8) CV_LEVEL(3, 7, 4, 8)
      CV_LEVEL(3, 0, 0, 4) CV_LEVEL(3, 1, 0, 4)
      SCHED();
#pragma unroll
      for (int i = 0; i < 4; ++i) *(LAS f32x4*)(L + LD_CX + w * 4096 + i * 1024 + lane * 16) = y[4 + i];
      *(volatile LAS u32*)(L + LD_PS + wv * 4) = 3u;
      SCHED();
      CV_LEVEL(3, 2, 0, 4) CV_LEVEL(3, 3, 0, 4)
#pragma unroll
      for (int i = 0; i < 4; ++i) mine[i] = y[i];
    }
    SCHED();
    TC(15);
    const u32 fh0 = __hip_atomic_load(ctl + WS_FH / 4 + (tg * 32 + ((7 * wv) >> 1) + (lane & 3)) * FL, RLX_AGENT);
    SCHED();
    wait_progress(L, wv ^ 1, 3u);
    f32x4 oy[4];
#pragma unroll
    for (int i = 0; i < 4; ++i) oy[i] = *(LAS f32x4*)(L + LD_CX + (w ^ 1) * 4096 + i * 1024 + lane * 16);
    set_progress(L, wv, 4u);
#pragma unroll
    for (int i = 0; i < 4; ++i) {
      v2u o;
      o.x = pk2(sadd(mine[i].x, oy[i].x), sadd(mine[i].y, oy[i].y));
      o.y = pk2(sadd(mine[i].z, oy[i].z), sadd(mine[i].w, oy[i].w));
      __builtin_amdgcn_raw_buffer_store_b64(o, rsWS, (16 * r16 + 4 * q) * 2 + WS_ST + ch * 4096 + (4 * hw + i) * 512, 0, SC1);
    }
    TS(8);
    gemm2_tail(aw, epoch, ctl + WS_FS / 4 + (hw * 512 + ch) * FL, fh0);
  } else {
    if (!helper) __builtin_amdgcn_s_setprio(3);
    const int po = ch * 128 + hw * 64 + lane;
    const u32 pLd = LD16(log_dt, ch), pAr = LD16(A_re, po), pAi = LD16(A_im, po);
    const u32 pBr = LD16(B_re, po), pBi = LD16(B_im, po), pCr = LD16(C_re, po), pCi = LD16(C_im, po);
    const u32 pB1 = LD16(b1, (helper ? 0 : nt * 128) + (tid & 127));
    const int uks = 2 * (helper ? 0 : idx) + (w >> 1), upp = w & 1;
    v4u w2a, w2b;
    {
      const GAS v4u* src = (const GAS v4u*)(w2 + (uks * 32 + (lane >> 2)) * 512 + xcd * 64 + upp * 32 + (lane & 3) * 8);
      asm volatile("" : "+v"(src));
      w2a = src[0];
      w2b = src[16 * 64];
    }
    if (lane == 0) *(LAS u32*)(L + LD_PS + wv * 4) = 0u;
    SCHED();
    TS(1);
    const int mi = w >> 1, ni = w & 1;
    f32x4 acc[4][4], csum[4];
#pragma unroll
    for (int i = 0; i < 4; ++i) {
#pragma unroll
      for (int j = 0; j < 4; ++j) { acc[i][j] = f32x4{0.f, 0.f, 0.f, 0.f}; asm volatile("" : "+v"(acc[i][j])); }
      csum[i] = f32x4{0.f, 0.f, 0.f, 0.f};
      asm volatile("" : "+v"(csum[i]));
    }
    u32 xb[2][2], wb[2][4][2];
#pragma unroll
    for (int h = 0; h < 2; ++h) {
#pragma unroll
      for (int k = 0; k < 2; ++k) {
        xb[h][k] = (u32)(((64 * mi + r16) * 128 + ((q ^ (r16 & 7)) << 4)) ^ (k * 64)) + h * 65536;
        asm volatile("" : "+v"(xb[h][k]));
      }
#pragma unroll
      for (int j = 0; j < 4; ++j)
#pragma unroll
        for (int t = 0; t < 2; ++t) {
          wb[h][j][t] = (u32)(16384 + (8 * q + 4 * t + qq) * 256 + (((2 * (4 * ni + j) + (p4 >> 1)) ^ ((qq << 2) | ((2 * q + t) & 3))) << 4) + 8 * (p4 & 1)) + h * 65536;
          asm volatile("" : "+v"(wb[h][j][t]));
        }
    }
    v4u onev = ones;
    asm volatile("" : "+v"(onev));
    u32 qad[16];
#pragma unroll
    for (int r = 0; r < 16; ++r) { qad[r] = (u32)(LD_Q + w * 8192) + (wo ^ (u32)(r << 4)); asm volatile("" : "+v"(qad[r])); }
    SCHED();
    TC(1);
    u32 epoch;
    asm volatile("s_waitcnt lgkmcnt(0)\n\ts_add_u32 %0, %1, 1" : "=s"(epoch) : "s"(eraw) : "memory", "scc");
    SCHED();
    {
      const float dt = bfr(__builtin_amdgcn_exp2f(bf2f(pLd) * 1.4426950408889634f));
      const float la = dt * bf2f(pAr) * 1.4426950408889634f;
      const float th = dt * bf2f(pAi) * 0.15915494309189535f;
      const f32x2 dBc = {dt * bf2f(pBr), -dt * bf2f(pBi)};
      const f32x2 Ccc = {bf2f(pCr), -bf2f(pCi)};
      const f32x2 z8 = zpow(la, th, 8.f), z16 = zpow(la, th, 16.f);
      const f32x2 y1 = zpow(la, -th, 1.f), y256 = zpow(la, -th, 256.f), y8 = {z8.x, -z8.y};
      const f32x2 y2 = cmul(y1, y1), y4 = cmul(y2, y2);
#define PROW(h, g) (((g) & 3) + 8 * ((g) >> 2) + 4 * (h))
#define PW(h, g, v) *(LAS u32*)(PT + (lane >> 2) * 464 + (lane & 3) * 4 + PROW(h, g) * 16) = pk2v(v)
      f32x2 W[8];
      W[0] = cmul(Ccc, dBc);
#pragma unroll
      for (int c = 1; c < 8; ++c) W[c] = cmul(W[c - 1], y256);
      {
        f32x2 te = W[0], to = cmul(W[0], y1), t1 = W[1];
        PW(0, 0, te); PW(0, 1, to); PW(1, 0, t1);
#pragma unroll
        for (int a = 1; a <= 4; ++a) {
          te = cmul(te, y2); PW(0, 2 * a, te);
          if (a < 4) { to = cmul(to, y2); PW(0, 2 * a + 1, to); }
          t1 = cmul(t1, y2); PW(1, a, t1);
        }
      }
      PW(1, 5, W[2]); PW(1, 6, cmul(W[2], y4)); PW(1, 7, cmul(W[2], y8));
      PW(1, 8, W[3]); PW(1, 9, cmul(W[3], y4)); PW(1, 10, cmul(W[3], y8));
      PW(0, 9, W[4]); PW(0, 10, cmul(W[4], y8));
      PW(0, 11, W[5]); PW(0, 12, cmul(W[5], y8));
      PW(0, 13, W[6]); PW(0, 14, cmul(W[6], y8));
      PW(1, 11, W[7]); PW(1, 12, cmul(W[7], y8));
      f32x2 qa = {1.f, 0.f}, qb = z8;
      u32 qv[32];
#pragma unroll
      for (int i = 0; i < 16; ++i) {
        qv[2 * i] = pk2v(qa);
        qv[2 * i + 1] = pk2v(qb);
        if (i < 15) { qa = (i == 0) ? z16 : cmul(qa, z16); qb = cmul(qb, z16); }
      }
#pragma unroll
      for (int r = 0; r < 16; ++r) {
        *(LAS u32*)(size_t)(qad[r] + r * 256) = qv[r];
        *(LAS u32*)(size_t)(qad[r] + (r + 16) * 256) = qv[r + 16];
      }
    }
    SCHED();
    TC(2);
    {
      LAS char* const WS2 = L + LD_W2S + w * 2048;
      *(LAS v4u*)(WS2 + lane * 16) = w2a;
      *(LAS v4u*)(WS2 + 1024 + lane * 16) = w2b;
#pragma unroll
      for (int e = 0; e < 2; ++e) {
        const v2u lo = trread(WS2 + (4 * q + qq) * 64 + 16 * p4 + 8 * e), hi = trread(WS2 + (16 + 4 * q + qq) * 64 + 16 * p4 + 8 * e);
        __builtin_amdgcn_raw_buffer_store_b128(v4u{lo.x, lo.y, hi.x, hi.y}, rsWS,
                                               lane * 16 + (helper ? WS_END : WS_W2T) + ((xcd * 56 + uks) * 4 + 2 * upp + e) * 1024, 0, SC1);
      }
    }
    *(LAS float*)(L + LD_B1 + (tid & 127) * 4) = bf2f(pB1);
    TC(3);
    TS(2);
    v4u aw[7][4];
    u32 fh0 = epoch + 1u;
    if (!helper) {
      v4u xf[2][4], wf[2][4];
#define G1_READ(n)                                                                                            \
  _Pragma("unroll") for (int i = 0; i < 4; ++i) {                                                             \
    const v2u lo = trread(L + (((n) >> 1) & 1) * 32768 + ((n) & 1) * 8192 + wb[((n) >> 2) & 1][i][0]);        \
    const v2u hi = trread(L + (((n) >> 1) & 1) * 32768 + ((n) & 1) * 8192 + wb[((n) >> 2) & 1][i][1]);        \
    wf[(n) & 1][i] = v4u{lo.x, lo.y, hi.x, hi.y};                                                             \
    xf[(n) & 1][i] = *(LAS v4u*)(L + (((n) >> 1) & 1) * 32768 + i * 2048 + xb[((n) >> 2) & 1][(n) & 1]);      \
  }
      __builtin_amdgcn_s_setprio(0);
      TC(4);
      BAR();
      TC(5);
      TS(3);
      G1_READ(0)
      SCHED();
#pragma unroll 1
      for (int it = 0; it < 2; ++it) {
#pragma unroll
        for (int n = 0; n < 8; ++n) {
          if (n & 1) {
            if (n < 4 || it == 0) BAR();
            if (n == 5 && it == 0) {
              __builtin_amdgcn_s_setprio(3);
              publish(ctl + WS_FW2 / 4 + ((xcd * 56 + uks) * 2 + upp) * FL, epoch, lane);
              __builtin_amdgcn_s_setprio(0);
            }
            SCHED();
          }
          G1_READ(n + 1)
#pragma unroll
          for (int j = 0; j < 4; ++j) {
#pragma unroll
            for (int i = 0; i < 4; ++i) acc[j][i] = mfma16(wf[n & 1][j], xf[n & 1][i], acc[j][i]);
            csum[j] = mfma16(wf[n & 1][j], onev, csum[j]);
          }
#pragma unroll
          for (int k = 0; k < 12; ++k) {
            __builtin_amdgcn_sched_group_barrier(0x008, 1, 0);
            __builtin_amdgcn_sched_group_barrier(0x100, 1, 0);
          }
          __builtin_amdgcn_sched_group_barrier(0x008, 8, 0);
          SCHED();
        }
      }
      TS(4);
      __builtin_amdgcn_s_setprio(3);
      wait_progress(L, 4 + (lane & 3), 1u);
      TS(5);
      float rs[4], rm[4];
      f32x4 bb[4];
#pragma unroll
      for (int i = 0; i < 4; ++i) {
        const f32x2 st = *(LAS f32x2*)(L + LD_STAT + (64 * mi + 16 * i + r16) * 8);
        rs[i] = st.x; rm[i] = -st.y;
        asm volatile("" : "+v"(rm[i]));
        bb[i] = *(LAS f32x4*)(L + LD_B1 + (64 * ni + 16 * i + 4 * q) * 4);
      }
      SCHED();
#pragma unroll
      for (int jp = 0; jp < 2; ++jp) {
#pragma unroll
        for (int i = 0; i < 4; ++i) {
          TC(8 + jp * 4 + i);
          f32x2 pre[4], ee[4];
#pragma unroll
          for (int k = 0; k < 4; ++k) {
            const f32x4 a4 = acc[2 * jp + (k >> 1)][i], c4 = csum[2 * jp + (k >> 1)], b4 = bb[2 * jp + (k >> 1)];
            const f32x2 a2 = (k & 1) ? f32x2{a4.z, a4.w} : f32x2{a4.x, a4.y};
            const f32x2 c2 = (k & 1) ? f32x2{c4.z, c4.w} : f32x2{c4.x, c4.y};
            const f32x2 b2v = (k & 1) ? f32x2{b4.z, b4.w} : f32x2{b4.x, b4.y};
            pre[k] = a2 * rs[i] + (c2 * rm[i] + b2v);
            f32x2 arg = pre[k] * (pre[k] * pre[k] * -0.1029432397f + -2.302208198f);
            asm("" : "+v"(arg));
            ee[k] = f32x2{__builtin_amdgcn_exp2f(arg.x), __builtin_amdgcn_exp2f(arg.y)};
          }
          u32 o[4];
#pragma unroll
          for (int k = 0; k < 4; ++k) {
            f32x2 dn = ee[k] + 1.0f;
            asm("" : "+v"(dn));
            o[k] = pk2v(pre[k] * f32x2{__builtin_amdgcn_rcpf(dn.x), __builtin_amdgcn_rcpf(dn.y)});
          }
          __builtin_amdgcn_raw_buffer_store_b128(v4u{o[0], o[1], o[2], o[3]}, rsWS,
                                                 lane * 16 + ABL_HT + (((2 * mt + mi) * 56 + nt * 4 + 2 * ni + jp) * 4 + i) * 1024, 0, SC1);
          SCHED();
        }
      }
      set_progress(L, wv, 4u);
      __builtin_amdgcn_s_setprio(0);
      TS(6);
#pragma unroll
      for (int c = 0; c < 8; ++c) AW_LOAD(c >> 2, c & 3);
      SCHED();
      __builtin_amdgcn_s_setprio(3);
      WAITV(8);
      if (lane == 0) __hip_atomic_store(ctl + WS_FH / 4 + ((2 * mt + mi) * 32 + 2 * nt + ni) * FL, epoch, RLX_AGENT);
      __builtin_amdgcn_s_setprio(0);
      SCHED();
      TS(7);
#pragma unroll
      for (int c = 8; c < 20; ++c) AW_LOAD(c >> 2, c & 3);
      fh0 = __hip_atomic_load(ctl + WS_FH / 4 + (tg * 32 + ((7 * wv) >> 1) + (lane & 3)) * FL, RLX_AGENT);
#pragma unroll
      for (int c = 20; c < 28; ++c) AW_LOAD(c >> 2, c & 3);
      SCHED();
    } else {
      __builtin_amdgcn_s_setprio(0);
      BAR();
      set_progress(L, wv, 4u);
      wait_progress(L, 4 + (lane & 3), 2u);
#pragma unroll
      for (int s = 0; s < 7; ++s)
#pragma unroll
        for (int c = 0; c < 4; ++c) AW_LOAD(s, c);
      SCHED();
    }
    TS(8);
    gemm2_tail(aw, epoch, nullptr, fh0);
  }
}

extern "C" void kernel_launch(void* const* d_in, const int* in_sizes, int n_in, void* d_out, int out_size, void* d_ws,
                              size_t ws_size, hipStream_t stream) {
  (void)in_sizes; (void)n_in; (void)out_size; (void)ws_size;
  (void)hipFuncSetAttribute((const void*)s4_mixer_kernel, hipFuncAttributeMaxDynamicSharedMemorySize, LDS_BYTES);
  s4_mixer_kernel<<<256, 512, LDS_BYTES, stream>>>(
      (const u16*)d_in[0], 2097152u, 0x00020000u, (const u16*)d_in[10], 1835008u, 0x00020000u, (const u16*)d_in[12], (char*)d_ws, (const u16*)d_in[1], (const u16*)d_in[2],
      (const u16*)d_in[3], (const u16*)d_in[4], (const u16*)d_in[5], (const u16*)d_in[6], (const u16*)d_in[7],
      (const u16*)d_in[8], (const u16*)d_in[11], (const u16*)d_in[13], (u16*)d_out);
}
```

```cpp
#define STAMP
#include <hip/hip_runtime.h>
#include <hip/hip_bf16.h>

typedef unsigned int u32;
typedef unsigned short u16;
typedef u32 v4u __attribute__((ext_vector_type(4)));
typedef u32 v2u __attribute__((ext_vector_type(2)));
typedef float f32x2 __attribute__((ext_vector_type(2)));
typedef float f32x4 __attribute__((ext_vector_type(4)));
typedef float f32x16 __attribute__((ext_vector_type(16)));
typedef short s16x4 __attribute__((ext_vector_type(4)));
typedef short s16x8 __attribute__((ext_vector_type(8)));
typedef __bf16 bf16x2 __attribute__((ext_vector_type(2)));
#define GAS __attribute__((address_space(1)))
#define LAS __attribute__((address_space(3)))
#define DEV __device__ __forceinline__
#define RLX_AGENT __ATOMIC_RELAXED, __HIP_MEMORY_SCOPE_AGENT
#define SC1 16
#define TS(i) do { SCHED(); ts[i] = (u32)__builtin_amdgcn_s_memrealtime(); SCHED(); } while (0)
#define TC(i) do { SCHED(); unsigned long long tt_; asm volatile("s_memtime %0\n\ts_waitcnt lgkmcnt(0)" : "=s"(tt_)::"memory"); tc[i] = (u32)tt_; SCHED(); } while (0)

constexpr int WS_E = 0;
constexpr int FL = 32;
constexpr int WS_STAT = 4096;
constexpr int WS_FXN = 65536;
constexpr int WS_FW2 = WS_FXN + 256 * 128;
constexpr int WS_FH = WS_FW2 + 1024 * 128;
constexpr int WS_FS = WS_FH + 1024 * 128;
constexpr int WS_XNT = 524288;
constexpr int WS_W2T = WS_XNT + 2097152;
constexpr int WS_HT = WS_W2T + 1835008;
constexpr int WS_ST = WS_HT + 7340032;
constexpr int WS_END = WS_ST + 2097152;
constexpr int LD_Q = 98304;
constexpr int LD_KREG = 131072;
constexpr int LD_PS = LD_KREG + 7424;
constexpr int LD_DUMMY = LD_KREG + 7680;
constexpr int LD_B1 = LD_KREG + 8192 + 7424;
constexpr int LD_HST = LD_KREG + 3 * 8192 + 7424;
constexpr int LD_STAT = 12288;
constexpr int LD_U = 0;
constexpr int LD_CX = 16384;
constexpr int LD_HT = 0;
constexpr int LD_HX = 65536;
constexpr int LD_RED = 0;
constexpr int LD_ST = 114688;
constexpr int ABL_HT = WS_HT;
constexpr int LDS_BYTES = 163840;
constexpr u32 SPIN_CAP = 1u << 16;
constexpr u32 CODE_BYTES = $$$$$$$$$$$$$$$$$$$$$$$$$$$$$$$$$$$$$$$$$$$$$$$$$$$$$$$$$$$$$$$$$$$$$$$$$$$$$$$$$$;

DEV float bf2f(u32 h) { return __builtin_bit_cast(float, h << 16); }
DEV float bflo(u32 d) { return __builtin_bit_cast(float, d << 16); }
DEV float bfhi(u32 d) { return __builtin_bit_cast(float, d & 0xffff0000u); }
DEV u32 pk2(float lo, float hi) {
  f32x2 v = {lo, hi};
  return __builtin_bit_cast(u32, __builtin_convertvector(v, bf16x2));
}
DEV u32 pk2v(f32x2 v) { return __builtin_bit_cast(u32, __builtin_convertvector(v, bf16x2)); }
DEV float bfr(float v) { return bflo(pk2(v, v)); }
DEV f32x4 mfma16(v4u a, v4u b, f32x4 c) {
  return __builtin_amdgcn_mfma_f32_16x16x32_bf16(__builtin_bit_cast(s16x8, a), __builtin_bit_cast(s16x8, b), c, 0, 0, 0);
}
DEV f32x16 mfma32(v4u a, v4u b, f32x16 c) {
  return __builtin_amdgcn_mfma_f32_32x32x16_bf16(__builtin_bit_cast(s16x8, a), __builtin_bit_cast(s16x8, b), c, 0, 0, 0);
}
DEV v2u trread(LAS char* p) { return __builtin_bit_cast(v2u, __builtin_amdgcn_ds_read_tr16_b64_v4i16((LAS s16x4*)p)); }
DEV v4u mk_rsrc(const void* p, u32 bytes, u32 fmt) {
  const unsigned long long a = (unsigned long long)p;
  v4u r = {(u32)a, (u32)(a >> 32), bytes, fmt};
  return r;
}
DEV void dma16(u32 voff, v4u rs, u32 soff, u32 lds) {
  asm volatile("s_mov_b32 m0, %0\n\ts_nop 0\n\tbuffer_load_dwordx4 %1, %2, %3 offen lds" ::"s"(lds), "v"(voff), "s"(rs), "s"(soff) : "memory");
}
#define WAITV(n) asm volatile("s_waitcnt vmcnt(" #n ")" ::: "memory")
#define WAITL() asm volatile("s_waitcnt lgkmcnt(0)" ::: "memory")
#define SCHED() __builtin_amdgcn_sched_barrier(0)
#define BAR() do { asm volatile("s_waitcnt lgkmcnt(0)" ::: "memory"); __builtin_amdgcn_s_barrier(); asm volatile("" ::: "memory"); SCHED(); } while (0)
#define ORDER_BEHIND_POLL() __builtin_amdgcn_fence(__ATOMIC_ACQUIRE, "wavefront")

DEV f32x2 cmul(f32x2 a, f32x2 z) {
  const f32x2 zr = {-z.y, z.x};
  f32x2 r;
  asm("v_pk_mul_f32 %0, %1, %2 op_sel:[1,0] op_sel_hi:[1,1]\n\tv_pk_fma_f32 %0, %1, %3, %0 op_sel_hi:[0,1,1]" : "=&v"(r) : "v"(a), "v"(zr), "v"(z));
  return r;
}
DEV float sadd(float a, float b) { float r; asm("v_add_f32 %0, %1, %2" : "=v"(r) : "v"(a), "v"(b)); return r; }
DEV float smul(float a, float b) { float r; asm("v_mul_f32 %0, %1, %2" : "=v"(r) : "v"(a), "v"(b)); return r; }
DEV f32x2 zpow(float la, float th, float k) {
  const float mag = __builtin_amdgcn_exp2f(la * k);
  const float fr = __builtin_amdgcn_fractf(th * k);
  f32x2 r = {mag * __builtin_amdgcn_cosf(fr), mag * __builtin_amdgcn_sinf(fr)};
  return r;
}
template <int SLEEP>
DEV void poll_flags(GAS u32* p, u32 epoch, u32 first) {
  if (!__all(first == epoch)) {
#pragma unroll 1
    for (u32 spins = 0; spins < SPIN_CAP; ++spins) {
      const u32 v = __hip_atomic_load(p, RLX_AGENT);
      if (__all(v == epoch)) break;
      __builtin_amdgcn_s_sleep(SLEEP);
    }
  }
  ORDER_BEHIND_POLL();
}
DEV void publish(GAS u32* p, u32 epoch, int lane) {
  WAITV(0);
  if (lane == 0) __hip_atomic_store(p, epoch, RLX_AGENT);
}
DEV void set_progress(LAS char* L, int wv, u32 k) {
  WAITL();
  *(volatile LAS u32*)(L + LD_PS + wv * 4) = k;
}
DEV void wait_progress(LAS char* L, int word, u32 k) {
#pragma unroll 1
  for (u32 spins = 0; spins < (1u << 22); ++spins) {
    const u32 pv = *(volatile LAS u32*)(L + LD_PS + word * 4);
    if (__all(pv >= k)) break;
    __builtin_amdgcn_s_sleep(1);
  }
  asm volatile("" ::: "memory");
}
DEV void ln_stats(float s1, float s2, float& mean, float& var, float& den1) {
  mean = s1 * (1.0f / 512.0f);
  var = fmaxf(s2 * (1.0f / 512.0f) - mean * mean, 0.0f);
  den1 = bfr(__builtin_amdgcn_sqrtf(bfr(bfr(var) + 1e-5f)));
}
DEV float pick4(f32x4 g, int k) { return k == 0 ? g.x : k == 1 ? g.y : k == 2 ? g.z : g.w; }
#define LD16(p, i) ((u32)__hip_atomic_load((const GAS u16*)(p) + (i), __ATOMIC_RELAXED, __HIP_MEMORY_SCOPE_WAVEFRONT))

__global__ void __launch_bounds__(512, 2)
s4_mixer_kernel(const u16* __restrict__ x, u32 x_bytes, u32 x_fmt, const u16* __restrict__ w1, u32 w1_bytes, u32 w1_fmt,
                const u16* __restrict__ w2, char* __restrict__ ws,
                const u16* __restrict__ A_re, const u16* __restrict__ A_im, const u16* __restrict__ B_re,
                const u16* __restrict__ B_im, const u16* __restrict__ C_re, const u16* __restrict__ C_im,
                const u16* __restrict__ log_dt, const u16* __restrict__ Dskip, const u16* __restrict__ b1,
                const u16* __restrict__ b2, u16* __restrict__ out) {
  extern __shared__ __attribute__((aligned(16))) char smem[];
  LAS char* const L = (LAS char*)smem;
  unsigned long long pc;
  asm volatile("s_getpc_b64 %0" : "=s"(pc));
  {
    u32 coff = ((((threadIdx.x & 63) << 3) | (threadIdx.x >> 6)) * 32u + (blockIdx.x >> 3)) * 64u;
    coff = coff < CODE_BYTES ? coff : 0u;
    asm volatile("s_mov_b32 m0, %0\n\ts_nop 0\n\tglobal_load_lds_dword %1, %2" ::"s"((u32)LD_DUMMY), "v"(coff), "s"(pc) : "memory");
    SCHED();
  }
  const int tid = threadIdx.x, lane = tid & 63;
  const int wv = __builtin_amdgcn_readfirstlane(tid >> 6);
  const int w = wv & 3;
  const bool swave = wv >= 4;
  const int b = blockIdx.x, xcd = b & 7, idx = b >> 3;
  const int r16 = lane & 15, q = lane >> 4, qq = r16 >> 2, p4 = lane & 3;
  const bool helper = idx >= 28;
  u32 ts[16], tc[16];
#pragma unroll
  for (int i = 0; i < 16; ++i) { ts[i] = 0; tc[i] = 0; }
  TS(0);
  const u32 cyc0 = (u32)__builtin_amdgcn_s_memtime();
  const v4u rsX = mk_rsrc(x, x_bytes, x_fmt);
  const v4u rsW1 = mk_rsrc(w1, w1_bytes, w1_fmt);
  asm volatile("" ::"s"(rsX), "s"(rsW1), "s"(w2), "s"(ws), "s"(A_re), "s"(A_im), "s"(B_re), "s"(B_im), "s"(C_re), "s"(C_im), "s"(log_dt), "s"(Dskip), "s"(b1));
  const int mq = (idx * 37) >> 8, mr = idx - 7 * mq;
  const int mt = (xcd >> 1) * 4 + mq, nt = (xcd & 1) * 7 + mr;
  const int cw = w >> 1, hw = w & 1;
  const int ch = 64 * xcd + 2 * idx + cw;
  const int rot = (mr + 2 * mq) & 7;
  const int uks = 2 * (helper ? 0 : idx) + cw;
  const int hid = xcd * 4 + (idx - 28);
  if (helper) {
#pragma unroll 1
    for (int p = 0; p < 8; ++p)
      dma16((u32)(wv * 8192 + ((lane ^ (((p & 3) << 2) | (p >> 2))) << 4)), rsX, (u32)((hid * 64 + p) * 1024), (u32)(LD_HX + (wv * 8 + p) * 1024));
  }
  if (swave) {
    if (!helper) {
      const u32 voX = (u32)((32 * w + (lane >> 3)) * 1024 + (((lane & 7) ^ (lane >> 3)) << 4));
      const u32 voW = (u32)((4 * w + q) * 3584 + ((r16 ^ ((q << 2) | w)) << 4));
#pragma unroll 1
      for (int i = 0; i < 8; ++i) {
        const int st = i >> 2, pp = i & 3, sa = (st + rot) & 7;
        dma16(voX, rsX, (u32)(mt * 131072 + pp * 8192 + sa * 128), (u32)(st * 32768 + w * 4096 + pp * 1024));
        dma16(voW, rsW1, (u32)((sa * 64 + 16 * pp) * 3584 + nt * 256), (u32)(st * 32768 + 16384 + pp * 4096 + w * 1024));
      }
    }
  } else {
    if (!helper) __builtin_amdgcn_s_setprio(3);
    const u32 po = (u32)(ch * 128 + hw * 64 + lane) * 2u, bo = (u32)((helper ? 0 : nt * 128) + (tid & 127)) * 2u;
    u32 co = (u32)ch * 2u;
    asm volatile("" : "+v"(co));
    const u32 pl = (u32)(LD_KREG + w * 8192);
#define PDMA(k, vo, base) asm volatile("s_add_u32 m0, %0, " #k "*256\n\ts_nop 0\n\tglobal_load_lds_ushort %1, %2" ::"s"(pl), "v"(vo), "s"(base) : "memory", "scc")
    PDMA(0, co, log_dt); PDMA(1, po, A_re); PDMA(2, po, A_im); PDMA(3, po, B_re); PDMA(4, po, B_im); PDMA(5, po, C_re); PDMA(6, po, C_im); PDMA(7, bo, b1);
    const u32 wo2 = (u32)((uks * 32 + (lane >> 2)) * 1024 + xcd * 128 + hw * 64 + (lane & 3) * 16), wo3 = wo2 + 16384u;
    const u32 wl = pl + 2048u;
    asm volatile("s_mov_b32 m0, %0\n\ts_nop 0\n\tglobal_load_lds_dwordx4 %1, %3\n\ts_add_u32 m0, %0, 1024\n\ts_nop 0\n\tglobal_load_lds_dwordx4 %2, %3"
                 ::"s"(wl), "v"(wo2), "v"(wo3), "s"(w2) : "memory", "scc");
  }
  SCHED();
  const u32 eraw = *(const __attribute__((address_space(4))) u32*)(ws + WS_E);
  const int tg = xcd * 4 + (idx >> 3), cg = idx & 7;
  const __amdgpu_buffer_rsrc_t rsWS = __builtin_amdgcn_make_buffer_rsrc((void*)ws, 0, WS_END, 0x00020000);
  GAS u32* const ctl = (GAS u32*)ws;
  const v4u ones = {0x3f803f80u, 0x3f803f80u, 0x3f803f80u, 0x3f803f80u};
  int rot2 = cg + 2 * (tg & 3);
  rot2 -= rot2 >= 7 ? 7 : 0;
#define KS2(s) (7 * wv + ((s) + rot2 >= 7 ? (s) + rot2 - 7 : (s) + rot2))
#define AW_LOAD(s, c) \
  aw[s][c] = __builtin_amdgcn_raw_buffer_load_b128(rsWS, lane * 16 + ((((c) + 2 * (wv & 1)) & 3) << 10), WS_W2T + (cg * 56 + KS2(s)) * 4096, SC1)
  const u32 wo = (u32)lane * 4u;
#define TW(rowp, row, val) *(LAS u32*)((rowp) + (wo ^ (u32)(((row) & 15) << 4))) = (val)
  LAS char* const PT = L + LD_KREG + w * 8192;

  auto gemm2_tail = [&](v4u (&aw)[7][4], const u32 epoch, GAS u32* const pend, u32 fh0) __attribute__((always_inline)) {
  {
    TS(9);
    int tz = threadIdx.x;
    asm volatile("" : "+v"(tz));
    const int lane = tz & 63, r16 = lane & 15, q = lane >> 4, qq = r16 >> 2, p4 = lane & 3;
    asm volatile("" : "+v"(fh0));
    poll_flags<2>(ctl + WS_FH / 4 + (tg * 32 + ((7 * wv) >> 1) + (lane & 3)) * FL, epoch, fh0);
    TS(10);
#define G2WIN 6
    constexpr int WIN = G2WIN;
    v4u hf[28];
#define H_LOAD(n) hf[n] = __builtin_amdgcn_raw_buffer_load_b128(rsWS, lane * 16 + (((((n) & 3) + 1 + (wv >> 1)) & 3) << 10), WS_HT + (tg * 56 + KS2((n) >> 2)) * 4096, SC1)
#pragma unroll
    for (int n = 0; n < WIN; ++n) { H_LOAD(n); SCHED(); }
    if (pend) {
      asm volatile("s_waitcnt vmcnt(%0)" ::"n"(WIN) : "memory");
      if (lane == 0) __hip_atomic_store(pend, epoch, RLX_AGENT);
      __builtin_amdgcn_s_setprio(0);
      SCHED();
    }
    GAS u32* const fsp = ctl + WS_FS / 4 + ((tg >> 4) * 512 + cg * 64 + wv * 8 + (lane >> 3)) * FL;
    const int tok = tg * 64 + 16 * (wv >> 1) + r16;
    const int c8 = cg * 64 + 32 * (wv & 1) + 8 * q;
    u32 fs, fe0 = epoch, fe1 = epoch;
    bool early;
    v4u xv, bv, sv;
    f32x4 acc[4][4];
#pragma unroll
    for (int i = 0; i < 4; ++i)
#pragma unroll
      for (int j = 0; j < 4; ++j) acc[i][j] = f32x4{0.f, 0.f, 0.f, 0.f};
#define RED_WRITE(j)                                                                                                  \
  _Pragma("unroll") for (int cp = 0; cp < 2; ++cp) if ((j) < 3 || cp) {                                               \
    const int d = ((((j) + 1 + (wv >> 1)) & 3) << 1) | ((cp + wv) & 1);                                               \
    _Pragma("unroll") for (int e = 0; e < 2; ++e)                                                                     \
      *(LAS f32x4*)(L + LD_RED + (d * 7 + ((j) < 3 ? 2 * (j) + cp : 6)) * 2048 + e * 1024 + lane * 16) = acc[2 * cp + e][j]; \
  }
#pragma unroll
    for (int n = 0; n < 28; ++n) {
#pragma unroll
      for (int c = 0; c < 4; ++c) acc[c][n & 3] = mfma16(aw[n >> 2][c], hf[n], acc[c][n & 3]);
      SCHED();
      if (n == 22) wait_progress(L, lane & 7, 4u);
      if (n >= 25) RED_WRITE(n - 25)
      if (n == 24 - 2 * WIN + 4) fs = __hip_atomic_load(fsp, RLX_AGENT);
      if (n + WIN < 28) H_LOAD(n + WIN);
      if (n == 27 - WIN) {
        xv = *(const GAS v4u*)(x + tok * 512 + c8);
        bv = *(const GAS v4u*)(b2 + c8);
      }
      if (n == 28 - WIN) {
        early = __all(fs == epoch);
        ORDER_BEHIND_POLL();
        sv = __builtin_amdgcn_raw_buffer_load_b128(rsWS, (lane >> 3) * 4096 + (lane & 7) * 16, early ? WS_ST + (cg * 64 + wv * 8) * 4096 + tg * 128 : WS_END, SC1);
        if (b == 0) {
          fe0 = __hip_atomic_load(ctl + WS_FS / 4 + tz * FL, RLX_AGENT);
          fe1 = __hip_atomic_load(ctl + WS_FS / 4 + (512 + tz) * FL, RLX_AGENT);
        }
      }
      SCHED();
    }
    TS(11);
    RED_WRITE(3)
    if (!early) {
      poll_flags<2>(fsp, epoch, epoch + 1u);
      sv = __builtin_amdgcn_raw_buffer_load_b128(rsWS, (lane >> 3) * 4096 + (lane & 7) * 16, WS_ST + (cg * 64 + wv * 8) * 4096 + tg * 128, SC1);
    }
    *(LAS v4u*)(L + LD_ST + wv * 1024 + lane * 16) = sv;
    TS(12);
    asm volatile("" : "+v"(fe0), "+v"(fe1));
    poll_flags<2>(ctl + WS_FS / 4 + tz * FL, epoch, fe0);
    poll_flags<2>(ctl + WS_FS / 4 + (512 + tz) * FL, epoch, fe1);
    BAR();
    if (b == 0 && tz == 0) __hip_atomic_store(ctl + WS_E / 4, epoch, RLX_AGENT);
    TS(13);
    f32x4 part[2][7];
    v2u sq[2];
#pragma unroll
    for (int e = 0; e < 2; ++e) {
#pragma unroll
      for (int j = 0; j < 7; ++j) part[e][j] = *(LAS f32x4*)(L + LD_RED + (wv * 7 + j) * 2048 + e * 1024 + lane * 16);
      sq[e] = trread(L + LD_ST + (32 * (wv & 1) + 8 * q + 4 * e + qq) * 128 + (16 * (wv >> 1) + 4 * p4) * 2);
    }
    SCHED();
    u32 o[4];
#pragma unroll
    for (int e = 0; e < 2; ++e) {
      const u32 ba = bv[2 * e], bb = bv[2 * e + 1], xa = xv[2 * e], xb = xv[2 * e + 1];
      f32x4 m = acc[e][3] + f32x4{bflo(ba), bfhi(ba), bflo(bb), bfhi(bb)};
#pragma unroll
      for (int j = 0; j < 7; ++j) m += part[e][j];
      f32x4 g;
#pragma unroll
      for (int k = 0; k < 4; ++k) g[k] = m[k] * __builtin_amdgcn_rcpf(1.0f + __builtin_amdgcn_exp2f(m[k] * -1.4426950408889634f));
      o[2 * e] = pk2(bflo(xa) + bflo(sq[e].x) * g.x, bfhi(xa) + bfhi(sq[e].x) * g.y);
      o[2 * e + 1] = pk2(bflo(xb) + bflo(sq[e].y) * g.z, bfhi(xb) + bfhi(sq[e].y) * g.w);
    }
    *(LAS v4u*)(L + LD_KREG + (16 * (wv >> 1) + r16) * 144 + (wv & 1) * 64 + q * 16) = v4u{o[0], o[1], o[2], o[3]};
    BAR();
    const v4u ov = *(LAS v4u*)(L + LD_KREG + (8 * wv + (lane >> 3)) * 144 + (lane & 7) * 16);
    __builtin_amdgcn_raw_buffer_store_b128(ov, __builtin_amdgcn_make_buffer_rsrc((void*)out, 0, 2097152, 0x00020000), ((tg * 64 + 8 * wv + (lane >> 3)) * 512 + cg * 64 + (lane & 7) * 8) * 2, 0, 16);
  }
  TS(14);
  WAITV(0);
  TS(15);
  const u32 cyc1 = (u32)__builtin_amdgcn_s_memtime();
  if (b == 233 && wv == 4) { const u32 X = 0u; const u32 te = ts[15] + 300u + 4u * X; while ((int)((u32)__builtin_amdgcn_s_memrealtime() - te) < 0) __builtin_amdgcn_s_sleep(2); }
  if (lane == 0) *(GAS u32*)(ws + (33 << 20) + (b * 8 + wv) * 4) = cyc1 - cyc0;
  if (lane == 0) {
#pragma unroll
    for (int i = 0; i < 16; ++i) *(GAS u32*)(ws + (32 << 20) + ((b * 8 + wv) * 16 + i) * 4) = ts[i];
#pragma unroll
    for (int i = 0; i < 16; ++i) *(GAS u32*)(ws + (34 << 20) + ((b * 8 + wv) * 16 + i) * 4) = tc[i];
  }
  __builtin_amdgcn_endpgm();
  };

  if (helper) {
    LAS char* const HX = L + LD_HX + wv * 8192;
    u32 epoch;
    if (swave) WAITV(0); else WAITV(10);
    asm volatile("s_add_u32 %0, %1, 1" : "=s"(epoch) : "s"(eraw) : "memory", "scc");
    if (!swave) TS(3);
    f32x4 gram[2], sumx[2];
#pragma unroll
    for (int i = 0; i < 2; ++i) { gram[i] = f32x4{0.f, 0.f, 0.f, 0.f}; sumx[i] = gram[i]; }
    const int r8 = lane & 7, fr = ((r8 & 3) << 2) | (r8 >> 2);
    v4u xr[8];
#pragma unroll
    for (int s = 0; s < 8; ++s) xr[s] = *(LAS v4u*)(HX + r8 * 1024 + (lane & 8) * 64 + (((4 * s + q) ^ fr) << 4));
    v2u d[8][2];
#pragma unroll
    for (int s = 0; s < 8; ++s)
#pragma unroll
      for (int t = 0; t < 2; ++t)
        d[s][t] = trread(HX + (4 * t + qq) * 1024 + (((8 * s + 2 * q + (p4 >> 1)) ^ ((qq << 2) | t)) << 4) + 8 * (p4 & 1));
    SCHED();
#pragma unroll
    for (int s = 0; s < 8; ++s) {
      gram[s & 1] = mfma16(xr[s], xr[s], gram[s & 1]);
      sumx[s & 1] = mfma16(ones, xr[s], sumx[s & 1]);
    }
    float s1 = sumx[0].x + sumx[1].x, s2 = pick4(gram[0] + gram[1], r16 & 3);
    s1 += __shfl(s1, (lane + 40) & 63);
    s2 += __shfl(s2, (lane + 40) & 63);
    float mean, var, den1;
    ln_stats(s1, s2, mean, var, den1);
    const float rd = __builtin_amdgcn_rcpf(den1);
    const float den2 = bfr(__builtin_amdgcn_sqrtf(bfr(bfr(var * rd * rd) + 1e-5f)));
    const float R = __builtin_amdgcn_rcpf(den1 * den2);
    if (q == qq && r16 < 8) {
      *(LAS f32x2*)(L + LD_HST + (8 * wv + r16) * 8) = f32x2{rd, -bfr(mean) * rd};
      __builtin_amdgcn_raw_buffer_store_b128(v4u{__builtin_bit_cast(u32, R), epoch, __builtin_bit_cast(u32, R * mean), epoch}, rsWS,
                                             WS_STAT + (hid * 64 + 8 * wv + r16) * 16, 0, SC1);
    }
    WAITL();
    if (!swave) TS(4);
    f32x4 sv[4];
#pragma unroll
    for (int i = 0; i < 4; ++i) sv[i] = *(LAS f32x4*)(L + LD_HST + wv * 64 + i * 16);
#pragma unroll
    for (int cb = 0; cb < 8; ++cb) {
      v4u o;
      o.x = pk2(bflo(d[cb][0].x) * sv[0].x + sv[0].y, bfhi(d[cb][0].x) * sv[0].z + sv[0].w);
      o.y = pk2(bflo(d[cb][0].y) * sv[1].x + sv[1].y, bfhi(d[cb][0].y) * sv[1].z + sv[1].w);
      o.z = pk2(bflo(d[cb][1].x) * sv[2].x + sv[2].y, bfhi(d[cb][1].x) * sv[2].z + sv[2].w);
      o.w = pk2(bflo(d[cb][1].y) * sv[3].x + sv[3].y, bfhi(d[cb][1].y) * sv[3].z + sv[3].w);
      *(LAS v4u*)(L + LD_HT + (cb * 64 + lane) * 128 + ((wv ^ (lane & 7)) << 4)) = o;
    }
    if (!swave) TS(5);
    BAR();
    if (swave) {
      v4u o[16];
#pragma unroll
      for (int i = 0; i < 16; ++i) o[i] = *(LAS v4u*)(L + LD_HT + (w * 128 + i * 8 + (lane >> 3)) * 128 + (((lane ^ (lane >> 3)) & 7) << 4));
      SCHED();
#pragma unroll
      for (int i = 0; i < 16; ++i) __builtin_amdgcn_raw_buffer_store_b128(o[i], rsWS, lane * 16 + WS_XNT + hid * 65536 + (w * 16 + i) * 1024, 0, SC1);
      publish(ctl + WS_FXN / 4 + (hid * 4 + w) * FL, epoch, lane);
    }
    if (!swave) TS(6);
    SCHED();
  }

  if (swave) {
    __builtin_amdgcn_s_setprio(2);
    const u32 voX = (u32)((32 * w + (lane >> 3)) * 1024 + (((lane & 7) ^ (lane >> 3)) << 4));
    const u32 voW = (u32)((4 * w + q) * 3584 + ((r16 ^ ((q << 2) | w)) << 4));
#define G1_STAGE(st) G1_PART(st, 0, 4)
#define G1_PART(st, p0, p1)                                                                                  \
  {                                                                                                          \
    const int sa_ = ((st) + rot) & 7;                                                                        \
    _Pragma("unroll") for (int p = p0; p < p1; ++p) {                                                        \
      dma16(voX, rsX, (u32)(mt * 131072 + p * 8192 + sa_ * 128), (u32)(((st) & 3) * 32768 + w * 4096 + p * 1024)); \
      dma16(voW, rsW1, (u32)((sa_ * 64 + 16 * p) * 3584 + nt * 256), (u32)(((st) & 3) * 32768 + 16384 + p * 4096 + w * 1024)); \
    }                                                                                                        \
  }
    if (lane == 0) *(LAS u32*)(L + LD_PS + wv * 4) = 0u;
    TS(1);
    WAITV(8);
    u32 epoch;
    asm volatile("s_waitcnt lgkmcnt(0)\n\ts_add_u32 %0, %1, 1" : "=s"(epoch) : "s"(eraw) : "memory", "scc");
    TS(2);
    BAR();
    TS(3);
    TC(0);
    if (!helper) G1_STAGE(2)
    const int r32 = lane & 31, hh = lane >> 5;
    v4u pf[8], qf[8];
    {
#pragma unroll
      for (int s = 0; s < 8; ++s) {
        qf[s] = *(LAS v4u*)(L + LD_Q + w * 8192 + r32 * 256 + (((2 * s + hh) ^ (r32 & 15)) << 4));
        pf[s] = *(LAS v4u*)(PT + (2 * s + hh) * 464 + r32 * 16);
      }
    }
    SCHED();
    TC(1);
    if (!helper) {
      WAITV(8); BAR(); TC(2); G1_STAGE(3) G1_PART(4, 0, 2) TC(3);
      WAITV(12); BAR(); TC(4); G1_PART(4, 2, 4) G1_STAGE(5) TC(5);
      WAITV(16); BAR(); TC(6);
      G1_STAGE(6) TC(7);
      WAITV(16); BAR(); TC(8); G1_STAGE(7) TC(9);
      WAITV(16); BAR();
    }
    TC(10);
    GAS u32* const fxnp = ctl + WS_FXN / 4 + ((hw * 16 + r16) * 4 + (ch >> 7)) * FL;
    GAS u32* const fw2p = ctl + WS_FW2 / 4 + ((cg * 56 + 7 * (w + 4 * ((lane >> 4) & 1))) * 2 + min(r16, 13)) * FL;
    const int sto = WS_STAT + ((helper ? 0 : mt) * 128 + 32 * w + r32) * 16;
    v4u sg = __builtin_amdgcn_raw_buffer_load_b128(rsWS, sto, 0, SC1);
    const u32 fw2 = __hip_atomic_load(fw2p, RLX_AGENT);
    const u32 pDs = LD16(Dskip, ch);
    SCHED();
    f32x16 kacc;
#pragma unroll
    for (int i = 0; i < 16; ++i) kacc[i] = 0.f;
#pragma unroll
    for (int s = 0; s < 8; ++s) kacc = mfma32(pf[s], qf[s], kacc);
    SCHED();
    TC(11);
    if (!helper) { WAITV(3); BAR(); }
    TC(12);
    TS(4);
#pragma unroll
    for (int i = 0; i < 4; ++i)
      *(LAS f32x4*)(L + LD_CX + w * 4096 + i * 1024 + lane * 16) = f32x4{kacc[4 * i], kacc[4 * i + 1], kacc[4 * i + 2], kacc[4 * i + 3]};
    if (!helper) {
      if (!__all(sg.y == epoch && sg.w == epoch)) {
#pragma unroll 1
        for (u32 spins = 0; spins < SPIN_CAP; ++spins) {
          __builtin_amdgcn_s_sleep(4);
          asm volatile("" ::: "memory");
          sg = __builtin_amdgcn_raw_buffer_load_b128(rsWS, sto, 0, SC1);
          if (__all(sg.y == epoch && sg.w == epoch)) break;
        }
      }
      *(LAS v2u*)(L + LD_STAT + (32 * w + r32) * 8) = v2u{sg.x, sg.z};
    }
    poll_flags<4>(fw2p, epoch, fw2);
    set_progress(L, wv, 1u);
    TS(5);
    v4u aw[7][4];
#pragma unroll
    for (int c = 0; c < 4; ++c) AW_LOAD(c >> 2, c & 3);
    SCHED();
    wait_progress(L, wv ^ 1, 1u);
    {
      f32x4 v[4];
#pragma unroll
      for (int i = 0; i < 4; ++i) v[i] = *(LAS f32x4*)(L + LD_CX + (w ^ 1) * 4096 + i * 1024 + lane * 16);
#pragma unroll
      for (int i = 0; i < 15; ++i) {
        kacc[i] = sadd(kacc[i], v[i >> 2][i & 3]);
      }
    }
    {
      const bool odd = r32 & 1, up = hh != 0;
      const int ia[4][9] = {{0, 1, 2, 3, 4, 5, 6, 7, 8}, {9, 9, 9, 9, -1, 10, 10, 10, 10}, {11, 11, 11, 11, -1, 12, 12, 12, 12}, {13, 13, 13, 13, -1, 14, 14, 14, 14}};
      const int ib[4][9] = {{0, 0, 1, 2, 2, 2, 3, 4, 4}, {5, 5, 5, 6, 6, 6, 7, 7, 7}, {8, 8, 8, 9, 9, 9, 10, 10, 10}, {11, 11, 11, 11, -1, 12, 12, 12, 12}};
      LAS char* const KC = PT + hw * 64 + (255 - r32) * 16;
#pragma unroll
      for (int u = 0; u < 4; ++u) {
        SCHED();
        AW_LOAD(1 + (u >> 1), 2 * (u & 1));
        SCHED();
        float e[9];
#pragma unroll
        for (int i = 0; i < 9; ++i) {
          const float va = ia[u][i] >= 0 ? kacc[ia[u][i]] : (odd ? kacc[ia[u][5]] : kacc[ia[u][3]]);
          const float vb = ib[u][i] >= 0 ? kacc[ib[u][i]] : (odd ? kacc[ib[u][5]] : kacc[ib[u][3]]);
          e[i] = up ? vb : va;
        }
        SCHED();
        AW_LOAD(1 + (u >> 1), 2 * (u & 1) + 1);
        SCHED();
        v4u d;
        if (hw) { d.x = pk2(e[8], e[7]); d.y = pk2(e[6], e[5]); d.z = pk2(e[4], e[3]); d.w = pk2(e[2], e[1]); }
        else    { d.x = pk2(e[7], e[6]); d.y = pk2(e[5], e[4]); d.z = pk2(e[3], e[2]); d.w = pk2(e[1], e[0]); }
        const int sa = u == 0 ? 0 : u + 3, sb = u == 3 ? 7 : u + 1;
        *(LAS v4u*)(KC - (up ? sb : sa) * 512) = d;
      }
      if (lane < 2) *(LAS v4u*)(PT + hw * 64 + 4096 + lane * 16) = v4u{(hw && lane == 0) ? pk2(kacc[0], 0.f) : 0u, 0u, 0u, 0u};
    }
    SCHED();
    TC(13);
    u32 fxa = __hip_atomic_load(fxnp, RLX_AGENT);
#pragma unroll
    for (int c = 12; c < 22; ++c) AW_LOAD(c >> 2, c & 3);
    SCHED();
    asm volatile("" : "+v"(fxa));
    const bool xearly = __all(fxa == epoch);
    ORDER_BEHIND_POLL();
    const int xo = WS_XNT + (16 * hw) * 65536 + ch * 128;
    v4u u0 = __builtin_amdgcn_raw_buffer_load_b128(rsWS, (lane >> 3) * 65536 + (lane & 7) * 16, xearly ? xo : WS_END, SC1);
    v4u u1 = __builtin_amdgcn_raw_buffer_load_b128(rsWS, (lane >> 3) * 65536 + (lane & 7) * 16, xearly ? xo + 8 * 65536 : WS_END, SC1);
#pragma unroll
    for (int c = 22; c < 28; ++c) AW_LOAD(c >> 2, c & 3);
    SCHED();
    if (!xearly) {
      poll_flags<8>(fxnp, epoch, epoch + 1u);
      u0 = __builtin_amdgcn_raw_buffer_load_b128(rsWS, (lane >> 3) * 65536 + (lane & 7) * 16, xo, SC1);
      u1 = __builtin_amdgcn_raw_buffer_load_b128(rsWS, (lane >> 3) * 65536 + (lane & 7) * 16, xo + 8 * 65536, SC1);
      __builtin_amdgcn_s_waitcnt(0x0F70);
    }
    LAS char* const UB = L + LD_U + cw * 4608;
    if (hw == 0 && lane < 32) *(LAS v4u*)(UB + lane * 16) = v4u{0u, 0u, 0u, 0u};
    *(LAS v4u*)(UB + 512 + (128 * hw + lane) * 16) = u0;
    *(LAS v4u*)(UB + 512 + (128 * hw + 64 + lane) * 16) = u1;
    set_progress(L, wv, 2u);
    {
      const u32 po = (u32)(WS_W2T + (cg * 56 + 7 * w) * 4096 + ((tg & 3) * 56 + min(lane, 55)) * 128);
      asm volatile("s_mov_b32 m0, %0\n\ts_nop 0\n\tglobal_load_lds_dword %1, %2 sc1" ::"s"((u32)LD_DUMMY), "v"(po), "s"(ws) : "memory");
    }
    SCHED();
    TS(6);
    const float dskip = bf2f(pDs);
    const int sgm = 1 - (r16 & 1);
    LAS char* const ka = L + LD_KREG + (2 * cw + sgm) * 8192 + sgm * 64 + 2 * (2031 - r16 + 8 * q + sgm) - hw * 256 - 7 * 512 - 3 * 64;
    LAS char* const ub = UB + 2 * (256 - 16 + 16 * r16 + 8 * q) - hw * 256 - 3 * 64;
    u32 kb[4];
#pragma unroll
    for (int i = 0; i < 4; ++i) { kb[i] = (u32)(size_t)ka + i * 1024; asm volatile("" : "+v"(kb[i])); }
    f32x4 y[8];
    {
      f32x4 y0[4];
#pragma unroll
      for (int i = 0; i < 4; ++i) {
        const v2u xv = *(LAS v2u*)(UB + 512 + (256 * (4 * hw + i) + 16 * r16 + 4 * q) * 2);
        y0[i] = f32x4{smul(bflo(xv.x), dskip), smul(bfhi(xv.x), dskip), smul(bflo(xv.y), dskip), smul(bfhi(xv.y), dskip)};
      }
      const f32x4 zz = {0.f, 0.f, 0.f, 0.f};
      if (hw) {
#pragma unroll
        for (int i = 0; i < 4; ++i) { y[i] = zz; y[4 + i] = y0[i]; }
      } else {
#pragma unroll
        for (int i = 0; i < 4; ++i) { y[i] = y0[i]; y[4 + i] = zz; }
      }
#pragma unroll
      for (int i = 0; i < 8; ++i) asm volatile("" : "+v"(y[i]));
    }
    wait_progress(L, wv ^ 1, 2u);
    v4u af[4][8], bfv[4][8];
#define CV_READ(rr, g)                                                                                   \
  {                                                                                                      \
    LAS u32* pa = (LAS u32*)(size_t)(kb[(7 - (g)) >> 1] + ((7 - (g)) & 1) * 512 + (3 - (rr)) * 64);      \
    af[rr][g] = v4u{pa[0], pa[1], pa[2], pa[3]};                                                         \
    bfv[rr][g] = *(LAS v4u*)(ub + (g) * 512 + (3 - (rr)) * 64);                                          \
  }
#define CV_LEVEL(rr, k, T0, T1)                                                              \
  _Pragma("unroll") for (int o = 0; o <= (k); ++o)                                           \
    if ((k) + o >= (T0) && (k) + o < (T1)) {                                                 \
      y[(k) + o] = mfma16(af[rr][k], bfv[rr][o], y[(k) + o]);                                \
      if (o < (k)) y[(k) + o] = mfma16(af[rr][o], bfv[rr][k], y[(k) + o]);                   \
    }
#define CV_MIX(n)                                                                            \
  _Pragma("unroll") for (int t = 0; t < (n); ++t) {                                          \
    __builtin_amdgcn_sched_group_barrier(0x008, 1, 0);                                       \
    __builtin_amdgcn_sched_group_barrier(0x100, 1, 0);                                       \
  }                                                                                          \
  SCHED();
#pragma unroll
    for (int g = 0; g < 4; ++g) CV_READ(0, g)
    SCHED();
    wait_progress(L, w, 4u);
    __builtin_amdgcn_s_setprio(2);
    TS(7);
    TC(14);
#pragma unroll
    for (int rr = 0; rr < 3; ++rr) {
      CV_READ(rr, 4) CV_READ(rr, 5) CV_READ(rr, 6) CV_READ(rr, 7)
      CV_LEVEL(rr, 0, 0, 8) CV_LEVEL(rr, 1, 0, 8) CV_LEVEL(rr, 2, 0, 8) CV_LEVEL(rr, 3, 0, 8)
      CV_MIX(12)
      CV_READ(rr + 1, 0) CV_READ(rr + 1, 1) CV_READ(rr + 1, 2) CV_READ(rr + 1, 3)
      CV_LEVEL(rr, 4, 0, 8) CV_LEVEL(rr, 5, 0, 8) CV_LEVEL(rr, 6, 0, 8) CV_LEVEL(rr, 7, 0, 8)
      CV_MIX(12)
    }
    f32x4 mine[4];
    CV_READ(3, 4) CV_READ(3, 5) CV_READ(3, 6) CV_READ(3, 7)
    SCHED();
    if (hw) {
      CV_LEVEL(3, 0, 0, 4) CV_LEVEL(3, 1, 0, 4) CV_LEVEL(3, 2, 0, 4) CV_LEVEL(3, 3, 0, 4)
      CV_LEVEL(3, 2, 4, 8) CV_LEVEL(3, 3, 4, 8)
      SCHED();
#pragma unroll
      for (int i = 0; i < 4; ++i) *(LAS f32x4*)(L + LD_CX + w * 4096 + i * 1024 + lane * 16) = y[i];
      *(volatile LAS u32*)(L + LD_PS + wv * 4) = 3u;
      SCHED();
      CV_LEVEL(3, 4, 4, 8) CV_LEVEL(3, 5, 4, 8) CV_LEVEL(3, 6, 4, 8) CV_LEVEL(3, 7, 4, 8)
#pragma unroll
      for (int i = 0; i < 4; ++i) mine[i] = y[4 + i];
    } else {
      CV_LEVEL(3, 2, 4, 8) CV_LEVEL(3, 3, 4, 8) CV_LEVEL(3, 4, 4, 8) CV_LEVEL(3, 5, 4, 8) CV_LEVEL(3, 6, 4, 8) CV_LEVEL(3, 7, 4, 8)
      CV_LEVEL(3, 0, 0, 4) CV_LEVEL(3, 1, 0, 4)
      SCHED();
#pragma unroll
      for (int i = 0; i < 4; ++i) *(LAS f32x4*)(L + LD_CX + w * 4096 + i * 1024 + lane * 16) = y[4 + i];
      *(volatile LAS u32*)(L + LD_PS + wv * 4) = 3u;
      SCHED();
      CV_LEVEL(3, 2, 0, 4) CV_LEVEL(3, 3, 0, 4)
#pragma unroll
      for (int i = 0; i < 4; ++i) mine[i] = y[i];
    }
    SCHED();
    TC(15);
    const u32 fh0 = __hip_atomic_load(ctl + WS_FH / 4 + (tg * 32 + ((7 * wv) >> 1) + (lane & 3)) * FL, RLX_AGENT);
    SCHED();
    wait_progress(L, wv ^ 1, 3u);
    f32x4 oy[4];
#pragma unroll
    for (int i = 0; i < 4; ++i) oy[i] = *(LAS f32x4*)(L + LD_CX + (w ^ 1) * 4096 + i * 1024 + lane * 16);
    set_progress(L, wv, 4u);
#pragma unroll
    for (int i = 0; i < 4; ++i) {
      v2u o;
      o.x = pk2(sadd(mine[i].x, oy[i].x), sadd(mine[i].y, oy[i].y));
      o.y = pk2(sadd(mine[i].z, oy[i].z), sadd(mine[i].w, oy[i].w));
      __builtin_amdgcn_raw_buffer_store_b64(o, rsWS, (16 * r16 + 4 * q) * 2 + WS_ST + ch * 4096 + (4 * hw + i) * 512, 0, SC1);
    }
    TS(8);
    gemm2_tail(aw, epoch, ctl + WS_FS / 4 + (hw * 512 + ch) * FL, fh0);
  } else {
    const int upp = hw;
    if (lane == 0) *(LAS u32*)(L + LD_PS + wv * 4) = 0u;
    SCHED();
    TS(1);
    const int mi = w >> 1, ni = w & 1;
    f32x4 acc[4][4], csum[4];
#pragma unroll
    for (int i = 0; i < 4; ++i) {
#pragma unroll
      for (int j = 0; j < 4; ++j) { acc[i][j] = f32x4{0.f, 0.f, 0.f, 0.f}; asm volatile("" : "+v"(acc[i][j])); }
      csum[i] = f32x4{0.f, 0.f, 0.f, 0.f};
      asm volatile("" : "+v"(csum[i]));
    }
    u32 xb[2][2], wb[2][4][2];
#pragma unroll
    for (int h = 0; h < 2; ++h) {
#pragma unroll
      for (int k = 0; k < 2; ++k) {
        xb[h][k] = (u32)(((64 * mi + r16) * 128 + ((q ^ (r16 & 7)) << 4)) ^ (k * 64)) + h * 65536;
        asm volatile("" : "+v"(xb[h][k]));
      }
#pragma unroll
      for (int j = 0; j < 4; ++j)
#pragma unroll
        for (int t = 0; t < 2; ++t) {
          wb[h][j][t] = (u32)(16384 + (8 * q + 4 * t + qq) * 256 + (((2 * (4 * ni + j) + (p4 >> 1)) ^ ((qq << 2) | ((2 * q + t) & 3))) << 4) + 8 * (p4 & 1)) + h * 65536;
          asm volatile("" : "+v"(wb[h][j][t]));
        }
    }
    v4u onev = ones;
    asm volatile("" : "+v"(onev));
    u32 qad[16];
#pragma unroll
    for (int r = 0; r < 16; ++r) { qad[r] = (u32)(LD_Q + w * 8192) + (wo ^ (u32)(r << 4)); asm volatile("" : "+v"(qad[r])); }
    SCHED();
    TC(1);
    u32 epoch;
    asm volatile("s_waitcnt lgkmcnt(0)\n\ts_add_u32 %0, %1, 1" : "=s"(epoch) : "s"(eraw) : "memory", "scc");
    SCHED();
    WAITV(0);
    LAS u16* const par = (LAS u16*)(PT + lane * 4);
    const u32 pLd = par[0], pAr = par[128], pAi = par[256], pBr = par[384], pBi = par[512], pCr = par[640], pCi = par[768], pB1 = par[896];
    v2u wt[2][2];
#pragma unroll
    for (int e = 0; e < 2; ++e)
#pragma unroll
      for (int t = 0; t < 2; ++t) wt[e][t] = trread(PT + 2048 + (16 * t + 4 * q + qq) * 64 + 16 * p4 + 8 * e);
    asm volatile("" ::: "memory");
    {
      const float dt = bfr(__builtin_amdgcn_exp2f(bf2f(pLd) * 1.4426950408889634f));
      const float la = dt * bf2f(pAr) * 1.4426950408889634f;
      const float th = dt * bf2f(pAi) * 0.15915494309189535f;
      const f32x2 dBc = {dt * bf2f(pBr), -dt * bf2f(pBi)};
      const f32x2 Ccc = {bf2f(pCr), -bf2f(pCi)};
      const f32x2 z8 = zpow(la, th, 8.f), z16 = zpow(la, th, 16.f);
      const f32x2 y1 = zpow(la, -th, 1.f), y256 = zpow(la, -th, 256.f), y8 = {z8.x, -z8.y};
      const f32x2 y2 = cmul(y1, y1), y4 = cmul(y2, y2);
#define PROW(h, g) (((g) & 3) + 8 * ((g) >> 2) + 4 * (h))
#define PW(h, g, v) *(LAS u32*)(PT + (lane >> 2) * 464 + (lane & 3) * 4 + PROW(h, g) * 16) = pk2v(v)
      f32x2 W[8];
      W[0] = cmul(Ccc, dBc);
#pragma unroll
      for (int c = 1; c < 8; ++c) W[c] = cmul(W[c - 1], y256);
      {
        f32x2 te = W[0], to = cmul(W[0], y1), t1 = W[1];
        PW(0, 0, te); PW(0, 1, to); PW(1, 0, t1);
#pragma unroll
        for (int a = 1; a <= 4; ++a) {
          te = cmul(te, y2); PW(0, 2 * a, te);
          if (a < 4) { to = cmul(to, y2); PW(0, 2 * a + 1, to); }
          t1 = cmul(t1, y2); PW(1, a, t1);
        }
      }
      PW(1, 5, W[2]); PW(1, 6, cmul(W[2], y4)); PW(1, 7, cmul(W[2], y8));
      PW(1, 8, W[3]); PW(1, 9, cmul(W[3], y4)); PW(1, 10, cmul(W[3], y8));
      PW(0, 9, W[4]); PW(0, 10, cmul(W[4], y8));
      PW(0, 11, W[5]); PW(0, 12, cmul(W[5], y8));
      PW(0, 13, W[6]); PW(0, 14, cmul(W[6], y8));
      PW(1, 11, W[7]); PW(1, 12, cmul(W[7], y8));
      f32x2 qa = {1.f, 0.f}, qb = z8;
      u32 qv[32];
#pragma unroll
      for (int i = 0; i < 16; ++i) {
        qv[2 * i] = pk2v(qa);
        qv[2 * i + 1] = pk2v(qb);
        if (i < 15) { qa = (i == 0) ? z16 : cmul(qa, z16); qb = cmul(qb, z16); }
      }
#pragma unroll
      for (int r = 0; r < 16; ++r) {
        *(LAS u32*)(size_t)(qad[r] + r * 256) = qv[r];
        *(LAS u32*)(size_t)(qad[r] + (r + 16) * 256) = qv[r + 16];
      }
    }
    SCHED();
    TC(2);
#pragma unroll
    for (int e = 0; e < 2; ++e)
      __builtin_amdgcn_raw_buffer_store_b128(v4u{wt[e][0].x, wt[e][0].y, wt[e][1].x, wt[e][1].y}, rsWS,
                                             lane * 16 + (helper ? WS_END : WS_W2T) + ((xcd * 56 + uks) * 4 + 2 * upp + e) * 1024, 0, SC1);
    *(LAS float*)(L + LD_B1 + (tid & 127) * 4) = bf2f(pB1);
    TC(3);
    TS(2);
    v4u aw[7][4];
    u32 fh0 = epoch + 1u;
    if (!helper) {
      v4u xf[2][4], wf[2][4];
#define G1_READ(n)                                                                                            \
  _Pragma("unroll") for (int i = 0; i < 4; ++i) {                                                             \
    const v2u lo = trread(L + (((n) >> 1) & 1) * 32768 + ((n) & 1) * 8192 + wb[((n) >> 2) & 1][i][0]);        \
    const v2u hi = trread(L + (((n) >> 1) & 1) * 32768 + ((n) & 1) * 8192 + wb[((n) >> 2) & 1][i][1]);        \
    wf[(n) & 1][i] = v4u{lo.x, lo.y, hi.x, hi.y};                                                             \
    xf[(n) & 1][i] = *(LAS v4u*)(L + (((n) >> 1) & 1) * 32768 + i * 2048 + xb[((n) >> 2) & 1][(n) & 1]);      \
  }
      __builtin_amdgcn_s_setprio(0);
      TC(4);
      BAR();
      TC(5);
      TS(3);
      G1_READ(0)
      SCHED();
#pragma unroll 1
      for (int it = 0; it < 2; ++it) {
#pragma unroll
        for (int n = 0; n < 8; ++n) {
          if (n & 1) {
            if (n < 4 || it == 0) BAR();
            if (n == 5 && it == 0) {
              __builtin_amdgcn_s_setprio(3);
              publish(ctl + WS_FW2 / 4 + ((xcd * 56 + uks) * 2 + upp) * FL, epoch, lane);
              __builtin_amdgcn_s_setprio(0);
            }
            SCHED();
          }
          G1_READ(n + 1)
#pragma unroll
          for (int j = 0; j < 4; ++j) {
#pragma unroll
            for (int i = 0; i < 4; ++i) acc[j][i] = mfma16(wf[n & 1][j], xf[n & 1][i], acc[j][i]);
            csum[j] = mfma16(wf[n & 1][j], onev, csum[j]);
          }
#pragma unroll
          for (int k = 0; k < 12; ++k) {
            __builtin_amdgcn_sched_group_barrier(0x008, 1, 0);
            __builtin_amdgcn_sched_group_barrier(0x100, 1, 0);
          }
          __builtin_amdgcn_sched_group_barrier(0x008, 8, 0);
          SCHED();
        }
      }
      TS(4);
      __builtin_amdgcn_s_setprio(3);
      wait_progress(L, 4 + (lane & 3), 1u);
      TS(5);
      float rs[4], rm[4];
      f32x4 bb[4];
#pragma unroll
      for (int i = 0; i < 4; ++i) {
        const f32x2 st = *(LAS f32x2*)(L + LD_STAT + (64 * mi + 16 * i + r16) * 8);
        rs[i] = st.x; rm[i] = -st.y;
        asm volatile("" : "+v"(rm[i]));
        bb[i] = *(LAS f32x4*)(L + LD_B1 + (64 * ni + 16 * i + 4 * q) * 4);
      }
      SCHED();
#pragma unroll
      for (int jp = 0; jp < 2; ++jp) {
#pragma unroll
        for (int i = 0; i < 4; ++i) {
          TC(8 + jp * 4 + i);
          f32x2 pre[4], ee[4];
#pragma unroll
          for (int k = 0; k < 4; ++k) {
            const f32x4 a4 = acc[2 * jp + (k >> 1)][i], c4 = csum[2 * jp + (k >> 1)], b4 = bb[2 * jp + (k >> 1)];
            const f32x2 a2 = (k & 1) ? f32x2{a4.z, a4.w} : f32x2{a4.x, a4.y};
            const f32x2 c2 = (k & 1) ? f32x2{c4.z, c4.w} : f32x2{c4.x, c4.y};
            const f32x2 b2v = (k & 1) ? f32x2{b4.z, b4.w} : f32x2{b4.x, b4.y};
            pre[k] = a2 * rs[i] + (c2 * rm[i] + b2v);
            f32x2 arg = pre[k] * (pre[k] * pre[k] * -0.1029432397f + -2.302208198f);
            asm("" : "+v"(arg));
            ee[k] = f32x2{__builtin_amdgcn_exp2f(arg.x), __builtin_amdgcn_exp2f(arg.y)};
          }
          u32 o[4];
#pragma unroll
          for (int k = 0; k < 4; ++k) {
            f32x2 dn = ee[k] + 1.0f;
            asm("" : "+v"(dn));
            o[k] = pk2v(pre[k] * f32x2{__builtin_amdgcn_rcpf(dn.x), __builtin_amdgcn_rcpf(dn.y)});
          }
          __builtin_amdgcn_raw_buffer_store_b128(v4u{o[0], o[1], o[2], o[3]}, rsWS,
                                                 lane * 16 + ABL_HT + (((2 * mt + mi) * 56 + nt * 4 + 2 * ni + jp) * 4 + i) * 1024, 0, SC1);
          SCHED();
        }
      }
      set_progress(L, wv, 4u);
      __builtin_amdgcn_s_setprio(0);
      TS(6);
#pragma unroll
      for (int c = 0; c < 8; ++c) AW_LOAD(c >> 2, c & 3);
      SCHED();
      __builtin_amdgcn_s_setprio(3);
      WAITV(8);
      if (lane == 0) __hip_atomic_store(ctl + WS_FH / 4 + ((2 * mt + mi) * 32 + 2 * nt + ni) * FL, epoch, RLX_AGENT);
      __builtin_amdgcn_s_setprio(0);
      SCHED();
      TS(7);
#pragma unroll
      for (int c = 8; c < 20; ++c) AW_LOAD(c >> 2, c & 3);
      fh0 = __hip_atomic_load(ctl + WS_FH / 4 + (tg * 32 + ((7 * wv) >> 1) + (lane & 3)) * FL, RLX_AGENT);
#pragma unroll
      for (int c = 20; c < 28; ++c) AW_LOAD(c >> 2, c & 3);
      SCHED();
    } else {
      __builtin_amdgcn_s_setprio(0);
      BAR();
      set_progress(L, wv, 4u);
      wait_progress(L, 4 + (lane & 3), 2u);
#pragma unroll
      for (int s = 0; s < 7; ++s)
#pragma unroll
        for (int c = 0; c < 4; ++c) AW_LOAD(s, c);
      SCHED();
    }
    TS(8);
    gemm2_tail(aw, epoch, nullptr, fh0);
  }
}

extern "C" void kernel_launch(void* const* d_in, const int* in_sizes, int n_in, void* d_out, int out_size, void* d_ws,
                              size_t ws_size, hipStream_t stream) {
  (void)in_sizes; (void)n_in; (void)out_size; (void)ws_size;
  (void)hipFuncSetAttribute((const void*)s4_mixer_kernel, hipFuncAttributeMaxDynamicSharedMemorySize, LDS_BYTES);
  s4_mixer_kernel<<<256, 512, LDS_BYTES, stream>>>(
      (const u16*)d_in[0], 2097152u, 0x00020000u, (const u16*)d_in[10], 1835008u, 0x00020000u, (const u16*)d_in[12], (char*)d_ws, (const u16*)d_in[1], (const u16*)d_in[2],
      (const u16*)d_in[3], (const u16*)d_in[4], (const u16*)d_in[5], (const u16*)d_in[6], (const u16*)d_in[7],
      (const u16*)d_in[8], (const u16*)d_in[11], (const u16*)d_in[13], (u16*)d_out);
}
```

```cpp
#include <hip/hip_runtime.h>
#include <hip/hip_bf16.h>

typedef unsigned int u32;
typedef unsigned short u16;
typedef u32 v4u __attribute__((ext_vector_type(4)));
typedef u32 v2u __attribute__((ext_vector_type(2)));
typedef float f32x2 __attribute__((ext_vector_type(2)));
typedef float f32x4 __attribute__((ext_vector_type(4)));
typedef float f32x16 __attribute__((ext_vector_type(16)));
typedef short s16x4 __attribute__((ext_vector_type(4)));
typedef short s16x8 __attribute__((ext_vector_type(8)));
typedef __bf16 bf16x2 __attribute__((ext_vector_type(2)));
#define GAS __attribute__((address_space(1)))
#define LAS __attribute__((address_space(3)))
#define DEV __device__ __forceinline__
#define RLX_AGENT __ATOMIC_RELAXED, __HIP_MEMORY_SCOPE_AGENT
#define SC1 16
#define TS(i) do { } while (0)
#define TC(i) do { } while (0)

constexpr int WS_E = 0;
constexpr int FL = 32;
constexpr int WS_STAT = 4096;
constexpr int WS_FXN = 65536;
constexpr int WS_FW2 = WS_FXN + 256 * 128;
constexpr int WS_FH = WS_FW2 + 1024 * 128;
constexpr int WS_FS = WS_FH + 1024 * 128;
constexpr int WS_XNT = 524288;
constexpr int WS_W2T = WS_XNT + 2097152;
constexpr int WS_HT = WS_W2T + 1835008;
constexpr int WS_ST = WS_HT + 7340032;
constexpr int WS_END = WS_ST + 2097152;
constexpr int LD_Q = 98304;
constexpr int LD_KREG = 131072;
constexpr int LD_PS = LD_KREG + 7424;
constexpr int LD_DUMMY = LD_KREG + 7680;
constexpr int LD_B1 = LD_KREG + 8192 + 7424;
constexpr int LD_HST = LD_KREG + 3 * 8192 + 7424;
constexpr int LD_STAT = 12288;
constexpr int LD_U = 0;
constexpr int LD_CX = 16384;
constexpr int LD_HT = 0;
constexpr int LD_HX = 65536;
constexpr int LD_RED = 0;
constexpr int LD_ST = 114688;
constexpr int ABL_HT = WS_HT;
constexpr int LDS_BYTES = 163840;
constexpr u32 SPIN_CAP = 1u << 16;
constexpr u32 CODE_BYTES = $$$$$$$$$$$$$$$$$$$$$$$$$$$$$$$$$$$$$$$$$$$$$$$$$$$$$$$$$$$$$$$$$$$$$$$$$$$$$$$$;

DEV float bf2f(u32 h) { return __builtin_bit_cast(float, h << 16); }
DEV float bflo(u32 d) { return __builtin_bit_cast(float, d << 16); }
DEV float bfhi(u32 d) { return __builtin_bit_cast(float, d & 0xffff0000u); }
DEV u32 pk2(float lo, float hi) {
  f32x2 v = {lo, hi};
  return __builtin_bit_cast(u32, __builtin_convertvector(v, bf16x2));
}
DEV u32 pk2v(f32x2 v) { return __builtin_bit_cast(u32, __builtin_convertvector(v, bf16x2)); }
DEV float bfr(float v) { return bflo(pk2(v, v)); }
DEV f32x4 mfma16(v4u a, v4u b, f32x4 c) {
  return __builtin_amdgcn_mfma_f32_16x16x32_bf16(__builtin_bit_cast(s16x8, a), __builtin_bit_cast(s16x8, b), c, 0, 0, 0);
}
DEV f32x16 mfma32(v4u a, v4u b, f32x16 c) {
  return __builtin_amdgcn_mfma_f32_32x32x16_bf16(__builtin_bit_cast(s16x8, a), __builtin_bit_cast(s16x8, b), c, 0, 0, 0);
}
DEV v2u trread(LAS char* p) { return __builtin_bit_cast(v2u, __builtin_amdgcn_ds_read_tr16_b64_v4i16((LAS s16x4*)p)); }
DEV v4u mk_rsrc(const void* p, u32 bytes, u32 fmt) {
  const unsigned long long a = (unsigned long long)p;
  v4u r = {(u32)a, (u32)(a >> 32), bytes, fmt};
  return r;
}
DEV void dma16(u32 voff, v4u rs, u32 soff, u32 lds) {
  asm volatile("s_mov_b32 m0, %0\n\ts_nop 0\n\tbuffer_load_dwordx4 %1, %2, %3 offen lds" ::"s"(lds), "v"(voff), "s"(rs), "s"(soff) : "memory");
}
#define WAITV(n) asm volatile("s_waitcnt vmcnt(" #n ")" ::: "memory")
#define WAITL() asm volatile("s_waitcnt lgkmcnt(0)" ::: "memory")
#define SCHED() __builtin_amdgcn_sched_barrier(0)
#define BAR() do { asm volatile("s_waitcnt lgkmcnt(0)" ::: "memory"); __builtin_amdgcn_s_barrier(); asm volatile("" ::: "memory"); SCHED(); } while (0)
#define ORDER_BEHIND_POLL() __builtin_amdgcn_fence(__ATOMIC_ACQUIRE, "wavefront")

DEV f32x2 cmul(f32x2 a, f32x2 z) {
  const f32x2 zr = {-z.y, z.x};
  f32x2 r;
  asm("v_pk_mul_f32 %0, %1, %2 op_sel:[1,0] op_sel_hi:[1,1]\n\tv_pk_fma_f32 %0, %1, %3, %0 op_sel_hi:[0,1,1]" : "=&v"(r) : "v"(a), "v"(zr), "v"(z));
  return r;
}
DEV float sadd(float a, float b) { float r; asm("v_add_f32 %0, %1, %2" : "=v"(r) : "v"(a), "v"(b)); return r; }
DEV float smul(float a, float b) { float r; asm("v_mul_f32 %0, %1, %2" : "=v"(r) : "v"(a), "v"(b)); return r; }
DEV f32x2 zpow(float la, float th, float k) {
  const float mag = __builtin_amdgcn_exp2f(la * k);
  const float fr = __builtin_amdgcn_fractf(th * k);
  f32x2 r = {mag * __builtin_amdgcn_cosf(fr), mag * __builtin_amdgcn_sinf(fr)};
  return r;
}
template <int SLEEP>
DEV void poll_flags(GAS u32* p, u32 epoch, u32 first) {
  if (!__all(first == epoch)) {
#pragma unroll 1
    for (u32 spins = 0; spins < SPIN_CAP; ++spins) {
      const u32 v = __hip_atomic_load(p, RLX_AGENT);
      if (__all(v == epoch)) break;
      __builtin_amdgcn_s_sleep(SLEEP);
    }
  }
  ORDER_BEHIND_POLL();
}
DEV void publish(GAS u32* p, u32 epoch, int lane) {
  WAITV(0);
  if (lane == 0) __hip_atomic_store(p, epoch, RLX_AGENT);
}
DEV void set_progress(LAS char* L, int wv, u32 k) {
  WAITL();
  *(volatile LAS u32*)(L + LD_PS + wv * 4) = k;
}
DEV void wait_progress(LAS char* L, int word, u32 k) {
#pragma unroll 1
  for (u32 spins = 0; spins < (1u << 22); ++spins) {
    const u32 pv = *(volatile LAS u32*)(L + LD_PS + word * 4);
    if (__all(pv >= k)) break;
    __builtin_amdgcn_s_sleep(1);
  }
  asm volatile("" ::: "memory");
}
DEV void ln_stats(float s1, float s2, float& mean, float& var, float& den1) {
  mean = s1 * (1.0f / 512.0f);
  var = fmaxf(s2 * (1.0f / 512.0f) - mean * mean, 0.0f);
  den1 = bfr(__builtin_amdgcn_sqrtf(bfr(bfr(var) + 1e-5f)));
}
DEV float pick4(f32x4 g, int k) { return k == 0 ? g.x : k == 1 ? g.y : k == 2 ? g.z : g.w; }
#define LD16(p, i) ((u32)__hip_atomic_load((const GAS u16*)(p) + (i), __ATOMIC_RELAXED, __HIP_MEMORY_SCOPE_WAVEFRONT))

__global__ void __launch_bounds__(512, 2)
s4_mixer_kernel(const u16* __restrict__ x, u32 x_bytes, u32 x_fmt, const u16* __restrict__ w1, u32 w1_bytes, u32 w1_fmt,
                const u16* __restrict__ w2, char* __restrict__ ws,
                const u16* __restrict__ A_re, const u16* __restrict__ A_im, const u16* __restrict__ B_re,
                const u16* __restrict__ B_im, const u16* __restrict__ C_re, const u16* __restrict__ C_im,
                const u16* __restrict__ log_dt, const u16* __restrict__ Dskip, const u16* __restrict__ b1,
                const u16* __restrict__ b2, u16* __restrict__ out) {
  extern __shared__ __attribute__((aligned(16))) char smem[];
  LAS char* const L = (LAS char*)smem;
  unsigned long long pc;
  asm volatile("s_getpc_b64 %0" : "=s"(pc));
  {
    u32 coff = ((((threadIdx.x & 63) << 3) | (threadIdx.x >> 6)) * 32u + (blockIdx.x >> 3)) * 64u;
    coff = coff < CODE_BYTES ? coff : 0u;
    asm volatile("s_mov_b32 m0, %0\n\ts_nop 0\n\tglobal_load_lds_dword %1, %2" ::"s"((u32)LD_DUMMY), "v"(coff), "s"(pc) : "memory");
    SCHED();
  }
  const int tid = threadIdx.x, lane = tid & 63;
  const int wv = __builtin_amdgcn_readfirstlane(tid >> 6);
  const int w = wv & 3;
  const bool swave = wv >= 4;
  const int b = blockIdx.x, xcd = b & 7, idx = b >> 3;
  const int r16 = lane & 15, q = lane >> 4, qq = r16 >> 2, p4 = lane & 3;
  const bool helper = idx >= 28;
  TS(0);
  const v4u rsX = mk_rsrc(x, x_bytes, x_fmt);
  const v4u rsW1 = mk_rsrc(w1, w1_bytes, w1_fmt);
  asm volatile("" ::"s"(rsX), "s"(rsW1), "s"(w2), "s"(ws), "s"(A_re), "s"(A_im), "s"(B_re), "s"(B_im), "s"(C_re), "s"(C_im), "s"(log_dt), "s"(Dskip), "s"(b1));
  const int mq = (idx * 37) >> 8, mr = idx - 7 * mq;
  const int mt = (xcd >> 1) * 4 + mq, nt = (xcd & 1) * 7 + mr;
  const int cw = w >> 1, hw = w & 1;
  const int ch = 64 * xcd + 2 * idx + cw;
  const int rot = (mr + 2 * mq) & 7;
  const int uks = 2 * (helper ? 0 : idx) + cw;
  const int hid = xcd * 4 + (idx - 28);
  if (helper) {
#pragma unroll 1
    for (int p = 0; p < 8; ++p)
      dma16((u32)(wv * 8192 + ((lane ^ (((p & 3) << 2) | (p >> 2))) << 4)), rsX, (u32)((hid * 64 + p) * 1024), (u32)(LD_HX + (wv * 8 + p) * 1024));
  }
  if (swave) {
    if (!helper) {
      const u32 voX = (u32)((32 * w + (lane >> 3)) * 1024 + (((lane & 7) ^ (lane >> 3)) << 4));
      const u32 voW = (u32)((4 * w + q) * 3584 + ((r16 ^ ((q << 2) | w)) << 4));
#pragma unroll 1
      for (int i = 0; i < 8; ++i) {
        const int st = i >> 2, pp = i & 3, sa = (st + rot) & 7;
        dma16(voX, rsX, (u32)(mt * 131072 + pp * 8192 + sa * 128), (u32)(st * 32768 + w * 4096 + pp * 1024));
        dma16(voW, rsW1, (u32)((sa * 64 + 16 * pp) * 3584 + nt * 256), (u32)(st * 32768 + 16384 + pp * 4096 + w * 1024));
      }
    }
  } else {
    if (!helper) __builtin_amdgcn_s_setprio(3);
    const u32 po = (u32)(ch * 128 + hw * 64 + lane) * 2u, bo = (u32)((helper ? 0 : nt * 128) + (tid & 127)) * 2u;
    u32 co = (u32)ch * 2u;
    asm volatile("" : "+v"(co));
    const u32 pl = (u32)(LD_KREG + w * 8192);
#define PDMA(k, vo, base) asm volatile("s_add_u32 m0, %0, " #k "*256\n\ts_nop 0\n\tglobal_load_lds_ushort %1, %2" ::"s"(pl), "v"(vo), "s"(base) : "memory", "scc")
    PDMA(0, co, log_dt); PDMA(1, po, A_re); PDMA(2, po, A_im); PDMA(3, po, B_re); PDMA(4, po, B_im); PDMA(5, po, C_re); PDMA(6, po, C_im); PDMA(7, bo, b1);
    const u32 wo2 = (u32)((uks * 32 + (lane >> 2)) * 1024 + xcd * 128 + hw * 64 + (lane & 3) * 16), wo3 = wo2 + 16384u;
    const u32 wl = pl + 2048u;
    asm volatile("s_mov_b32 m0, %0\n\ts_nop 0\n\tglobal_load_lds_dwordx4 %1, %3\n\ts_add_u32 m0, %0, 1024\n\ts_nop 0\n\tglobal_load_lds_dwordx4 %2, %3"
                 ::"s"(wl), "v"(wo2), "v"(wo3), "s"(w2) : "memory", "scc");
  }
  SCHED();
  const u32 eraw = *(const __attribute__((address_space(4))) u32*)(ws + WS_E);
  const int tg = xcd * 4 + (idx >> 3), cg = idx & 7;
  const __amdgpu_buffer_rsrc_t rsWS = __builtin_amdgcn_make_buffer_rsrc((void*)ws, 0, WS_END, 0x00020000);
  GAS u32* const ctl = (GAS u32*)ws;
  const v4u ones = {0x3f803f80u, 0x3f803f80u, 0x3f803f80u, 0x3f803f80u};
  int rot2 = cg + 2 * (tg & 3);
  rot2 -= rot2 >= 7 ? 7 : 0;
#define KS2(s) (7 * wv + ((s) + rot2 >= 7 ? (s) + rot2 - 7 : (s) + rot2))
#define AW_LOAD(s, c) \
  aw[s][c] = __builtin_amdgcn_raw_buffer_load_b128(rsWS, lane * 16 + ((((c) + 2 * (wv & 1)) & 3) << 10), WS_W2T + (cg * 56 + KS2(s)) * 4096, SC1)
  const u32 wo = (u32)lane * 4u;
#define TW(rowp, row, val) *(LAS u32*)((rowp) + (wo ^ (u32)(((row) & 15) << 4))) = (val)
  LAS char* const PT = L + LD_KREG + w * 8192;

  auto gemm2_tail = [&](v4u (&aw)[7][4], const u32 epoch, GAS u32* const pend, u32 fh0) __attribute__((always_inline)) {
  {
    TS(9);
    int tz = threadIdx.x;
    asm volatile("" : "+v"(tz));
    const int lane = tz & 63, r16 = lane & 15, q = lane >> 4, qq = r16 >> 2, p4 = lane & 3;
    asm volatile("" : "+v"(fh0));
    poll_flags<2>(ctl + WS_FH / 4 + (tg * 32 + ((7 * wv) >> 1) + (lane & 3)) * FL, epoch, fh0);
    TS(10);
#define G2WIN 6
    constexpr int WIN = G2WIN;
    v4u hf[28];
#define H_LOAD(n) hf[n] = __builtin_amdgcn_raw_buffer_load_b128(rsWS, lane * 16 + (((((n) & 3) + 1 + (wv >> 1)) & 3) << 10), WS_HT + (tg * 56 + KS2((n) >> 2)) * 4096, SC1)
#pragma unroll
    for (int n = 0; n < WIN; ++n) { H_LOAD(n); SCHED(); }
    if (pend) {
      asm volatile("s_waitcnt vmcnt(%0)" ::"n"(WIN) : "memory");
      if (lane == 0) __hip_atomic_store(pend, epoch, RLX_AGENT);
      __builtin_amdgcn_s_setprio(0);
      SCHED();
    }
    GAS u32* const fsp = ctl + WS_FS / 4 + ((tg >> 4) * 512 + cg * 64 + wv * 8 + (lane >> 3)) * FL;
    const int tok = tg * 64 + 16 * (wv >> 1) + r16;
    const int c8 = cg * 64 + 32 * (wv & 1) + 8 * q;
    u32 fs, fe0 = epoch, fe1 = epoch;
    bool early;
    v4u xv, bv, sv;
    f32x4 acc[4][4];
#pragma unroll
    for (int i = 0; i < 4; ++i)
#pragma unroll
      for (int j = 0; j < 4; ++j) acc[i][j] = f32x4{0.f, 0.f, 0.f, 0.f};
#define RED_WRITE(j)                                                                                                  \
  _Pragma("unroll") for (int cp = 0; cp < 2; ++cp) if ((j) < 3 || cp) {                                               \
    const int d = ((((j) + 1 + (wv >> 1)) & 3) << 1) | ((cp + wv) & 1);                                               \
    _Pragma("unroll") for (int e = 0; e < 2; ++e)                                                                     \
      *(LAS f32x4*)(L + LD_RED + (d * 7 + ((j) < 3 ? 2 * (j) + cp : 6)) * 2048 + e * 1024 + lane * 16) = acc[2 * cp + e][j]; \
  }
#pragma unroll
    for (int n = 0; n < 28; ++n) {
#pragma unroll
      for (int c = 0; c < 4; ++c) acc[c][n & 3] = mfma16(aw[n >> 2][c], hf[n], acc[c][n & 3]);
      SCHED();
      if (n == 22) wait_progress(L, lane & 7, 4u);
      if (n >= 25) RED_WRITE(n - 25)
      if (n == 24 - 2 * WIN + 4) fs = __hip_atomic_load(fsp, RLX_AGENT);
      if (n + WIN < 28) H_LOAD(n + WIN);
      if (n == 27 - WIN) {
        xv = *(const GAS v4u*)(x + tok * 512 + c8);
        bv = *(const GAS v4u*)(b2 + c8);
      }
      if (n == 28 - WIN) {
        early = __all(fs == epoch);
        ORDER_BEHIND_POLL();
        sv = __builtin_amdgcn_raw_buffer_load_b128(rsWS, (lane >> 3) * 4096 + (lane & 7) * 16, early ? WS_ST + (cg * 64 + wv * 8) * 4096 + tg * 128 : WS_END, SC1);
        if (b == 0) {
          fe0 = __hip_atomic_load(ctl + WS_FS / 4 + tz * FL, RLX_AGENT);
          fe1 = __hip_atomic_load(ctl + WS_FS / 4 + (512 + tz) * FL, RLX_AGENT);
        }
      }
      SCHED();
    }
    TS(11);
    RED_WRITE(3)
    if (!early) {
      poll_flags<2>(fsp, epoch, epoch + 1u);
      sv = __builtin_amdgcn_raw_buffer_load_b128(rsWS, (lane >> 3) * 4096 + (lane & 7) * 16, WS_ST + (cg * 64 + wv * 8) * 4096 + tg * 128, SC1);
    }
    *(LAS v4u*)(L + LD_ST + wv * 1024 + lane * 16) = sv;
    TS(12);
    asm volatile("" : "+v"(fe0), "+v"(fe1));
    poll_flags<2>(ctl + WS_FS / 4 + tz * FL, epoch, fe0);
    poll_flags<2>(ctl + WS_FS / 4 + (512 + tz) * FL, epoch, fe1);
    BAR();
    if (b == 0 && tz == 0) __hip_atomic_store(ctl + WS_E / 4, epoch, RLX_AGENT);
    TS(13);
    f32x4 part[2][7];
    v2u sq[2];
#pragma unroll
    for (int e = 0; e < 2; ++e) {
#pragma unroll
      for (int j = 0; j < 7; ++j) part[e][j] = *(LAS f32x4*)(L + LD_RED + (wv * 7 + j) * 2048 + e * 1024 + lane * 16);
      sq[e] = trread(L + LD_ST + (32 * (wv & 1) + 8 * q + 4 * e + qq) * 128 + (16 * (wv >> 1) + 4 * p4) * 2);
    }
    SCHED();
    u32 o[4];
#pragma unroll
    for (int e = 0; e < 2; ++e) {
      const u32 ba = bv[2 * e], bb = bv[2 * e + 1], xa = xv[2 * e], xb = xv[2 * e + 1];
      f32x4 m = acc[e][3] + f32x4{bflo(ba), bfhi(ba), bflo(bb), bfhi(bb)};
#pragma unroll
      for (int j = 0; j < 7; ++j) m += part[e][j];
      f32x4 g;
#pragma unroll
      for (int k = 0; k < 4; ++k) g[k] = m[k] * __builtin_amdgcn_rcpf(1.0f + __builtin_amdgcn_exp2f(m[k] * -1.4426950408889634f));
      o[2 * e] = pk2(bflo(xa) + bflo(sq[e].x) * g.x, bfhi(xa) + bfhi(sq[e].x) * g.y);
      o[2 * e + 1] = pk2(bflo(xb) + bflo(sq[e].y) * g.z, bfhi(xb) + bfhi(sq[e].y) * g.w);
    }
    *(LAS v4u*)(L + LD_KREG + (16 * (wv >> 1) + r16) * 144 + (wv & 1) * 64 + q * 16) = v4u{o[0], o[1], o[2], o[3]};
    BAR();
    const v4u ov = *(LAS v4u*)(L + LD_KREG + (8 * wv + (lane >> 3)) * 144 + (lane & 7) * 16);
    __builtin_amdgcn_raw_buffer_store_b128(ov, __builtin_amdgcn_make_buffer_rsrc((void*)out, 0, 2097152, 0x00020000), ((tg * 64 + 8 * wv + (lane >> 3)) * 512 + cg * 64 + (lane & 7) * 8) * 2, 0, 16);
  }
  TS(14);
  __builtin_amdgcn_endpgm();
  };

  if (helper) {
    LAS char* const HX = L + LD_HX + wv * 8192;
    u32 epoch;
    if (swave) WAITV(0); else WAITV(10);
    asm volatile("s_add_u32 %0, %1, 1" : "=s"(epoch) : "s"(eraw) : "memory", "scc");
    if (!swave) TS(3);
    f32x4 gram[2], sumx[2];
#pragma unroll
    for (int i = 0; i < 2; ++i) { gram[i] = f32x4{0.f, 0.f, 0.f, 0.f}; sumx[i] = gram[i]; }
    const int r8 = lane & 7, fr = ((r8 & 3) << 2) | (r8 >> 2);
    v4u xr[8];
#pragma unroll
    for (int s = 0; s < 8; ++s) xr[s] = *(LAS v4u*)(HX + r8 * 1024 + (lane & 8) * 64 + (((4 * s + q) ^ fr) << 4));
    v2u d[8][2];
#pragma unroll
    for (int s = 0; s < 8; ++s)
#pragma unroll
      for (int t = 0; t < 2; ++t)
        d[s][t] = trread(HX + (4 * t + qq) * 1024 + (((8 * s + 2 * q + (p4 >> 1)) ^ ((qq << 2) | t)) << 4) + 8 * (p4 & 1));
    SCHED();
#pragma unroll
    for (int s = 0; s < 8; ++s) {
      gram[s & 1] = mfma16(xr[s], xr[s], gram[s & 1]);
      sumx[s & 1] = mfma16(ones, xr[s], sumx[s & 1]);
    }
    float s1 = sumx[0].x + sumx[1].x, s2 = pick4(gram[0] + gram[1], r16 & 3);
    s1 += __shfl(s1, (lane + 40) & 63);
    s2 += __shfl(s2, (lane + 40) & 63);
    float mean, var, den1;
    ln_stats(s1, s2, mean, var, den1);
    const float rd = __builtin_amdgcn_rcpf(den1);
    const float den2 = bfr(__builtin_amdgcn_sqrtf(bfr(bfr(var * rd * rd) + 1e-5f)));
    const float R = __builtin_amdgcn_rcpf(den1 * den2);
    if (q == qq && r16 < 8) {
      *(LAS f32x2*)(L + LD_HST + (8 * wv + r16) * 8) = f32x2{rd, -bfr(mean) * rd};
      __builtin_amdgcn_raw_buffer_store_b128(v4u{__builtin_bit_cast(u32, R), epoch, __builtin_bit_cast(u32, R * mean), epoch}, rsWS,
                                             WS_STAT + (hid * 64 + 8 * wv + r16) * 16, 0, SC1);
    }
    WAITL();
    if (!swave) TS(4);
    f32x4 sv[4];
#pragma unroll
    for (int i = 0; i < 4; ++i) sv[i] = *(LAS f32x4*)(L + LD_HST + wv * 64 + i * 16);
#pragma unroll
    for (int cb = 0; cb < 8; ++cb) {
      v4u o;
      o.x = pk2(bflo(d[cb][0].x) * sv[0].x + sv[0].y, bfhi(d[cb][0].x) * sv[0].z + sv[0].w);
      o.y = pk2(bflo(d[cb][0].y) * sv[1].x + sv[1].y, bfhi(d[cb][0].y) * sv[1].z + sv[1].w);
      o.z = pk2(bflo(d[cb][1].x) * sv[2].x + sv[2].y, bfhi(d[cb][1].x) * sv[2].z + sv[2].w);
      o.w = pk2(bflo(d[cb][1].y) * sv[3].x + sv[3].y, bfhi(d[cb][1].y) * sv[3].z + sv[3].w);
      *(LAS v4u*)(L + LD_HT + (cb * 64 + lane) * 128 + ((wv ^ (lane & 7)) << 4)) = o;
    }
    if (!swave) TS(5);
    BAR();
    if (swave) {
      v4u o[16];
#pragma unroll
      for (int i = 0; i < 16; ++i) o[i] = *(LAS v4u*)(L + LD_HT + (w * 128 + i * 8 + (lane >> 3)) * 128 + (((lane ^ (lane >> 3)) & 7) << 4));
      SCHED();
#pragma unroll
      for (int i = 0; i < 16; ++i) __builtin_amdgcn_raw_buffer_store_b128(o[i], rsWS, lane * 16 + WS_XNT + hid * 65536 + (w * 16 + i) * 1024, 0, SC1);
      publish(ctl + WS_FXN / 4 + (hid * 4 + w) * FL, epoch, lane);
    }
    if (!swave) TS(6);
    SCHED();
  }

  if (swave) {
    __builtin_amdgcn_s_setprio(2);
    const u32 voX = (u32)((32 * w + (lane >> 3)) * 1024 + (((lane & 7) ^ (lane >> 3)) << 4));
    const u32 voW = (u32)((4 * w + q) * 3584 + ((r16 ^ ((q << 2) | w)) << 4));
#define G1_STAGE(st) G1_PART(st, 0, 4)
#define G1_PART(st, p0, p1)                                                                                  \
  {                                                                                                          \
    const int sa_ = ((st) + rot) & 7;                                                                        \
    _Pragma("unroll") for (int p = p0; p < p1; ++p) {                                                        \
      dma16(voX, rsX, (u32)(mt * 131072 + p * 8192 + sa_ * 128), (u32)(((st) & 3) * 32768 + w * 4096 + p * 1024)); \
      dma16(voW, rsW1, (u32)((sa_ * 64 + 16 * p) * 3584 + nt * 256), (u32)(((st) & 3) * 32768 + 16384 + p * 4096 + w * 1024)); \
    }                                                                                                        \
  }
    if (lane == 0) *(LAS u32*)(L + LD_PS + wv * 4) = 0u;
    TS(1);
    WAITV(8);
    u32 epoch;
    asm volatile("s_waitcnt lgkmcnt(0)\n\ts_add_u32 %0, %1, 1" : "=s"(epoch) : "s"(eraw) : "memory", "scc");
    TS(2);
    BAR();
    TS(3);
    TC(0);
    if (!helper) G1_STAGE(2)
    const int r32 = lane & 31, hh = lane >> 5;
    v4u pf[8], qf[8];
    {
#pragma unroll
      for (int s = 0; s < 8; ++s) {
        qf[s] = *(LAS v4u*)(L + LD_Q + w * 8192 + r32 * 256 + (((2 * s + hh) ^ (r32 & 15)) << 4));
        pf[s] = *(LAS v4u*)(PT + (2 * s + hh) * 464 + r32 * 16);
      }
    }
    SCHED();
    TC(1);
    if (!helper) {
      WAITV(8); BAR(); TC(2); G1_STAGE(3) G1_PART(4, 0, 2) TC(3);
      WAITV(12); BAR(); TC(4); G1_PART(4, 2, 4) G1_STAGE(5) TC(5);
      WAITV(16); BAR(); TC(6);
      G1_STAGE(6) TC(7);
      WAITV(16); BAR(); TC(8); G1_STAGE(7) TC(9);
      WAITV(16); BAR();
    }
    TC(10);
    GAS u32* const fxnp = ctl + WS_FXN / 4 + ((hw * 16 + r16) * 4 + (ch >> 7)) * FL;
    GAS u32* const fw2p = ctl + WS_FW2 / 4 + ((cg * 56 + 7 * (w + 4 * ((lane >> 4) & 1))) * 2 + min(r16, 13)) * FL;
    const int sto = WS_STAT + ((helper ? 0 : mt) * 128 + 32 * w + r32) * 16;
    v4u sg = __builtin_amdgcn_raw_buffer_load_b128(rsWS, sto, 0, SC1);
    const u32 fw2 = __hip_atomic_load(fw2p, RLX_AGENT);
    const u32 pDs = LD16(Dskip, ch);
    SCHED();
    f32x16 kacc;
#pragma unroll
    for (int i = 0; i < 16; ++i) kacc[i] = 0.f;
#pragma unroll
    for (int s = 0; s < 8; ++s) kacc = mfma32(pf[s], qf[s], kacc);
    SCHED();
    TC(11);
    if (!helper) { WAITV(3); BAR(); }
    TC(12);
    TS(4);
#pragma unroll
    for (int i = 0; i < 4; ++i)
      *(LAS f32x4*)(L + LD_CX + w * 4096 + i * 1024 + lane * 16) = f32x4{kacc[4 * i], kacc[4 * i + 1], kacc[4 * i + 2], kacc[4 * i + 3]};
    if (!helper) {
      if (!__all(sg.y == epoch && sg.w == epoch)) {
#pragma unroll 1
        for (u32 spins = 0; spins < SPIN_CAP; ++spins) {
          __builtin_amdgcn_s_sleep(4);
          asm volatile("" ::: "memory");
          sg = __builtin_amdgcn_raw_buffer_load_b128(rsWS, sto, 0, SC1);
          if (__all(sg.y == epoch && sg.w == epoch)) break;
        }
      }
      *(LAS v2u*)(L + LD_STAT + (32 * w + r32) * 8) = v2u{sg.x, sg.z};
    }
    poll_flags<4>(fw2p, epoch, fw2);
    set_progress(L, wv, 1u);
    TS(5);
    v4u aw[7][4];
#pragma unroll
    for (int c = 0; c < 4; ++c) AW_LOAD(c >> 2, c & 3);
    SCHED();
    wait_progress(L, wv ^ 1, 1u);
    {
      f32x4 v[4];
#pragma unroll
      for (int i = 0; i < 4; ++i) v[i] = *(LAS f32x4*)(L + LD_CX + (w ^ 1) * 4096 + i * 1024 + lane * 16);
#pragma unroll
      for (int i = 0; i < 15; ++i) {
        kacc[i] = sadd(kacc[i], v[i >> 2][i & 3]);
      }
    }
    {
      const bool odd = r32 & 1, up = hh != 0;
      const int ia[4][9] = {{0, 1, 2, 3, 4, 5, 6, 7, 8}, {9, 9, 9, 9, -1, 10, 10, 10, 10}, {11, 11, 11, 11, -1, 12, 12, 12, 12}, {13, 13, 13, 13, -1, 14, 14, 14, 14}};
      const int ib[4][9] = {{0, 0, 1, 2, 2, 2, 3, 4, 4}, {5, 5, 5, 6, 6, 6, 7, 7, 7}, {8, 8, 8, 9, 9, 9, 10, 10, 10}, {11, 11, 11, 11, -1, 12, 12, 12, 12}};
      LAS char* const KC = PT + hw * 64 + (255 - r32) * 16;
#pragma unroll
      for (int u = 0; u < 4; ++u) {
        SCHED();
        AW_LOAD(1 + (u >> 1), 2 * (u & 1));
        SCHED();
        float e[9];
#pragma unroll
        for (int i = 0; i < 9; ++i) {
          const float va = ia[u][i] >= 0 ? kacc[ia[u][i]] : (odd ? kacc[ia[u][5]] : kacc[ia[u][3]]);
          const float vb = ib[u][i] >= 0 ? kacc[ib[u][i]] : (odd ? kacc[ib[u][5]] : kacc[ib[u][3]]);
          e[i] = up ? vb : va;
        }
        SCHED();
        AW_LOAD(1 + (u >> 1), 2 * (u & 1) + 1);
        SCHED();
        v4u d;
        if (hw) { d.x = pk2(e[8], e[7]); d.y = pk2(e[6], e[5]); d.z = pk2(e[4], e[3]); d.w = pk2(e[2], e[1]); }
        else    { d.x = pk2(e[7], e[6]); d.y = pk2(e[5], e[4]); d.z = pk2(e[3], e[2]); d.w = pk2(e[1], e[0]); }
        const int sa = u == 0 ? 0 : u + 3, sb = u == 3 ? 7 : u + 1;
        *(LAS v4u*)(KC - (up ? sb : sa) * 512) = d;
      }
      if (lane < 2) *(LAS v4u*)(PT + hw * 64 + 4096 + lane * 16) = v4u{(hw && lane == 0) ? pk2(kacc[0], 0.f) : 0u, 0u, 0u, 0u};
    }
    SCHED();
    TC(13);
    u32 fxa = __hip_atomic_load(fxnp, RLX_AGENT);
#pragma unroll
    for (int c = 12; c < 22; ++c) AW_LOAD(c >> 2, c & 3);
    SCHED();
    asm volatile("" : "+v"(fxa));
    const bool xearly = __all(fxa == epoch);
    ORDER_BEHIND_POLL();
    const int xo = WS_XNT + (16 * hw) * 65536 + ch * 128;
    v4u u0 = __builtin_amdgcn_raw_buffer_load_b128(rsWS, (lane >> 3) * 65536 + (lane & 7) * 16, xearly ? xo : WS_END, SC1);
    v4u u1 = __builtin_amdgcn_raw_buffer_load_b128(rsWS, (lane >> 3) * 65536 + (lane & 7) * 16, xearly ? xo + 8 * 65536 : WS_END, SC1);
#pragma unroll
    for (int c = 22; c < 28; ++c) AW_LOAD(c >> 2, c & 3);
    SCHED();
    if (!xearly) {
      poll_flags<8>(fxnp, epoch, epoch + 1u);
      u0 = __builtin_amdgcn_raw_buffer_load_b128(rsWS, (lane >> 3) * 65536 + (lane & 7) * 16, xo, SC1);
      u1 = __builtin_amdgcn_raw_buffer_load_b128(rsWS, (lane >> 3) * 65536 + (lane & 7) * 16, xo + 8 * 65536, SC1);
      __builtin_amdgcn_s_waitcnt(0x0F70);
    }
    LAS char* const UB = L + LD_U + cw * 4608;
    if (hw == 0 && lane < 32) *(LAS v4u*)(UB + lane * 16) = v4u{0u, 0u, 0u, 0u};
    *(LAS v4u*)(UB + 512 + (128 * hw + lane) * 16) = u0;
    *(LAS v4u*)(UB + 512 + (128 * hw + 64 + lane) * 16) = u1;
    set_progress(L, wv, 2u);
    {
      const u32 po = (u32)(WS_W2T + (cg * 56 + 7 * w) * 4096 + ((tg & 3) * 56 + min(lane, 55)) * 128);
      asm volatile("s_mov_b32 m0, %0\n\ts_nop 0\n\tglobal_load_lds_dword %1, %2 sc1" ::"s"((u32)LD_DUMMY), "v"(po), "s"(ws) : "memory");
    }
    SCHED();
    TS(6);
    const float dskip = bf2f(pDs);
    const int sgm = 1 - (r16 & 1);
    LAS char* const ka = L + LD_KREG + (2 * cw + sgm) * 8192 + sgm * 64 + 2 * (2031 - r16 + 8 * q + sgm) - hw * 256 - 7 * 512 - 3 * 64;
    LAS char* const ub = UB + 2 * (256 - 16 + 16 * r16 + 8 * q) - hw * 256 - 3 * 64;
    u32 kb[4];
#pragma unroll
    for (int i = 0; i < 4; ++i) { kb[i] = (u32)(size_t)ka + i * 1024; asm volatile("" : "+v"(kb[i])); }
    f32x4 y[8];
    {
      f32x4 y0[4];
#pragma unroll
      for (int i = 0; i < 4; ++i) {
        const v2u xv = *(LAS v2u*)(UB + 512 + (256 * (4 * hw + i) + 16 * r16 + 4 * q) * 2);
        y0[i] = f32x4{smul(bflo(xv.x), dskip), smul(bfhi(xv.x), dskip), smul(bflo(xv.y), dskip), smul(bfhi(xv.y), dskip)};
      }
      const f32x4 zz = {0.f, 0.f, 0.f, 0.f};
      if (hw) {
#pragma unroll
        for (int i = 0; i < 4; ++i) { y[i] = zz; y[4 + i] = y0[i]; }
      } else {
#pragma unroll
        for (int i = 0; i < 4; ++i) { y[i] = y0[i]; y[4 + i] = zz; }
      }
#pragma unroll
      for (int i = 0; i < 8; ++i) asm volatile("" : "+v"(y[i]));
    }
    wait_progress(L, wv ^ 1, 2u);
    v4u af[4][8], bfv[4][8];
#define CV_READ(rr, g)                                                                                   \
  {                                                                                                      \
    LAS u32* pa = (LAS u32*)(size_t)(kb[(7 - (g)) >> 1] + ((7 - (g)) & 1) * 512 + (3 - (rr)) * 64);      \
    af[rr][g] = v4u{pa[0], pa[1], pa[2], pa[3]};                                                         \
    bfv[rr][g] = *(LAS v4u*)(ub + (g) * 512 + (3 - (rr)) * 64);                                          \
  }
#define CV_LEVEL(rr, k, T0, T1)                                                              \
  _Pragma("unroll") for (int o = 0; o <= (k); ++o)                                           \
    if ((k) + o >= (T0) && (k) + o < (T1)) {                                                 \
      y[(k) + o] = mfma16(af[rr][k], bfv[rr][o], y[(k) + o]);                                \
      if (o < (k)) y[(k) + o] = mfma16(af[rr][o], bfv[rr][k], y[(k) + o]);                   \
    }
#define CV_MIX(n)                                                                            \
  _Pragma("unroll") for (int t = 0; t < (n); ++t) {                                          \
    __builtin_amdgcn_sched_group_barrier(0x008, 1, 0);                                       \
    __builtin_amdgcn_sched_group_barrier(0x100, 1, 0);                                       \
  }                                                                                          \
  SCHED();
#pragma unroll
    for (int g = 0; g < 4; ++g) CV_READ(0, g)
    SCHED();
    wait_progress(L, w, 4u);
    __builtin_amdgcn_s_setprio(2);
    TS(7);
    TC(14);
#pragma unroll
    for (int rr = 0; rr < 3; ++rr) {
      CV_READ(rr, 4) CV_READ(rr, 5) CV_READ(rr, 6) CV_READ(rr, 7)
      CV_LEVEL(rr, 0, 0, 8) CV_LEVEL(rr, 1, 0, 8) CV_LEVEL(rr, 2, 0, 8) CV_LEVEL(rr, 3, 0, 8)
      CV_MIX(12)
      CV_READ(rr + 1, 0) CV_READ(rr + 1, 1) CV_READ(rr + 1, 2) CV_READ(rr + 1, 3)
      CV_LEVEL(rr, 4, 0, 8) CV_LEVEL(rr, 5, 0, 8) CV_LEVEL(rr, 6, 0, 8) CV_LEVEL(rr, 7, 0, 8)
      CV_MIX(12)
    }
    f32x4 mine[4];
    CV_READ(3, 4) CV_READ(3, 5) CV_READ(3, 6) CV_READ(3, 7)
    SCHED();
    if (hw) {
      CV_LEVEL(3, 0, 0, 4) CV_LEVEL(3, 1, 0, 4) CV_LEVEL(3, 2, 0, 4) CV_LEVEL(3, 3, 0, 4)
      CV_LEVEL(3, 2, 4, 8) CV_LEVEL(3, 3, 4, 8)
      SCHED();
#pragma unroll
      for (int i = 0; i < 4; ++i) *(LAS f32x4*)(L + LD_CX + w * 4096 + i * 1024 + lane * 16) = y[i];
      *(volatile LAS u32*)(L + LD_PS + wv * 4) = 3u;
      SCHED();
      CV_LEVEL(3, 4, 4, 8) CV_LEVEL(3, 5, 4, 8) CV_LEVEL(3, 6, 4, 8) CV_LEVEL(3, 7, 4, 8)
#pragma unroll
      for (int i = 0; i < 4; ++i) mine[i] = y[4 + i];
    } else {
      CV_LEVEL(3, 2, 4, 8) CV_LEVEL(3, 3, 4, 8) CV_LEVEL(3, 4, 4, 8) CV_LEVEL(3, 5, 4, 8) CV_LEVEL(3, 6, 4, 8) CV_LEVEL(3, 7, 4, 8)
      CV_LEVEL(3, 0, 0, 4) CV_LEVEL(3, 1, 0, 4)
      SCHED();
#pragma unroll
      for (int i = 0; i < 4; ++i) *(LAS f32x4*)(L + LD_CX + w * 4096 + i * 1024 + lane * 16) = y[4 + i];
      *(volatile LAS u32*)(L + LD_PS + wv * 4) = 3u;
      SCHED();
      CV_LEVEL(3, 2, 0, 4) CV_LEVEL(3, 3, 0, 4)
#pragma unroll
      for (int i = 0; i < 4; ++i) mine[i] = y[i];
    }
    SCHED();
    TC(15);
    const u32 fh0 = __hip_atomic_load(ctl + WS_FH / 4 + (tg * 32 + ((7 * wv) >> 1) + (lane & 3)) * FL, RLX_AGENT);
    SCHED();
    wait_progress(L, wv ^ 1, 3u);
    f32x4 oy[4];
#pragma unroll
    for (int i = 0; i < 4; ++i) oy[i] = *(LAS f32x4*)(L + LD_CX + (w ^ 1) * 4096 + i * 1024 + lane * 16);
    set_progress(L, wv, 4u);
#pragma unroll
    for (int i = 0; i < 4; ++i) {
      v2u o;
      o.x = pk2(sadd(mine[i].x, oy[i].x), sadd(mine[i].y, oy[i].y));
      o.y = pk2(sadd(mine[i].z, oy[i].z), sadd(mine[i].w, oy[i].w));
      __builtin_amdgcn_raw_buffer_store_b64(o, rsWS, (16 * r16 + 4 * q) * 2 + WS_ST + ch * 4096 + (4 * hw + i) * 512, 0, SC1);
    }
    TS(8);
    gemm2_tail(aw, epoch, ctl + WS_FS / 4 + (hw * 512 + ch) * FL, fh0);
  } else {
    const int upp = hw;
    if (lane == 0) *(LAS u32*)(L + LD_PS + wv * 4) = 0u;
    SCHED();
    TS(1);
    const int mi = w >> 1, ni = w & 1;
    f32x4 acc[4][4], csum[4];
#pragma unroll
    for (int i = 0; i < 4; ++i) {
#pragma unroll
      for (int j = 0; j < 4; ++j) { acc[i][j] = f32x4{0.f, 0.f, 0.f, 0.f}; asm volatile("" : "+v"(acc[i][j])); }
      csum[i] = f32x4{0.f, 0.f, 0.f, 0.f};
      asm volatile("" : "+v"(csum[i]));
    }
    u32 xb[2][2], wb[2][4][2];
#pragma unroll
    for (int h = 0; h < 2; ++h) {
#pragma unroll
      for (int k = 0; k < 2; ++k) {
        xb[h][k] = (u32)(((64 * mi + r16) * 128 + ((q ^ (r16 & 7)) << 4)) ^ (k * 64)) + h * 65536;
        asm volatile("" : "+v"(xb[h][k]));
      }
#pragma unroll
      for (int j = 0; j < 4; ++j)
#pragma unroll
        for (int t = 0; t < 2; ++t) {
          wb[h][j][t] = (u32)(16384 + (8 * q + 4 * t + qq) * 256 + (((2 * (4 * ni + j) + (p4 >> 1)) ^ ((qq << 2) | ((2 * q + t) & 3))) << 4) + 8 * (p4 & 1)) + h * 65536;
          asm volatile("" : "+v"(wb[h][j][t]));
        }
    }
    v4u onev = ones;
    asm volatile("" : "+v"(onev));
    u32 qad[16];
#pragma unroll
    for (int r = 0; r < 16; ++r) { qad[r] = (u32)(LD_Q + w * 8192) + (wo ^ (u32)(r << 4)); asm volatile("" : "+v"(qad[r])); }
    SCHED();
    TC(1);
    u32 epoch;
    asm volatile("s_waitcnt lgkmcnt(0)\n\ts_add_u32 %0, %1, 1" : "=s"(epoch) : "s"(eraw) : "memory", "scc");
    SCHED();
    WAITV(0);
    LAS u16* const par = (LAS u16*)(PT + lane * 4);
    const u32 pLd = par[0], pAr = par[128], pAi = par[256], pBr = par[384], pBi = par[512], pCr = par[640], pCi = par[768], pB1 = par[896];
    v2u wt[2][2];
#pragma unroll
    for (int e = 0; e < 2; ++e)
#pragma unroll
      for (int t = 0; t < 2; ++t) wt[e][t] = trread(PT + 2048 + (16 * t + 4 * q + qq) * 64 + 16 * p4 + 8 * e);
    asm volatile("" ::: "memory");
    {
      const float dt = bfr(__builtin_amdgcn_exp2f(bf2f(pLd) * 1.4426950408889634f));
      const float la = dt * bf2f(pAr) * 1.4426950408889634f;
      const float th = dt * bf2f(pAi) * 0.15915494309189535f;
      const f32x2 dBc = {dt * bf2f(pBr), -dt * bf2f(pBi)};
      const f32x2 Ccc = {bf2f(pCr), -bf2f(pCi)};
      const f32x2 z8 = zpow(la, th, 8.f), z16 = zpow(la, th, 16.f);
      const f32x2 y1 = zpow(la, -th, 1.f), y256 = zpow(la, -th, 256.f), y8 = {z8.x, -z8.y};
      const f32x2 y2 = cmul(y1, y1), y4 = cmul(y2, y2);
#define PROW(h, g) (((g) & 3) + 8 * ((g) >> 2) + 4 * (h))
#define PW(h, g, v) *(LAS u32*)(PT + (lane >> 2) * 464 + (lane & 3) * 4 + PROW(h, g) * 16) = pk2v(v)
      f32x2 W[8];
      W[0] = cmul(Ccc, dBc);
#pragma unroll
      for (int c = 1; c < 8; ++c) W[c] = cmul(W[c - 1], y256);
      {
        f32x2 te = W[0], to = cmul(W[0], y1), t1 = W[1];
        PW(0, 0, te); PW(0, 1, to); PW(1, 0, t1);
#pragma unroll
        for (int a = 1; a <= 4; ++a) {
          te = cmul(te, y2); PW(0, 2 * a, te);
          if (a < 4) { to = cmul(to, y2); PW(0, 2 * a + 1, to); }
          t1 = cmul(t1, y2); PW(1, a, t1);
        }
      }
      PW(1, 5, W[2]); PW(1, 6, cmul(W[2], y4)); PW(1, 7, cmul(W[2], y8));
      PW(1, 8, W[3]); PW(1, 9, cmul(W[3], y4)); PW(1, 10, cmul(W[3], y8));
      PW(0, 9, W[4]); PW(0, 10, cmul(W[4], y8));
      PW(0, 11, W[5]); PW(0, 12, cmul(W[5], y8));
      PW(0, 13, W[6]); PW(0, 14, cmul(W[6], y8));
      PW(1, 11, W[7]); PW(1, 12, cmul(W[7], y8));
      f32x2 qa = {1.f, 0.f}, qb = z8;
      u32 qv[32];
#pragma unroll
      for (int i = 0; i < 16; ++i) {
        qv[2 * i] = pk2v(qa);
        qv[2 * i + 1] = pk2v(qb);
        if (i < 15) { qa = (i == 0) ? z16 : cmul(qa, z16); qb = cmul(qb, z16); }
      }
#pragma unroll
      for (int r = 0; r < 16; ++r) {
        *(LAS u32*)(size_t)(qad[r] + r * 256) = qv[r];
        *(LAS u32*)(size_t)(qad[r] + (r + 16) * 256) = qv[r + 16];
      }
    }
    SCHED();
    TC(2);
#pragma unroll
    for (int e = 0; e < 2; ++e)
      __builtin_amdgcn_raw_buffer_store_b128(v4u{wt[e][0].x, wt[e][0].y, wt[e][1].x, wt[e][1].y}, rsWS,
                                             lane * 16 + (helper ? WS_END : WS_W2T) + ((xcd * 56 + uks) * 4 + 2 * upp + e) * 1024, 0, SC1);
    *(LAS float*)(L + LD_B1 + (tid & 127) * 4) = bf2f(pB1);
    TC(3);
    TS(2);
    v4u aw[7][4];
    u32 fh0 = epoch + 1u;
    if (!helper) {
      v4u xf[2][4], wf[2][4];
#define G1_READ(n)                                                                                            \
  _Pragma("unroll") for (int i = 0; i < 4; ++i) {                                                             \
    const v2u lo = trread(L + (((n) >> 1) & 1) * 32768 + ((n) & 1) * 8192 + wb[((n) >> 2) & 1][i][0]);        \
    const v2u hi = trread(L + (((n) >> 1) & 1) * 32768 + ((n) & 1) * 8192 + wb[((n) >> 2) & 1][i][1]);        \
    wf[(n) & 1][i] = v4u{lo.x, lo.y, hi.x, hi.y};                                                             \
    xf[(n) & 1][i] = *(LAS v4u*)(L + (((n) >> 1) & 1) * 32768 + i * 2048 + xb[((n) >> 2) & 1][(n) & 1]);      \
  }
      __builtin_amdgcn_s_setprio(0);
      TC(4);
      BAR();
      TC(5);
      TS(3);
      G1_READ(0)
      SCHED();
#pragma unroll 1
      for (int it = 0; it < 2; ++it) {
#pragma unroll
        for (int n = 0; n < 8; ++n) {
          if (n & 1) {
            if (n < 4 || it == 0) BAR();
            if (n == 5 && it == 0) {
              __builtin_amdgcn_s_setprio(3);
              publish(ctl + WS_FW2 / 4 + ((xcd * 56 + uks) * 2 + upp) * FL, epoch, lane);
              __builtin_amdgcn_s_setprio(0);
            }
            SCHED();
          }
          G1_READ(n + 1)
#pragma unroll
          for (int j = 0; j < 4; ++j) {
#pragma unroll
            for (int i = 0; i < 4; ++i) acc[j][i] = mfma16(wf[n & 1][j], xf[n & 1][i], acc[j][i]);
            csum[j] = mfma16(wf[n & 1][j], onev, csum[j]);
          }
#pragma unroll
          for (int k = 0; k < 12; ++k) {
            __builtin_amdgcn_sched_group_barrier(0x008, 1, 0);
            __builtin_amdgcn_sched_group_barrier(0x100, 1, 0);
          }
          __builtin_amdgcn_sched_group_barrier(0x008, 8, 0);
          SCHED();
        }
      }
      TS(4);
      __builtin_amdgcn_s_setprio(3);
      wait_progress(L, 4 + (lane & 3), 1u);
      TS(5);
      float rs[4], rm[4];
      f32x4 bb[4];
#pragma unroll
      for (int i = 0; i < 4; ++i) {
        const f32x2 st = *(LAS f32x2*)(L + LD_STAT + (64 * mi + 16 * i + r16) * 8);
        rs[i] = st.x; rm[i] = -st.y;
        asm volatile("" : "+v"(rm[i]));
        bb[i] = *(LAS f32x4*)(L + LD_B1 + (64 * ni + 16 * i + 4 * q) * 4);
      }
      SCHED();
#pragma unroll
      for (int jp = 0; jp < 2; ++jp) {
#pragma unroll
        for (int i = 0; i < 4; ++i) {
          TC(8 + jp * 4 + i);
          f32x2 pre[4], ee[4];
#pragma unroll
          for (int k = 0; k < 4; ++k) {
            const f32x4 a4 = acc[2 * jp + (k >> 1)][i], c4 = csum[2 * jp + (k >> 1)], b4 = bb[2 * jp + (k >> 1)];
            const f32x2 a2 = (k & 1) ? f32x2{a4.z, a4.w} : f32x2{a4.x, a4.y};
            const f32x2 c2 = (k & 1) ? f32x2{c4.z, c4.w} : f32x2{c4.x, c4.y};
            const f32x2 b2v = (k & 1) ? f32x2{b4.z, b4.w} : f32x2{b4.x, b4.y};
            pre[k] = a2 * rs[i] + (c2 * rm[i] + b2v);
            f32x2 arg = pre[k] * (pre[k] * pre[k] * -0.1029432397f + -2.302208198f);
            asm("" : "+v"(arg));
            ee[k] = f32x2{__builtin_amdgcn_exp2f(arg.x), __builtin_amdgcn_exp2f(arg.y)};
          }
          u32 o[4];
#pragma unroll
          for (int k = 0; k < 4; ++k) {
            f32x2 dn = ee[k] + 1.0f;
            asm("" : "+v"(dn));
            o[k] = pk2v(pre[k] * f32x2{__builtin_amdgcn_rcpf(dn.x), __builtin_amdgcn_rcpf(dn.y)});
          }
          __builtin_amdgcn_raw_buffer_store_b128(v4u{o[0], o[1], o[2], o[3]}, rsWS,
                                                 lane * 16 + ABL_HT + (((2 * mt + mi) * 56 + nt * 4 + 2 * ni + jp) * 4 + i) * 1024, 0, SC1);
          SCHED();
        }
      }
      set_progress(L, wv, 4u);
      __builtin_amdgcn_s_setprio(0);
      TS(6);
#pragma unroll
      for (int c = 0; c < 8; ++c) AW_LOAD(c >> 2, c & 3);
      SCHED();
      __builtin_amdgcn_s_setprio(3);
      WAITV(8);
      if (lane == 0) __hip_atomic_store(ctl + WS_FH / 4 + ((2 * mt + mi) * 32 + 2 * nt + ni) * FL, epoch, RLX_AGENT);
      __builtin_amdgcn_s_setprio(0);
      SCHED();
      TS(7);
#pragma unroll
      for (int c = 8; c < 20; ++c) AW_LOAD(c >> 2, c & 3);
      fh0 = __hip_atomic_load(ctl + WS_FH / 4 + (tg * 32 + ((7 * wv) >> 1) + (lane & 3)) * FL, RLX_AGENT);
#pragma unroll
      for (int c = 20; c < 28; ++c) AW_LOAD(c >> 2, c & 3);
      SCHED();
    } else {
      __builtin_amdgcn_s_setprio(0);
      BAR();
      set_progress(L, wv, 4u);
      wait_progress(L, 4 + (lane & 3), 2u);
#pragma unroll
      for (int s = 0; s < 7; ++s)
#pragma unroll
        for (int c = 0; c < 4; ++c) AW_LOAD(s, c);
      SCHED();
    }
    TS(8);
    gemm2_tail(aw, epoch, nullptr, fh0);
  }
}

extern "C" void kernel_launch(void* const* d_in, const int* in_sizes, int n_in, void* d_out, int out_size, void* d_ws,
                              size_t ws_size, hipStream_t stream) {
  (void)in_sizes; (void)n_in; (void)out_size; (void)ws_size;
  (void)hipFuncSetAttribute((const void*)s4_mixer_kernel, hipFuncAttributeMaxDynamicSharedMemorySize, LDS_BYTES);
  s4_mixer_kernel<<<256, 512, LDS_BYTES, stream>>>(
      (const u16*)d_in[0], 2097152u, 0x00020000u, (const u16*)d_in[10], 1835008u, 0x00020000u, (const u16*)d_in[12], (char*)d_ws, (const u16*)d_in[1], (const u16*)d_in[2],
      (const u16*)d_in[3], (const u16*)d_in[4], (const u16*)d_in[5], (const u16*)d_in[6], (const u16*)d_in[7],
      (const u16*)d_in[8], (const u16*)d_in[11], (const u16*)d_in[13], (u16*)d_out);
}
```

```cpp
#include <hip/hip_runtime.h>
#include <hip/hip_bf16.h>

typedef unsigned int u32;
typedef unsigned short u16;
typedef u32 v4u __attribute__((ext_vector_type(4)));
typedef u32 v2u __attribute__((ext_vector_type(2)));
typedef float f32x2 __attribute__((ext_vector_type(2)));
typedef float f32x4 __attribute__((ext_vector_type(4)));
typedef float f32x16 __attribute__((ext_vector_type(16)));
typedef short s16x4 __attribute__((ext_vector_type(4)));
typedef short s16x8 __attribute__((ext_vector_type(8)));
typedef __bf16 bf16x2 __attribute__((ext_vector_type(2)));
#define GAS __attribute__((address_space(1)))
#define LAS __attribute__((address_space(3)))
#define DEV __device__ __forceinline__
#define RLX_AGENT __ATOMIC_RELAXED, __HIP_MEMORY_SCOPE_AGENT
#define SC1 16

constexpr int WS_E = 0;
constexpr int FL = 32;
constexpr int WS_STAT = 4096;
constexpr int WS_FXN = 65536;
constexpr int WS_FW2 = WS_FXN + 256 * 128;
constexpr int WS_FH = WS_FW2 + 1024 * 128;
constexpr int WS_FS = WS_FH + 1024 * 128;
constexpr int WS_XNT = 524288;
constexpr int WS_W2T = WS_XNT + 2097152;
constexpr int WS_HT = WS_W2T + 1835008;
constexpr int WS_ST = WS_HT + 7340032;
constexpr int WS_END = WS_ST + 2097152;
constexpr int LD_Q = 98304;
constexpr int LD_KREG = 131072;
constexpr int LD_PS = LD_KREG + 7424;
constexpr int LD_DUMMY = LD_KREG + 7680;
constexpr int LD_B1 = LD_KREG + 8192 + 7424;
constexpr int LD_CS = LD_KREG + 2 * 8192 + 7424;
constexpr int LD_HST = LD_KREG + 3 * 8192 + 7424;
constexpr int LD_STAT = 12288;
constexpr int LD_W2S = 90112;
constexpr int LD_U = 0;
constexpr int LD_CX = 16384;
constexpr int LD_SB = 32768;
constexpr int LD_HT = 0;
constexpr int LD_HX = 65536;
constexpr int LD_RED = 0;
constexpr int LD_ST = 114688;
constexpr int MWIN = 6, SWIN = 5;
constexpr int NKM = 8, NKS = 6;
constexpr int MEARLY = 3;
constexpr int LDS_BYTES = 163840;
constexpr u32 SPIN_CAP = 1u << 16;
constexpr u32 CODE_BYTES = 31744;

DEV float bf2f(u32 h) { return __builtin_bit_cast(float, h << 16); }
DEV float bflo(u32 d) { return __builtin_bit_cast(float, d << 16); }
DEV float bfhi(u32 d) { return __builtin_bit_cast(float, d & 0xffff0000u); }
DEV u32 pk2(float lo, float hi) {
  f32x2 v = {lo, hi};
  return __builtin_bit_cast(u32, __builtin_convertvector(v, bf16x2));
}
DEV u32 pk2v(f32x2 v) { return __builtin_bit_cast(u32, __builtin_convertvector(v, bf16x2)); }
DEV float bfr(float v) { return bflo(pk2(v, v)); }
DEV f32x4 mfma16(v4u a, v4u b, f32x4 c) {
  return __builtin_amdgcn_mfma_f32_16x16x32_bf16(__builtin_bit_cast(s16x8, a), __builtin_bit_cast(s16x8, b), c, 0, 0, 0);
}
DEV f32x16 mfma32(v4u a, v4u b, f32x16 c) {
  return __builtin_amdgcn_mfma_f32_32x32x16_bf16(__builtin_bit_cast(s16x8, a), __builtin_bit_cast(s16x8, b), c, 0, 0, 0);
}
DEV v2u trread(LAS char* p) { return __builtin_bit_cast(v2u, __builtin_amdgcn_ds_read_tr16_b64_v4i16((LAS s16x4*)p)); }
DEV v4u mk_rsrc(const void* p, u32 bytes, u32 fmt) {
  const unsigned long long a = (unsigned long long)p;
  v4u r = {(u32)a, (u32)(a >> 32), bytes, fmt};
  return r;
}
DEV void dma16(u32 voff, v4u rs, u32 soff, u32 lds) {
  asm volatile("s_mov_b32 m0, %0\n\ts_nop 0\n\tbuffer_load_dwordx4 %1, %2, %3 offen lds" ::"s"(lds), "v"(voff), "s"(rs), "s"(soff) : "memory");
}
#define WAITV(n) asm volatile("s_waitcnt vmcnt(" #n ")" ::: "memory")
#define WAITL() asm volatile("s_waitcnt lgkmcnt(0)" ::: "memory")
#define SCHED() __builtin_amdgcn_sched_barrier(0)
#define BAR() do { asm volatile("s_waitcnt lgkmcnt(0)" ::: "memory"); __builtin_amdgcn_s_barrier(); asm volatile("" ::: "memory"); SCHED(); } while (0)
#define ORDER_BEHIND_POLL() __builtin_amdgcn_fence(__ATOMIC_ACQUIRE, "wavefront")

DEV f32x2 cmul(f32x2 a, f32x2 z) {
  const f32x2 zr = {-z.y, z.x};
  f32x2 r;
  asm("v_pk_mul_f32 %0, %1, %2 op_sel:[1,0] op_sel_hi:[1,1]\n\tv_pk_fma_f32 %0, %1, %3, %0 op_sel_hi:[0,1,1]" : "=&v"(r) : "v"(a), "v"(zr), "v"(z));
  return r;
}
DEV float sadd(float a, float b) { float r; asm("v_add_f32 %0, %1, %2" : "=v"(r) : "v"(a), "v"(b)); return r; }
DEV float smul(float a, float b) { float r; asm("v_mul_f32 %0, %1, %2" : "=v"(r) : "v"(a), "v"(b)); return r; }
DEV f32x2 zpow(float la, float th, float k) {
  const float mag = __builtin_amdgcn_exp2f(la * k);
  const float fr = __builtin_amdgcn_fractf(th * k);
  f32x2 r = {mag * __builtin_amdgcn_cosf(fr), mag * __builtin_amdgcn_sinf(fr)};
  return r;
}
template <int SLEEP>
DEV void poll_flags(GAS u32* p, u32 epoch, u32 first) {
  if (!__all(first == epoch)) {
#pragma unroll 1
    for (u32 spins = 0; spins < SPIN_CAP; ++spins) {
      const u32 v = __hip_atomic_load(p, RLX_AGENT);
      if (__all(v == epoch)) break;
      __builtin_amdgcn_s_sleep(SLEEP);
    }
  }
  ORDER_BEHIND_POLL();
}
DEV void publish(GAS u32* p, u32 epoch, int lane) {
  WAITV(0);
  if (lane == 0) __hip_atomic_store(p, epoch, RLX_AGENT);
}
DEV void set_progress(LAS char* L, int wv, u32 k) {
  WAITL();
  *(volatile LAS u32*)(L + LD_PS + wv * 4) = k;
}
DEV void wait_progress(LAS char* L, int word, u32 k) {
#pragma unroll 1
  for (u32 spins = 0; spins < (1u << 22); ++spins) {
    const u32 pv = *(volatile LAS u32*)(L + LD_PS + word * 4);
    if (__all(pv >= k)) break;
    __builtin_amdgcn_s_sleep(1);
  }
  asm volatile("" ::: "memory");
}
DEV void ln_stats(float s1, float s2, float& mean, float& var, float& den1) {
  mean = s1 * (1.0f / 512.0f);
  var = fmaxf(s2 * (1.0f / 512.0f) - mean * mean, 0.0f);
  den1 = bfr(__builtin_amdgcn_sqrtf(bfr(bfr(var) + 1e-5f)));
}
DEV float pick4(f32x4 g, int k) { return k == 0 ? g.x : k == 1 ? g.y : k == 2 ? g.z : g.w; }
#define LD16(p, i) ((u32)__hip_atomic_load((const GAS u16*)(p) + (i), __ATOMIC_RELAXED, __HIP_MEMORY_SCOPE_WAVEFRONT))

__global__ void __launch_bounds__(512, 2)
s4_mixer_kernel(const u16* __restrict__ x, u32 x_bytes, u32 x_fmt, const u16* __restrict__ w1, u32 w1_bytes, u32 w1_fmt,
                const u16* __restrict__ w2, char* __restrict__ ws,
                const u16* __restrict__ A_re, const u16* __restrict__ A_im, const u16* __restrict__ B_re,
                const u16* __restrict__ B_im, const u16* __restrict__ C_re, const u16* __restrict__ C_im,
                const u16* __restrict__ log_dt, const u16* __restrict__ Dskip, const u16* __restrict__ b1,
                const u16* __restrict__ b2, u16* __restrict__ out) {
  extern __shared__ __attribute__((aligned(16))) char smem[];
  LAS char* const L = (LAS char*)smem;
  unsigned long long pc;
  asm volatile("s_getpc_b64 %0" : "=s"(pc));
  {
    u32 coff = ((((threadIdx.x & 63) << 2) | ((threadIdx.x >> 6) & 3)) * 32u + (blockIdx.x >> 3)) * 64u;
    coff = (coff < CODE_BYTES && threadIdx.x >= 256) ? coff : 0u;
    asm volatile("s_mov_b32 m0, %0\n\ts_nop 0\n\tglobal_load_lds_dword %1, %2" ::"s"((u32)LD_DUMMY), "v"(coff), "s"(pc) : "memory");
    SCHED();
  }
  const int tid = threadIdx.x, lane = tid & 63;
  const int wv = __builtin_amdgcn_readfirstlane(tid >> 6);
  const int w = wv & 3;
  const bool swave = wv >= 4;
  const int b = blockIdx.x, xcd = b & 7, idx = b >> 3;
  const int r16 = lane & 15, q = lane >> 4, qq = r16 >> 2, p4 = lane & 3;
  const bool helper = idx >= 28;
  const v4u rsX = mk_rsrc(x, x_bytes, x_fmt);
  const v4u rsW1 = mk_rsrc(w1, w1_bytes, w1_fmt);
  asm volatile("" ::"s"(rsX), "s"(rsW1), "s"(w2), "s"(ws), "s"(A_re), "s"(A_im), "s"(B_re), "s"(B_im), "s"(C_re), "s"(C_im), "s"(log_dt), "s"(Dskip), "s"(b1));
  const u32 eraw = *(const __attribute__((address_space(4))) u32*)(ws + WS_E);
#define GET_EPOCH() ({ u32 e_; asm volatile("s_add_u32 %0, %1, 1" : "=s"(e_) : "s"(eraw) : "memory", "scc"); e_; })
  const int mq = (idx * 37) >> 8, mr = idx - 7 * mq;
  const int mt = (xcd >> 1) * 4 + mq, nt = (xcd & 1) * 7 + mr;
  const int cw = w >> 1, hw = w & 1;
  const int ch = 64 * xcd + 2 * idx + cw;
  const int rot = (mr + 2 * mq) & 7;
  const int uks = 2 * (helper ? 0 : idx) + cw;
  const int hid = xcd * 4 + (idx - 28);
  if (helper) {
#pragma unroll 1
    for (int p = 0; p < 8; ++p)
      dma16((u32)(wv * 8192 + ((lane ^ (((p & 3) << 2) | (p >> 2))) << 4)), rsX, (u32)((hid * 64 + p) * 1024), (u32)(LD_HX + (wv * 8 + p) * 1024));
  }
  if (swave) {
    if (!helper) {
      const u32 voX = (u32)((32 * w + (lane >> 3)) * 1024 + (((lane & 7) ^ (lane >> 3)) << 4));
      const u32 voW = (u32)((4 * w + q) * 3584 + ((r16 ^ ((q << 2) | (w & 2))) << 4));
#pragma unroll 1
      for (int i = 0; i < 8; ++i) {
        const int st = i >> 2, pp = i & 3, sa = (st + rot) & 7;
        dma16(voX, rsX, (u32)(mt * 131072 + pp * 8192 + sa * 128), (u32)(st * 32768 + w * 4096 + pp * 1024));
        dma16(voW, rsW1, (u32)((sa * 64 + 16 * pp) * 3584 + nt * 256), (u32)(st * 32768 + 16384 + pp * 4096 + w * 1024));
      }
    }
  } else {
    if (!helper) __builtin_amdgcn_s_setprio(3);
    const u32 po = (u32)(ch * 128 + hw * 64 + lane) * 2u, bo = (u32)((helper ? 0 : nt * 128) + (tid & 127)) * 2u;
    u32 co = (u32)ch * 2u;
    asm volatile("" : "+v"(co));
    const u32 pl = (u32)(LD_KREG + w * 8192);
#define PDMA(k, vo, base) asm volatile("s_add_u32 m0, %0, " #k "*256\n\ts_nop 0\n\tglobal_load_lds_ushort %1, %2" ::"s"(pl), "v"(vo), "s"(base) : "memory", "scc")
    PDMA(0, co, log_dt); PDMA(1, po, A_re); PDMA(2, po, A_im); PDMA(3, po, B_re); PDMA(4, po, B_im); PDMA(5, po, C_re); PDMA(6, po, C_im); PDMA(7, bo, b1);
    const u32 wo2 = (u32)((uks * 32 + (lane >> 2)) * 1024 + xcd * 128 + hw * 64 + (lane & 3) * 16), wo3 = wo2 + 16384u;
    const u32 wl = (u32)(LD_W2S + w * 2048);
    if (!helper) asm volatile("s_mov_b32 m0, %0\n\ts_nop 0\n\tglobal_load_lds_dwordx4 %1, %3\n\ts_add_u32 m0, %0, 1024\n\ts_nop 0\n\tglobal_load_lds_dwordx4 %2, %3"
                 ::"s"(wl), "v"(wo2), "v"(wo3), "s"(w2) : "memory", "scc");
  }
  SCHED();

  const int tg = xcd * 4 + (idx >> 3), cg = idx & 7;
  const __amdgpu_buffer_rsrc_t rsWS = __builtin_amdgcn_make_buffer_rsrc((void*)ws, 0, WS_END, 0x00020000);
  GAS u32* const ctl = (GAS u32*)ws;
  const v4u ones = {0x3f803f80u, 0x3f803f80u, 0x3f803f80u, 0x3f803f80u};
  const int nk = swave ? NKS : NKM, k0 = swave ? 4 * NKM + NKS * w : NKM * w;
  const int rot2 = (cg + 2 * (tg & 3)) % nk;
#define KS2(s) (k0 + ((s) + rot2 >= nk ? (s) + rot2 - nk : (s) + rot2))
#define HX4(ln) ((k0 >> 1) + min((ln) & 3, nk / 2 - 1))
#define AW_LOADB(s, c, base) \
  aw[s][c] = __builtin_amdgcn_raw_buffer_load_b128(rsWS, lane * 16 + ((((c) + 2 * (wv & 1)) & 3) << 10), (base) + (cg * 56 + KS2(s)) * 4096, SC1)
#define AW_LOAD(s, c) AW_LOADB(s, c, WS_W2T)
  const u32 wo = (u32)lane * 4u;
  LAS char* const PT = L + LD_KREG + w * 8192;

  auto gemm2_tail = [&](v4u (&aw)[NKM][4], const u32 epoch, GAS u32* const pend, u32 fh0, const int WIN, const int NF) __attribute__((always_inline)) {
  {
    int tz = threadIdx.x;
    asm volatile("" : "+v"(tz));
    const int lane = tz & 63, r16 = lane & 15, q = lane >> 4, qq = r16 >> 2, p4 = lane & 3;
    asm volatile("" : "+v"(fh0));
    GAS u32* const fhp = ctl + WS_FH / 4 + (tg * 32 + HX4(lane)) * FL;
    u32 rdy = (u32)__builtin_amdgcn_ballot_w64(fh0 == epoch);
#define H_GATE(s)                                                                     \
  {                                                                                   \
    const u32 bit = 1u << ((KS2(s) >> 1) - (k0 >> 1));                                \
    if (!(rdy & bit)) {                                                               \
      if (!pdone) { publish(pend, epoch, lane); pdone = true; }                       \
      _Pragma("unroll 1") for (u32 spins = 0; spins < SPIN_CAP; ++spins) {            \
        rdy = (u32)__builtin_amdgcn_ballot_w64(__hip_atomic_load(fhp, RLX_AGENT) == epoch); \
        if (rdy & bit) break;                                                         \
        __builtin_amdgcn_s_sleep(2);                                                  \
      }                                                                               \
    }                                                                                 \
    ORDER_BEHIND_POLL();                                                              \
  }
    bool pdone = pend == nullptr;
    H_GATE(0)
    v4u hf[4 * NKM];
#define H_LOAD(n) hf[n] = __builtin_amdgcn_raw_buffer_load_b128(rsWS, lane * 16 + (((((n) & 3) + 1 + (wv >> 1)) & 3) << 10), WS_HT + (tg * 56 + KS2((n) >> 2)) * 4096, SC1)
#pragma unroll
    for (int n = 0; n < WIN; ++n) {
      if (n && (n & 3) == 0) H_GATE(n >> 2)
      H_LOAD(n);
      SCHED();
    }
    if (!pdone) {
      asm volatile("s_waitcnt vmcnt(%0)" ::"n"(SWIN) : "memory");
      if (lane == 0) __hip_atomic_store(pend, epoch, RLX_AGENT);
      pdone = true;
    }
    __builtin_amdgcn_s_setprio(0);
    SCHED();
    GAS u32* const fsp = ctl + WS_FS / 4 + ((tg >> 4) * 512 + cg * 64 + wv * 8 + (lane >> 3)) * FL;
    const int tok = tg * 64 + 16 * (wv >> 1) + r16;
    const int c8 = cg * 64 + 32 * (wv & 1) + 8 * q;
    u32 ra = (u32)(LD_RED + wv * 7 * 2048 + lane * 16), sa = (u32)(LD_ST + (32 * (wv & 1) + 8 * q + qq) * 128 + (16 * (wv >> 1) + 4 * p4) * 2);
    u32 ta = (u32)(LD_KREG + (16 * (wv >> 1) + r16) * 144 + (wv & 1) * 64 + q * 16), tb = (u32)(LD_KREG + (8 * wv + (lane >> 3)) * 144 + (lane & 7) * 16);
    u32 oa = (u32)(((tg * 64 + 8 * wv + (lane >> 3)) * 512 + cg * 64 + (lane & 7) * 8) * 2);
    asm volatile("" : "+v"(ra), "+v"(sa), "+v"(ta), "+v"(tb), "+v"(oa));
    u32 fs;
    bool early;
    v4u xv, bv, sv;
    f32x4 acc[4][4];
#pragma unroll
    for (int i = 0; i < 4; ++i)
#pragma unroll
      for (int j = 0; j < 4; ++j) acc[i][j] = f32x4{0.f, 0.f, 0.f, 0.f};
#define RED_WRITE(j)                                                                                                  \
  _Pragma("unroll") for (int cp = 0; cp < 2; ++cp) if ((j) < 3 || cp) {                                               \
    const int d = ((((j) + 1 + (wv >> 1)) & 3) << 1) | ((cp + wv) & 1);                                               \
    _Pragma("unroll") for (int e = 0; e < 2; ++e)                                                                     \
      *(LAS f32x4*)(L + LD_RED + (d * 7 + ((j) < 3 ? 2 * (j) + cp : 6)) * 2048 + e * 1024 + lane * 16) = acc[2 * cp + e][j]; \
  }
#pragma unroll
    for (int n = 0; n < NF; ++n) {
#pragma unroll
      for (int c = 0; c < 4; ++c) acc[c][n & 3] = mfma16(aw[n >> 2][c], hf[n], acc[c][n & 3]);
      SCHED();
      if (n == NF - 6) wait_progress(L, lane & 7, 4u);
      if (n >= NF - 3) RED_WRITE(n - (NF - 3))
      if (n == NF - 10) fs = __hip_atomic_load(fsp, RLX_AGENT);
      if (n + WIN < NF) {
        if (((n + WIN) & 3) == 0) H_GATE((n + WIN) >> 2)
        H_LOAD(n + WIN);
      }
      if (n == NF - 6) {
        xv = *(const GAS v4u*)(x + tok * 512 + c8);
        bv = *(const GAS v4u*)(b2 + c8);
      }
      if (n == NF - 5) {
        early = __all(fs == epoch);
        ORDER_BEHIND_POLL();
        sv = __builtin_amdgcn_raw_buffer_load_b128(rsWS, (lane >> 3) * 4096 + (lane & 7) * 16, early ? WS_ST + (cg * 64 + wv * 8) * 4096 + tg * 128 : WS_END, SC1);
      }
      SCHED();
    }
    RED_WRITE(3)
    if (!early) {
      poll_flags<2>(fsp, epoch, epoch + 1u);
      sv = __builtin_amdgcn_raw_buffer_load_b128(rsWS, (lane >> 3) * 4096 + (lane & 7) * 16, WS_ST + (cg * 64 + wv * 8) * 4096 + tg * 128, SC1);
    }
    *(LAS v4u*)(L + LD_ST + wv * 1024 + lane * 16) = sv;
    f32x2 m0[4], xf[4];
#pragma unroll
    for (int k = 0; k < 4; ++k) {
      m0[k] = f32x2{sadd(acc[k >> 1][3][2 * (k & 1)], bflo(bv[k])), sadd(acc[k >> 1][3][2 * (k & 1) + 1], bfhi(bv[k]))};
      xf[k] = f32x2{bflo(xv[k]), bfhi(xv[k])};
      asm volatile("" : "+v"(m0[k]), "+v"(xf[k]));
    }
    const __amdgpu_buffer_rsrc_t rsO = __builtin_amdgcn_make_buffer_rsrc((void*)out, 0, 2097152, 0x00020000);
    BAR();
    f32x4 part[7][2];
    v2u sq[2];
#pragma unroll
    for (int j = 0; j < 7; ++j)
#pragma unroll
      for (int e = 0; e < 2; ++e) part[j][e] = *(LAS f32x4*)(L + ra + j * 2048 + e * 1024);
#pragma unroll
    for (int e = 0; e < 2; ++e) sq[e] = trread(L + sa + e * 512);
    SCHED();
#pragma unroll
    for (int j = 0; j < 7; ++j)
#pragma unroll
      for (int k = 0; k < 4; ++k) m0[k] += (k & 1) ? f32x2{part[j][k >> 1].z, part[j][k >> 1].w} : f32x2{part[j][k >> 1].x, part[j][k >> 1].y};
    u32 o[4];
#pragma unroll
    for (int k = 0; k < 4; ++k) {
      f32x2 t = m0[k] * -1.4426950408889634f;
      asm("" : "+v"(t));
      f32x2 dn = f32x2{__builtin_amdgcn_exp2f(t.x), __builtin_amdgcn_exp2f(t.y)} + 1.0f;
      asm("" : "+v"(dn));
      const u32 sw = sq[k >> 1][k & 1];
      const f32x2 ms = m0[k] * f32x2{bflo(sw), bfhi(sw)};
      o[k] = pk2v(ms * f32x2{__builtin_amdgcn_rcpf(dn.x), __builtin_amdgcn_rcpf(dn.y)} + xf[k]);
    }
    *(LAS v4u*)(L + ta) = v4u{o[0], o[1], o[2], o[3]};
    BAR();
    __builtin_amdgcn_raw_buffer_store_b128(*(LAS v4u*)(L + tb), rsO, oa, 0, SC1);
  }
  __builtin_amdgcn_endpgm();
  };

  if (helper) {
    LAS char* const HX = L + LD_HX + wv * 8192;
    if (swave) WAITV(0); else WAITV(8);
    const u32 epoch = GET_EPOCH();
    f32x4 gram[2], sumx[2];
#pragma unroll
    for (int i = 0; i < 2; ++i) { gram[i] = f32x4{0.f, 0.f, 0.f, 0.f}; sumx[i] = gram[i]; }
    const int r8 = lane & 7, fr = ((r8 & 3) << 2) | (r8 >> 2);
    v4u xr[8];
#pragma unroll
    for (int s = 0; s < 8; ++s) xr[s] = *(LAS v4u*)(HX + r8 * 1024 + (lane & 8) * 64 + (((4 * s + q) ^ fr) << 4));
    v2u d[8][2];
#pragma unroll
    for (int s = 0; s < 8; ++s)
#pragma unroll
      for (int t = 0; t < 2; ++t)
        d[s][t] = trread(HX + (4 * t + qq) * 1024 + (((8 * s + 2 * q + (p4 >> 1)) ^ ((qq << 2) | t)) << 4) + 8 * (p4 & 1));
    SCHED();
#pragma unroll
    for (int s = 0; s < 8; ++s) {
      gram[s & 1] = mfma16(xr[s], xr[s], gram[s & 1]);
      sumx[s & 1] = mfma16(ones, xr[s], sumx[s & 1]);
    }
    float s1 = sumx[0].x + sumx[1].x, s2 = pick4(gram[0] + gram[1], r16 & 3);
    s1 += __shfl(s1, (lane + 40) & 63);
    s2 += __shfl(s2, (lane + 40) & 63);
    float mean, var, den1;
    ln_stats(s1, s2, mean, var, den1);
    const float rd = __builtin_amdgcn_rcpf(den1);
    const float den2 = bfr(__builtin_amdgcn_sqrtf(bfr(bfr(var * rd * rd) + 1e-5f)));
    const float R = __builtin_amdgcn_rcpf(den1 * den2);
    if (q == qq && r16 < 8) {
      *(LAS f32x2*)(L + LD_HST + (8 * wv + r16) * 8) = f32x2{rd, -bfr(mean) * rd};
      __builtin_amdgcn_raw_buffer_store_b128(v4u{__builtin_bit_cast(u32, R), epoch, __builtin_bit_cast(u32, R * mean), epoch}, rsWS,
                                             WS_STAT + (hid * 64 + 8 * wv + r16) * 16, 0, SC1);
    }
    WAITL();
    f32x4 sv[4];
#pragma unroll
    for (int i = 0; i < 4; ++i) sv[i] = *(LAS f32x4*)(L + LD_HST + wv * 64 + i * 16);
#pragma unroll
    for (int cb = 0; cb < 8; ++cb) {
      v4u o;
      o.x = pk2(bflo(d[cb][0].x) * sv[0].x + sv[0].y, bfhi(d[cb][0].x) * sv[0].z + sv[0].w);
      o.y = pk2(bflo(d[cb][0].y) * sv[1].x + sv[1].y, bfhi(d[cb][0].y) * sv[1].z + sv[1].w);
      o.z = pk2(bflo(d[cb][1].x) * sv[2].x + sv[2].y, bfhi(d[cb][1].x) * sv[2].z + sv[2].w);
      o.w = pk2(bflo(d[cb][1].y) * sv[3].x + sv[3].y, bfhi(d[cb][1].y) * sv[3].z + sv[3].w);
      *(LAS v4u*)(L + LD_HT + (cb * 64 + lane) * 128 + ((wv ^ (lane & 7)) << 4)) = o;
    }
    BAR();
    if (swave) {
      v4u o[16];
#pragma unroll
      for (int i = 0; i < 16; ++i) o[i] = *(LAS v4u*)(L + LD_HT + (w * 128 + i * 8 + (lane >> 3)) * 128 + (((lane ^ (lane >> 3)) & 7) << 4));
      SCHED();
#pragma unroll
      for (int i = 0; i < 16; ++i) __builtin_amdgcn_raw_buffer_store_b128(o[i], rsWS, lane * 16 + WS_XNT + hid * 65536 + (w * 16 + i) * 1024, 0, SC1);
      publish(ctl + WS_FXN / 4 + (hid * 4 + w) * FL, epoch, lane);
    }
    SCHED();
  }

  if (swave) {
    __builtin_amdgcn_s_setprio(2);
    const u32 voX = (u32)((32 * w + (lane >> 3)) * 1024 + (((lane & 7) ^ (lane >> 3)) << 4));
    const u32 voW = (u32)((4 * w + q) * 3584 + ((r16 ^ ((q << 2) | (w & 2))) << 4));
#define G1_STAGE(st) G1_PART(st, 0, 4)
#define G1_PART(st, p0, p1)                                                                                  \
  {                                                                                                          \
    const int sa_ = ((st) + rot) & 7;                                                                        \
    _Pragma("unroll") for (int p = p0; p < p1; ++p) {                                                        \
      dma16(voX, rsX, (u32)(mt * 131072 + p * 8192 + sa_ * 128), (u32)(((st) & 3) * 32768 + w * 4096 + p * 1024)); \
      dma16(voW, rsW1, (u32)((sa_ * 64 + 16 * p) * 3584 + nt * 256), (u32)(((st) & 3) * 32768 + 16384 + p * 4096 + w * 1024)); \
    }                                                                                                        \
  }
    if (lane == 0) *(LAS u32*)(L + LD_PS + wv * 4) = 0u;
    WAITV(8);
    const u32 epoch = GET_EPOCH();
    BAR();
    if (!helper) G1_STAGE(2)
    const int r32 = lane & 31, hh = lane >> 5;
    v4u pf[8], qf[8];
    {
#pragma unroll
      for (int s = 0; s < 8; ++s) {
        qf[s] = *(LAS v4u*)(L + LD_Q + w * 8192 + r32 * 256 + (((2 * s + hh) ^ (r32 & 15)) << 4));
        pf[s] = *(LAS v4u*)(PT + (2 * s + hh) * 464 + r32 * 16);
      }
    }
    SCHED();
    if (!helper) {
      WAITV(8); BAR(); G1_STAGE(3) G1_PART(4, 0, 1)
      WAITV(10); BAR(); G1_PART(4, 1, 4) G1_PART(5, 0, 2)
      WAITV(12); BAR(); G1_PART(5, 2, 4) G1_PART(6, 0, 3)
      WAITV(14); BAR(); G1_PART(6, 3, 4) G1_STAGE(7)
      WAITV(16); BAR();
    }
    GAS u32* const fxnp = ctl + WS_FXN / 4 + ((hw * 16 + r16) * 4 + (ch >> 7)) * FL;
    GAS u32* const fw2p = ctl + WS_FW2 / 4 + (cg * 28 + ((lane & 4) ? HX4(lane) : NKM / 2 * w + (lane & 3))) * 4 * FL;
    const int sto = WS_STAT + ((helper ? 0 : mt) * 128 + 32 * w + r32) * 16;
    v4u sg = __builtin_amdgcn_raw_buffer_load_b128(rsWS, sto, 0, SC1);
    const u32 fw2 = __hip_atomic_load(fw2p, RLX_AGENT);
    const u32 pDs = LD16(Dskip, ch);
    SCHED();
    f32x16 kacc;
#pragma unroll
    for (int i = 0; i < 16; ++i) kacc[i] = 0.f;
#pragma unroll
    for (int s = 0; s < 8; ++s) kacc = mfma32(pf[s], qf[s], kacc);
    SCHED();
    if (!helper) { WAITV(3); BAR(); }
#pragma unroll
    for (int i = 0; i < 4; ++i)
      *(LAS f32x4*)(L + LD_CX + w * 4096 + i * 1024 + lane * 16) = f32x4{kacc[4 * i], kacc[4 * i + 1], kacc[4 * i + 2], kacc[4 * i + 3]};
    if (!helper) {
      if (!__all(sg.y == epoch && sg.w == epoch)) {
#pragma unroll 1
        for (u32 spins = 0; spins < SPIN_CAP; ++spins) {
          __builtin_amdgcn_s_sleep(4);
          asm volatile("" ::: "memory");
          sg = __builtin_amdgcn_raw_buffer_load_b128(rsWS, sto, 0, SC1);
          if (__all(sg.y == epoch && sg.w == epoch)) break;
        }
      }
      *(LAS v2u*)(L + LD_STAT + (32 * w + r32) * 8) = v2u{sg.x, sg.z};
    }
    poll_flags<4>(fw2p, epoch, fw2);
    set_progress(L, wv, 1u);
    v4u aw[NKM][4];
#pragma unroll
    for (int c = 0; c < 4; ++c) AW_LOAD(c >> 2, c & 3);
    SCHED();
    wait_progress(L, wv ^ 1, 1u);
    {
      f32x4 v[4];
#pragma unroll
      for (int i = 0; i < 4; ++i) v[i] = *(LAS f32x4*)(L + LD_CX + (w ^ 1) * 4096 + i * 1024 + lane * 16);
#pragma unroll
      for (int i = 0; i < 15; ++i) {
        kacc[i] = sadd(kacc[i], v[i >> 2][i & 3]);
      }
    }
    {
      const bool odd = r32 & 1, up = hh != 0;
      const int ia[4][9] = {{0, 1, 2, 3, 4, 5, 6, 7, 8}, {9, 9, 9, 9, -1, 10, 10, 10, 10}, {11, 11, 11, 11, -1, 12, 12, 12, 12}, {13, 13, 13, 13, -1, 14, 14, 14, 14}};
      const int ib[4][9] = {{0, 0, 1, 2, 2, 2, 3, 4, 4}, {5, 5, 5, 6, 6, 6, 7, 7, 7}, {8, 8, 8, 9, 9, 9, 10, 10, 10}, {11, 11, 11, 11, -1, 12, 12, 12, 12}};
      LAS char* const KC = PT + hw * 64 + (255 - r32) * 16;
#pragma unroll
      for (int u = 0; u < 4; ++u) {
        SCHED();
        AW_LOAD(1 + (u >> 1), 2 * (u & 1));
        SCHED();
        float e[9];
#pragma unroll
        for (int i = 0; i < 9; ++i) {
          const float va = ia[u][i] >= 0 ? kacc[ia[u][i]] : (odd ? kacc[ia[u][5]] : kacc[ia[u][3]]);
          const float vb = ib[u][i] >= 0 ? kacc[ib[u][i]] : (odd ? kacc[ib[u][5]] : kacc[ib[u][3]]);
          e[i] = up ? vb : va;
        }
        SCHED();
        AW_LOAD(1 + (u >> 1), 2 * (u & 1) + 1);
        SCHED();
        v4u d;
        if (hw) { d.x = pk2(e[8], e[7]); d.y = pk2(e[6], e[5]); d.z = pk2(e[4], e[3]); d.w = pk2(e[2], e[1]); }
        else    { d.x = pk2(e[7], e[6]); d.y = pk2(e[5], e[4]); d.z = pk2(e[3], e[2]); d.w = pk2(e[1], e[0]); }
        const int sa = u == 0 ? 0 : u + 3, sb = u == 3 ? 7 : u + 1;
        *(LAS v4u*)(KC - (up ? sb : sa) * 512) = d;
      }
      if (lane < 2) *(LAS v4u*)(PT + hw * 64 + 4096 + lane * 16) = v4u{(hw && lane == 0) ? pk2(kacc[0], 0.f) : 0u, 0u, 0u, 0u};
    }
    SCHED();
    u32 fxa = __hip_atomic_load(fxnp, RLX_AGENT);
#pragma unroll
    for (int c = 12; c < 4 * NKS - 4; ++c) AW_LOAD(c >> 2, c & 3);
    SCHED();
    asm volatile("" : "+v"(fxa));
    const bool xearly = __all(fxa == epoch);
    ORDER_BEHIND_POLL();
    const int xo = WS_XNT + (16 * hw) * 65536 + ch * 128;
    v4u u0 = __builtin_amdgcn_raw_buffer_load_b128(rsWS, (lane >> 3) * 65536 + (lane & 7) * 16, xearly ? xo : WS_END, SC1);
    v4u u1 = __builtin_amdgcn_raw_buffer_load_b128(rsWS, (lane >> 3) * 65536 + (lane & 7) * 16, xearly ? xo + 8 * 65536 : WS_END, SC1);
#pragma unroll
    for (int c = 4 * NKS - 4; c < 4 * NKS; ++c) AW_LOAD(c >> 2, c & 3);
    SCHED();
    if (!xearly) {
      poll_flags<8>(fxnp, epoch, epoch + 1u);
      u0 = __builtin_amdgcn_raw_buffer_load_b128(rsWS, (lane >> 3) * 65536 + (lane & 7) * 16, xo, SC1);
      u1 = __builtin_amdgcn_raw_buffer_load_b128(rsWS, (lane >> 3) * 65536 + (lane & 7) * 16, xo + 8 * 65536, SC1);
      __builtin_amdgcn_s_waitcnt(0x0F70);
    }
    LAS char* const UB = L + LD_U + cw * 4608;
    if (hw == 0 && lane < 32) *(LAS v4u*)(UB + lane * 16) = v4u{0u, 0u, 0u, 0u};
    *(LAS v4u*)(UB + 512 + (128 * hw + lane) * 16) = u0;
    *(LAS v4u*)(UB + 512 + (128 * hw + 64 + lane) * 16) = u1;
    set_progress(L, wv, 2u);
    SCHED();
    const float dskip = bf2f(pDs);
    const int sgm = 1 - (r16 & 1);
    LAS char* const ka = L + LD_KREG + (2 * cw + sgm) * 8192 + sgm * 64 + 2 * (2031 - r16 + 8 * q + sgm) - hw * 256 - 7 * 512 - 3 * 64;
    LAS char* const ub = UB + 2 * (256 - 16 + 16 * r16 + 8 * q) - hw * 256 - 3 * 64;
    u32 kb[4];
#pragma unroll
    for (int i = 0; i < 4; ++i) { kb[i] = (u32)(size_t)ka + i * 1024; asm volatile("" : "+v"(kb[i])); }
    f32x4 y[8];
    {
      f32x4 y0[4];
#pragma unroll
      for (int i = 0; i < 4; ++i) {
        const v2u xv = *(LAS v2u*)(UB + 512 + (256 * (4 * hw + i) + 16 * r16 + 4 * q) * 2);
        y0[i] = f32x4{smul(bflo(xv.x), dskip), smul(bfhi(xv.x), dskip), smul(bflo(xv.y), dskip), smul(bfhi(xv.y), dskip)};
      }
      const f32x4 zz = {0.f, 0.f, 0.f, 0.f};
      if (hw) {
#pragma unroll
        for (int i = 0; i < 4; ++i) { y[i] = zz; y[4 + i] = y0[i]; }
      } else {
#pragma unroll
        for (int i = 0; i < 4; ++i) { y[i] = y0[i]; y[4 + i] = zz; }
      }
#pragma unroll
      for (int i = 0; i < 8; ++i) asm volatile("" : "+v"(y[i]));
    }
    wait_progress(L, wv ^ 1, 2u);
    v4u af[4][8], bfv[4][8];
#define CV_READ(rr, g)                                                                                   \
  {                                                                                                      \
    LAS u32* pa = (LAS u32*)(size_t)(kb[(7 - (g)) >> 1] + ((7 - (g)) & 1) * 512 + (3 - (rr)) * 64);      \
    af[rr][g] = v4u{pa[0], pa[1], pa[2], pa[3]};                                                         \
    bfv[rr][g] = *(LAS v4u*)(ub + (g) * 512 + (3 - (rr)) * 64);                                          \
  }
#define CV_LEVEL(rr, k, T0, T1)                                                              \
  _Pragma("unroll") for (int o = 0; o <= (k); ++o)                                           \
    if ((k) + o >= (T0) && (k) + o < (T1)) {                                                 \
      y[(k) + o] = mfma16(af[rr][k], bfv[rr][o], y[(k) + o]);                                \
      if (o < (k)) y[(k) + o] = mfma16(af[rr][o], bfv[rr][k], y[(k) + o]);                   \
    }
#define CV_MIX(n)                                                                            \
  _Pragma("unroll") for (int t = 0; t < (n); ++t) {                                          \
    __builtin_amdgcn_sched_group_barrier(0x008, 1, 0);                                       \
    __builtin_amdgcn_sched_group_barrier(0x100, 1, 0);                                       \
  }                                                                                          \
  SCHED();
#pragma unroll
    for (int g = 0; g < 4; ++g) CV_READ(0, g)
    SCHED();
    wait_progress(L, w, 4u);
    __builtin_amdgcn_s_setprio(2);
#pragma unroll
    for (int rr = 0; rr < 3; ++rr) {
      CV_READ(rr, 4) CV_READ(rr, 5) CV_READ(rr, 6) CV_READ(rr, 7)
      CV_LEVEL(rr, 0, 0, 8) CV_LEVEL(rr, 1, 0, 8) CV_LEVEL(rr, 2, 0, 8) CV_LEVEL(rr, 3, 0, 8)
      CV_MIX(12)
      CV_READ(rr + 1, 0) CV_READ(rr + 1, 1) CV_READ(rr + 1, 2) CV_READ(rr + 1, 3)
      CV_LEVEL(rr, 4, 0, 8) CV_LEVEL(rr, 5, 0, 8) CV_LEVEL(rr, 6, 0, 8) CV_LEVEL(rr, 7, 0, 8)
      CV_MIX(12)
    }
    f32x4 mine[4];
    CV_READ(3, 4) CV_READ(3, 5) CV_READ(3, 6) CV_READ(3, 7)
    SCHED();
    if (hw) {
      CV_LEVEL(3, 0, 0, 4) CV_LEVEL(3, 1, 0, 4) CV_LEVEL(3, 2, 0, 4) CV_LEVEL(3, 3, 0, 4)
      CV_LEVEL(3, 2, 4, 8) CV_LEVEL(3, 3, 4, 8)
      SCHED();
#pragma unroll
      for (int i = 0; i < 4; ++i) *(LAS f32x4*)(L + LD_CX + w * 4096 + i * 1024 + lane * 16) = y[i];
      *(volatile LAS u32*)(L + LD_PS + wv * 4) = 3u;
      SCHED();
      CV_LEVEL(3, 4, 4, 8) CV_LEVEL(3, 5, 4, 8) CV_LEVEL(3, 6, 4, 8) CV_LEVEL(3, 7, 4, 8)
#pragma unroll
      for (int i = 0; i < 4; ++i) mine[i] = y[4 + i];
    } else {
      CV_LEVEL(3, 2, 4, 8) CV_LEVEL(3, 3, 4, 8) CV_LEVEL(3, 4, 4, 8) CV_LEVEL(3, 5, 4, 8) CV_LEVEL(3, 6, 4, 8) CV_LEVEL(3, 7, 4, 8)
      CV_LEVEL(3, 0, 0, 4) CV_LEVEL(3, 1, 0, 4)
      SCHED();
#pragma unroll
      for (int i = 0; i < 4; ++i) *(LAS f32x4*)(L + LD_CX + w * 4096 + i * 1024 + lane * 16) = y[4 + i];
      *(volatile LAS u32*)(L + LD_PS + wv * 4) = 3u;
      SCHED();
      CV_LEVEL(3, 2, 0, 4) CV_LEVEL(3, 3, 0, 4)
#pragma unroll
      for (int i = 0; i < 4; ++i) mine[i] = y[i];
    }
    SCHED();
    const u32 fh0 = __hip_atomic_load(ctl + WS_FH / 4 + (tg * 32 + HX4(lane)) * FL, RLX_AGENT);
    SCHED();
    wait_progress(L, wv ^ 1, 3u);
    f32x4 oy[4];
#pragma unroll
    for (int i = 0; i < 4; ++i) oy[i] = *(LAS f32x4*)(L + LD_CX + (w ^ 1) * 4096 + i * 1024 + lane * 16);
    LAS char* const SB = L + LD_SB + w * 2048;
#pragma unroll
    for (int i = 0; i < 4; ++i) {
      v2u o;
      o.x = pk2(sadd(mine[i].x, oy[i].x), sadd(mine[i].y, oy[i].y));
      o.y = pk2(sadd(mine[i].z, oy[i].z), sadd(mine[i].w, oy[i].w));
      *(LAS v2u*)(SB + i * 512 + r16 * 32 + q * 8) = o;
    }
    const v4u s0 = *(LAS v4u*)(SB + lane * 16), s1 = *(LAS v4u*)(SB + 1024 + lane * 16);
    set_progress(L, wv, 4u);
    __builtin_amdgcn_raw_buffer_store_b128(s0, rsWS, lane * 16 + WS_ST + ch * 4096 + hw * 2048, 0, SC1);
    __builtin_amdgcn_raw_buffer_store_b128(s1, rsWS, lane * 16 + WS_ST + ch * 4096 + hw * 2048 + 1024, 0, SC1);
    gemm2_tail(aw, epoch, ctl + WS_FS / 4 + (hw * 512 + ch) * FL, fh0, SWIN, 4 * NKS);
  } else {
    const int upp = hw;
    if (lane == 0) *(LAS u32*)(L + LD_PS + wv * 4) = 0u;
    SCHED();
    const int mi = w >> 1, ni = w & 1;
    f32x4 acc[4][4], csum[2];
#pragma unroll
    for (int i = 0; i < 4; ++i) {
#pragma unroll
      for (int j = 0; j < 4; ++j) { acc[i][j] = f32x4{0.f, 0.f, 0.f, 0.f}; asm volatile("" : "+v"(acc[i][j])); }
      if (i < 2) { csum[i] = f32x4{0.f, 0.f, 0.f, 0.f}; asm volatile("" : "+v"(csum[i])); }
    }
    u32 xb[2][2], wb[2][4];
#pragma unroll
    for (int h = 0; h < 2; ++h) {
#pragma unroll
      for (int k = 0; k < 2; ++k) {
        xb[h][k] = (u32)(((64 * mi + r16) * 128 + ((q ^ (r16 & 7)) << 4)) ^ (k * 64)) + h * 65536;
        asm volatile("" : "+v"(xb[h][k]));
      }
#pragma unroll
      for (int j = 0; j < 4; ++j) {
        wb[h][j] = (u32)(16384 + (8 * q + qq) * 256 + (((2 * (4 * ni + ((j + 2 * mi) & 3)) + (p4 >> 1)) ^ ((qq << 2) | ((q & 1) << 1))) << 4) + 8 * (p4 & 1)) + h * 65536;
        asm volatile("" : "+v"(wb[h][j]));
      }
    }
    v4u onev = ones;
    asm volatile("" : "+v"(onev));
    u32 qad[16];
#pragma unroll
    for (int r = 0; r < 16; ++r) { qad[r] = (u32)(LD_Q + w * 8192) + (wo ^ (u32)(r << 4)); asm volatile("" : "+v"(qad[r])); }
    SCHED();
    SCHED();
    if (helper) WAITV(0);
    WAITV(2);
    const u32 epoch = GET_EPOCH();
    LAS u16* const par = (LAS u16*)(PT + lane * 4);
    const u32 pLd = par[0], pAr = par[128], pAi = par[256], pBr = par[384], pBi = par[512], pCr = par[640], pCi = par[768], pB1 = par[896];
    asm volatile("" ::: "memory");
    {
      const float dt = bfr(__builtin_amdgcn_exp2f(bf2f(pLd) * 1.4426950408889634f));
      const float la = dt * bf2f(pAr) * 1.4426950408889634f;
      const float th = dt * bf2f(pAi) * 0.15915494309189535f;
      const f32x2 dBc = {dt * bf2f(pBr), -dt * bf2f(pBi)};
      const f32x2 Ccc = {bf2f(pCr), -bf2f(pCi)};
      const f32x2 z8 = zpow(la, th, 8.f), z16 = zpow(la, th, 16.f);
      const f32x2 y1 = zpow(la, -th, 1.f), y256 = zpow(la, -th, 256.f), y8 = {z8.x, -z8.y};
      const f32x2 y2 = cmul(y1, y1), y4 = cmul(y2, y2);
#define PROW(h, g) (((g) & 3) + 8 * ((g) >> 2) + 4 * (h))
#define PW(h, g, v) *(LAS u32*)(PT + (lane >> 2) * 464 + (lane & 3) * 4 + PROW(h, g) * 16) = pk2v(v)
      f32x2 W[8];
      W[0] = cmul(Ccc, dBc);
#pragma unroll
      for (int c = 1; c < 8; ++c) W[c] = cmul(W[c - 1], y256);
      {
        f32x2 te = W[0], to = cmul(W[0], y1), t1 = W[1];
        PW(0, 0, te); PW(0, 1, to); PW(1, 0, t1);
#pragma unroll
        for (int a = 1; a <= 4; ++a) {
          te = cmul(te, y2); PW(0, 2 * a, te);
          if (a < 4) { to = cmul(to, y2); PW(0, 2 * a + 1, to); }
          t1 = cmul(t1, y2); PW(1, a, t1);
        }
      }
      PW(1, 5, W[2]); PW(1, 6, cmul(W[2], y4)); PW(1, 7, cmul(W[2], y8));
      PW(1, 8, W[3]); PW(1, 9, cmul(W[3], y4)); PW(1, 10, cmul(W[3], y8));
      PW(0, 9, W[4]); PW(0, 10, cmul(W[4], y8));
      PW(0, 11, W[5]); PW(0, 12, cmul(W[5], y8));
      PW(0, 13, W[6]); PW(0, 14, cmul(W[6], y8));
      PW(1, 11, W[7]); PW(1, 12, cmul(W[7], y8));
      f32x2 qa = {1.f, 0.f}, qb = z8;
      u32 qv[32];
#pragma unroll
      for (int i = 0; i < 16; ++i) {
        qv[2 * i] = pk2v(qa);
        qv[2 * i + 1] = pk2v(qb);
        if (i < 15) { qa = (i == 0) ? z16 : cmul(qa, z16); qb = cmul(qb, z16); }
      }
#pragma unroll
      for (int r = 0; r < 16; ++r) {
        *(LAS u32*)(size_t)(qad[r] + r * 256) = qv[r];
        *(LAS u32*)(size_t)(qad[r] + (r + 16) * 256) = qv[r + 16];
      }
    }
    SCHED();
    WAITV(0);
    v2u wt[2][2];
#pragma unroll
    for (int e = 0; e < 2; ++e)
#pragma unroll
      for (int t = 0; t < 2; ++t) wt[e][t] = trread(L + LD_W2S + w * 2048 + (16 * t + 4 * q + qq) * 64 + 16 * p4 + 8 * e);
#pragma unroll
    for (int e = 0; e < 2; ++e)
      __builtin_amdgcn_raw_buffer_store_b128(v4u{wt[e][0].x, wt[e][0].y, wt[e][1].x, wt[e][1].y}, rsWS,
                                             lane * 16 + (helper ? WS_END : WS_W2T) + ((xcd * 56 + uks) * 4 + 2 * upp + e) * 1024, 0, SC1);
    *(LAS float*)(L + LD_B1 + (tid & 127) * 4) = bf2f(pB1);
    v4u aw[NKM][4];
    u32 fh0 = epoch + 1u;
    if (!helper) {
      v4u xf[2][4], wf[2][4];
#define G1_READ(n)                                                                                            \
  _Pragma("unroll") for (int i = 0; i < 4; ++i) {                                                             \
    const v2u lo = trread(L + (((n) >> 1) & 1) * 32768 + ((n) & 1) * 8192 + wb[((n) >> 2) & 1][i]);           \
    const v2u hi = trread(L + (((n) >> 1) & 1) * 32768 + ((n) & 1) * 8192 + 1024 + wb[((n) >> 2) & 1][i]);    \
    wf[(n) & 1][i] = v4u{lo.x, lo.y, hi.x, hi.y};                                                             \
    xf[(n) & 1][i] = *(LAS v4u*)(L + (((n) >> 1) & 1) * 32768 + i * 2048 + xb[((n) >> 2) & 1][(n) & 1]);      \
  }
      __builtin_amdgcn_s_setprio(0);
      BAR();
      G1_READ(0)
      SCHED();
      u32 fm = 0;
      bool mearly = false;
#pragma unroll
      for (int c = 0; c < 4 * MEARLY; ++c) asm volatile("" : "=v"(aw[c >> 2][c & 3]));
#pragma unroll 1
      for (int it = 0; it < 2; ++it) {
#pragma unroll
        for (int n = 0; n < 8; ++n) {
          if (n & 1) {
            if (n == 3 && it == 0) WAITV(0);
            if (n < 4 || it == 0) BAR();
            if (n == 3 && it == 0 && w == 0) {
              __builtin_amdgcn_s_setprio(3);
              if (lane == 0) __hip_atomic_store(ctl + WS_FW2 / 4 + (xcd * 28 + idx) * 4 * FL, epoch, RLX_AGENT);
              __builtin_amdgcn_s_setprio(0);
            }
            if (n == 7 && it == 0) {
              __builtin_amdgcn_s_setprio(3);
              fm = __hip_atomic_load(ctl + WS_FW2 / 4 + (cg * 28 + HX4(lane)) * 4 * FL, RLX_AGENT);
              __builtin_amdgcn_s_setprio(0);
            }
            if (n == 1 && it == 1) { mearly = __all(fm == epoch); ORDER_BEHIND_POLL(); }
            SCHED();
          }
          if (n >= 1 && n < 1 + MEARLY && mearly) {
#pragma unroll
            for (int c = 0; c < 4; ++c) AW_LOAD(n - 1, c);
            SCHED();
          }
          G1_READ(n + 1)
#pragma unroll
          for (int j = 0; j < 4; ++j) {
#pragma unroll
            for (int i = 0; i < 4; ++i) acc[j][i] = mfma16(wf[n & 1][j], xf[n & 1][i], acc[j][i]);
            if (j < 2) csum[j] = mfma16(wf[n & 1][j], onev, csum[j]);
          }
#pragma unroll
          for (int k = 0; k < 12; ++k) {
            __builtin_amdgcn_sched_group_barrier(0x008, 1, 0);
            __builtin_amdgcn_sched_group_barrier(0x100, 1, 0);
          }
          __builtin_amdgcn_sched_group_barrier(0x008, 6, 0);
          SCHED();
        }
      }
      __builtin_amdgcn_s_setprio(3);
      int ty = threadIdx.x;
      asm volatile("" : "+v"(ty));
      const int lane = ty & 63, r16 = lane & 15, q = lane >> 4;
      if (r16 == 0) {
#pragma unroll
        for (int u = 0; u < 2; ++u) *(LAS f32x4*)(L + LD_CS + (64 * ni + 32 * mi + 16 * u + 4 * q) * 4) = csum[u];
      }
      set_progress(L, wv, 1u);
      wait_progress(L, lane & 7, 1u);
      float rs[4], rm[4];
      f32x4 bb[4], cs[4];
#pragma unroll
      for (int i = 0; i < 4; ++i) {
        const f32x2 st = *(LAS f32x2*)(L + LD_STAT + (64 * mi + 16 * i + r16) * 8);
        rs[i] = st.x; rm[i] = -st.y;
        asm volatile("" : "+v"(rm[i]));
        bb[i] = *(LAS f32x4*)(L + LD_B1 + (64 * ni + 16 * ((i + 2 * mi) & 3) + 4 * q) * 4);
        cs[i] = i < 2 ? csum[i] : *(LAS f32x4*)(L + LD_CS + (64 * ni + 16 * ((i + 2 * mi) & 3) + 4 * q) * 4);
      }
      SCHED();
#pragma unroll
      for (int jp = 0; jp < 2; ++jp) {
#pragma unroll
        for (int i = 0; i < 4; ++i) {
          f32x2 pre[4], ee[4];
#pragma unroll
          for (int k = 0; k < 4; ++k) {
            const f32x4 a4 = acc[2 * jp + (k >> 1)][i], c4 = cs[2 * jp + (k >> 1)], b4 = bb[2 * jp + (k >> 1)];
            const f32x2 a2 = (k & 1) ? f32x2{a4.z, a4.w} : f32x2{a4.x, a4.y};
            const f32x2 c2 = (k & 1) ? f32x2{c4.z, c4.w} : f32x2{c4.x, c4.y};
            const f32x2 b2v = (k & 1) ? f32x2{b4.z, b4.w} : f32x2{b4.x, b4.y};
            pre[k] = a2 * rs[i] + (c2 * rm[i] + b2v);
            f32x2 arg = pre[k] * (pre[k] * pre[k] * -0.1029432397f + -2.302208198f);
            asm("" : "+v"(arg));
            ee[k] = f32x2{__builtin_amdgcn_exp2f(arg.x), __builtin_amdgcn_exp2f(arg.y)};
          }
          u32 o[4];
#pragma unroll
          for (int k = 0; k < 4; ++k) {
            f32x2 dn = ee[k] + 1.0f;
            asm("" : "+v"(dn));
            o[k] = pk2v(pre[k] * f32x2{__builtin_amdgcn_rcpf(dn.x), __builtin_amdgcn_rcpf(dn.y)});
          }
          __builtin_amdgcn_raw_buffer_store_b128(v4u{o[0], o[1], o[2], o[3]}, rsWS,
                                                 lane * 16 + WS_HT + (((2 * mt + mi) * 56 + nt * 4 + 2 * ni + (jp ^ mi)) * 4 + i) * 1024, 0, SC1);
          SCHED();
        }
      }
      set_progress(L, wv, 4u);
      __builtin_amdgcn_s_setprio(0);
#pragma unroll
      for (int c = 4 * MEARLY; c < 4 * MEARLY + 8; ++c) AW_LOAD(c >> 2, c & 3);
      SCHED();
      __builtin_amdgcn_s_setprio(3);
      WAITV(8);
      if (lane == 0) __hip_atomic_store(ctl + WS_FH / 4 + ((2 * mt + mi) * 32 + 2 * nt + ni) * FL, epoch, RLX_AGENT);
      __builtin_amdgcn_s_setprio(0);
      SCHED();
      if (!mearly) {
#pragma unroll
        for (int c = 0; c < 4 * MEARLY; ++c) AW_LOAD(c >> 2, c & 3);
      }
#pragma unroll
      for (int c = 4 * MEARLY + 8; c < 4 * NKM - 4; ++c) AW_LOAD(c >> 2, c & 3);
      fh0 = __hip_atomic_load(ctl + WS_FH / 4 + (tg * 32 + HX4(lane)) * FL, RLX_AGENT);
#pragma unroll
      for (int c = 4 * NKM - 4; c < 4 * NKM; ++c) AW_LOAD(c >> 2, c & 3);
      SCHED();
    } else {
      __builtin_amdgcn_s_setprio(0);
      BAR();
      set_progress(L, wv, 4u);
      if (hid == 0 && w == 0) {
#pragma unroll 1
        for (int k = 0; k < 4; ++k) {
          const int g = 64 * k + lane;
          poll_flags<16>(g < 224 ? ctl + WS_FW2 / 4 + g * 4 * FL : ctl + WS_FXN / 4 + (g - 224) * 4 * FL, epoch, epoch + 1u);
        }
        if (lane == 0) __hip_atomic_store(ctl + WS_E / 4, epoch, RLX_AGENT);
      }
      wait_progress(L, 4 + (lane & 3), 2u);
#pragma unroll
      for (int s = 0; s < NKM; ++s)
#pragma unroll
        for (int c = 0; c < 4; ++c) AW_LOAD(s, c);
      SCHED();
    }
    gemm2_tail(aw, epoch, nullptr, fh0, MWIN, 4 * NKM);
  }
}

extern "C" void kernel_launch(void* const* d_in, const int* in_sizes, int n_in, void* d_out, int out_size, void* d_ws,
                              size_t ws_size, hipStream_t stream) {
  (void)in_sizes; (void)n_in; (void)out_size; (void)ws_size;
  (void)hipFuncSetAttribute((const void*)s4_mixer_kernel, hipFuncAttributeMaxDynamicSharedMemorySize, LDS_BYTES);
  s4_mixer_kernel<<<256, 512, LDS_BYTES, stream>>>(
      (const u16*)d_in[0], 2097152u, 0x00020000u, (const u16*)d_in[10], 1835008u, 0x00020000u, (const u16*)d_in[12], (char*)d_ws, (const u16*)d_in[1], (const u16*)d_in[2],
      (const u16*)d_in[3], (const u16*)d_in[4], (const u16*)d_in[5], (const u16*)d_in[6], (const u16*)d_in[7],
      (const u16*)d_in[8], (const u16*)d_in[11], (const u16*)d_in[13], (u16*)d_out);
}
```
